```python
import torch

def _softmax(x, axis=-1):
    return torch.softmax(x, dim=axis)
def _gelu(x):
    return torch.nn.functional.gelu(x, approximate="tanh")
def _silu(x):
    return torch.nn.functional.silu(x)
def _sigmoid(x):
    return torch.sigmoid(x)
def _layernorm(x, axis=-1, eps=1e-5):
    m = x.mean(dim=axis, keepdim=True)
    v = x.var(dim=axis, keepdim=True, unbiased=False)
    return (x - m) / torch.sqrt(v + eps)
def _scatter_add(tgt, idx, src, axis=0):
    return tgt.clone().index_add_(axis, idx, src)
def _segment_sum(src, seg, n):
    out = torch.zeros((n,) + src.shape[1:], dtype=src.dtype)
    return out.index_add_(0, seg, src)
def _pad(x, pw):
    flat = tuple(v for pair in reversed(pw) for v in pair)
    return torch.nn.functional.pad(x, flat)

kernel_name = 'parallel_sum__mha_block__glu_gated_geglu__prompt'

def setup_inputs():

    _w = (lambda a: (lambda t: t.to(torch.bfloat16) if t.is_floating_point() else t)(torch.as_tensor(a)))
    def _A_setup():
        B, S, D, H = 1, 2048, 448, 16
        return {
            "x": _w((lambda *s: torch.randn(*s, dtype=torch.bfloat16) * 0.05)(B, S, D)),
            "wqkv": _w((lambda *s: torch.randn(*s, dtype=torch.bfloat16) * 0.05)(D, 3 * D)),
            "wo": _w((lambda *s: torch.randn(*s, dtype=torch.bfloat16) * 0.05)(D, D)),
        }
    _A_in = _A_setup()

    def _B_setup():
        B, S, Dm, Df = 1, 2048, 448, 1792
        return {
            "x": _w((lambda *s: torch.randn(*s, dtype=torch.bfloat16) * 0.05)(B, S, Dm)),
            "w_gate": _w((lambda *s: torch.randn(*s, dtype=torch.bfloat16) * 0.4)(Dm, Df) * (Dm ** -0.5)),
            "w_up": _w((lambda *s: torch.randn(*s, dtype=torch.bfloat16) * 0.4)(Dm, Df) * (Dm ** -0.5)),
            "w_down": _w((lambda *s: torch.randn(*s, dtype=torch.bfloat16) * 0.4)(Df, Dm) * (Df ** -0.5)),
        }
    _B_in = _B_setup()

    return {
        "A_x": _A_in["x"],
        "A_wqkv": _A_in["wqkv"],
        "A_wo": _A_in["wo"],
        "B_x": _B_in["x"],
        "B_w_gate": _B_in["w_gate"],
        "B_w_up": _B_in["w_up"],
        "B_w_down": _B_in["w_down"],
    }

def reference(A_x, A_wqkv, A_wo, B_x, B_w_gate, B_w_up, B_w_down):
    def _A_ref(x, wqkv, wo):
        B, S, D = x.shape
        H = 16
        Dh = D // H
        qkv = (lambda x, *d: x.permute(*d))((x @ wqkv).reshape(B, S, 3, H, Dh), 2, 0, 3, 1, 4)
        q, k, v = qkv[0], qkv[1], qkv[2]
        scores = torch.einsum("bhqd,bhkd->bhqk", q, k) * (Dh ** -0.5)
        p = _softmax(scores, axis=-1)
        o = torch.einsum("bhqk,bhkd->bhqd", p, v)
        return (lambda x, *d: x.permute(*d))(o, 0, 2, 1, 3).reshape(B, S, D) @ wo

    def _B_ref(x, w_gate, w_up, w_down):
        g = torch.einsum("bsd,df->bsf", x, w_gate)
        u = torch.einsum("bsd,df->bsf", x, w_up)
        h = _gelu(g) * u
        return torch.einsum("bsf,fd->bsd", h, w_down)

    x = A_x
    xn = _layernorm(x, axis=-1)
    return x + _A_ref(xn, A_wqkv, A_wo) + _B_ref(xn, B_w_gate, B_w_up, B_w_down)
```

```cpp
#include <hip/hip_runtime.h>
#include <hip/hip_bf16.h>
typedef unsigned short u16;
__device__ __forceinline__ float b2f(u16 v) { return __uint_as_float((unsigned)v << 16); }
__device__ __forceinline__ u16 f2b(float f) { unsigned u = __float_as_uint(f); u += 0x7FFF + ((u >> 16) & 1); return (u16)(u >> 16); }
constexpr int S = 2048, D = 448, H = 16, DH = 28, F = 1792;
__global__ void k_ln(const u16* x, float* xn) {
    int row = blockIdx.x; int l = threadIdx.x;
    float v[7]; float s = 0;
    for (int i = 0; i < 7; ++i) { v[i] = b2f(x[row * D + i * 64 + l]); s += v[i]; }
    for (int o = 32; o; o >>= 1) s += __shfl_xor(s, o);
    float m = s / D; float q = 0;
    for (int i = 0; i < 7; ++i) q += (v[i] - m) * (v[i] - m);
    for (int o = 32; o; o >>= 1) q += __shfl_xor(q, o);
    float r = rsqrtf(q / D + 1e-5f);
    for (int i = 0; i < 7; ++i) xn[row * D + i * 64 + l] = (v[i] - m) * r;
}
__global__ void k_gemm(const float* A, const u16* W, float* C, int N, int K) {
    int n = blockIdx.x * 64 + threadIdx.x; int m = blockIdx.y;
    float acc = 0;
    for (int k = 0; k < K; ++k) acc += A[m * K + k] * b2f(W[k * N + n]);
    C[m * N + n] = acc;
}
__global__ void k_attn(const float* qkv, float* o) {
    int h = blockIdx.y; int q = blockIdx.x * 64 + threadIdx.x;
    float qv[DH], acc[DH];
    for (int d = 0; d < DH; ++d) { qv[d] = qkv[q * 3 * D + h * DH + d] * 0.1889822365f; acc[d] = 0; }
    float m = -1e30f, l = 0;
    for (int k = 0; k < S; ++k) {
        float s = 0;
        for (int d = 0; d < DH; ++d) s += qv[d] * qkv[k * 3 * D + D + h * DH + d];
        float mn = fmaxf(m, s); float a = __expf(m - mn), p = __expf(s - mn);
        l = l * a + p;
        for (int d = 0; d < DH; ++d) acc[d] = acc[d] * a + p * qkv[k * 3 * D + 2 * D + h * DH + d];
        m = mn;
    }
    for (int d = 0; d < DH; ++d) o[q * D + h * DH + d] = acc[d] / l;
}
__global__ void k_gelu(const float* g, const float* u, float* h) {
    int i = blockIdx.x * 256 + threadIdx.x; float x = g[i];
    float t = tanhf(0.7978845608f * (x + 0.044715f * x * x * x));
    h[i] = 0.5f * x * (1.f + t) * u[i];
}
__global__ void k_fin(const u16* x, const float* a, const float* b, u16* out) {
    int i = blockIdx.x * 256 + threadIdx.x; out[i] = f2b(b2f(x[i]) + a[i] + b[i]);
}
extern "C" void kernel_launch(void* const* d_in, const int* in_sizes, int n_in, void* d_out, int out_size, void* d_ws, size_t ws_size, hipStream_t stream) {
    const u16* x = (const u16*)d_in[0]; const u16* wqkv = (const u16*)d_in[1]; const u16* wo = (const u16*)d_in[2];
    const u16* wg = (const u16*)d_in[4]; const u16* wu = (const u16*)d_in[5]; const u16* wd = (const u16*)d_in[6];
    float* ws = (float*)d_ws;
    float* xn = ws; float* qkv = xn + S * D; float* o = qkv + S * 3 * D; float* g = o + S * D; float* u = g + S * F; float* h = u + S * F; float* a = h + S * F; float* b = a + S * D;
    k_ln<<<S, 64, 0, stream>>>(x, xn);
    k_gemm<<<dim3(3 * D / 64, S), 64, 0, stream>>>(xn, wqkv, qkv, 3 * D, D);
    k_gemm<<<dim3(F / 64, S), 64, 0, stream>>>(xn, wg, g, F, D);
    k_gemm<<<dim3(F / 64, S), 64, 0, stream>>>(xn, wu, u, F, D);
    k_attn<<<dim3(S / 64, H), 64, 0, stream>>>(qkv, o);
    k_gelu<<<S * F / 256, 256, 0, stream>>>(g, u, h);
    k_gemm<<<dim3(D / 64, S), 64, 0, stream>>>(o, wo, a, D, D);
    k_gemm<<<dim3(D / 64, S), 64, 0, stream>>>(h, wd, b, D, F);
    k_fin<<<S * D / 256, 256, 0, stream>>>(x, a, b, (u16*)d_out);
}
```

```cpp
#include <hip/hip_runtime.h>
#include <stdint.h>

typedef short bf16x8 __attribute__((ext_vector_type(8)));
typedef short s16x4 __attribute__((ext_vector_type(4)));
typedef __bf16 bf16x2_t __attribute__((ext_vector_type(2)));
typedef float f32x16 __attribute__((ext_vector_type(16)));
typedef float f32x4 __attribute__((ext_vector_type(4)));
typedef float f32x2 __attribute__((ext_vector_type(2)));
typedef unsigned u32x4 __attribute__((ext_vector_type(4)));
typedef unsigned u32x2 __attribute__((ext_vector_type(2)));
typedef __attribute__((address_space(3))) char* lds_ptr;
typedef __attribute__((address_space(1))) char* glb_ptr;

#define DEV __device__ __forceinline__
#define MFMA(a, b, c) __builtin_amdgcn_mfma_f32_32x32x16_bf16(a, b, c, 0, 0, 0)
#define SBAR() __builtin_amdgcn_sched_barrier(0)
#define PIN(x) asm volatile("" : "+v"(x))

constexpr int S = 2048, D = 448, F = 1792;
constexpr size_t WS_QK = 0;
constexpr size_t WS_V = WS_QK + (size_t)64 * 112 * 512;
constexpr size_t WS_H = WS_V + (size_t)16 * 64 * 2048;
constexpr size_t WS_O = WS_H + (size_t)64 * 112 * 1024;
constexpr size_t WS_DP = WS_O + (size_t)64 * 56 * 512;
constexpr float QSCALE = 0.18898223650461362f * 1.4426950408889634f;

DEV unsigned cvtpk(float lo, float hi) { unsigned r; asm("v_cvt_pk_bf16_f32 %0, %1, %2" : "=v"(r) : "v"(lo), "v"(hi)); return r; }
DEV float rbf(float x) { return __uint_as_float(cvtpk(x, x) & 0xffff0000u); }
DEV void glds16(const void* sbase, unsigned voff, unsigned lds_dst) {
    unsigned sv;
    asm volatile("s_mov_b32 %0, m0\n\ts_mov_b32 m0, %3\n\ts_nop 0\n\tglobal_load_lds_dwordx4 %1, %2\n\ts_mov_b32 m0, %0"
                 : "=&s"(sv) : "v"(voff), "s"(sbase), "s"(lds_dst) : "memory");
}
DEV s16x4 trd(lds_ptr p) { return __builtin_amdgcn_ds_read_tr16_b64_v4i16((__attribute__((address_space(3))) s16x4*)p); }
DEV bf16x8 wfrag(lds_ptr p) {
    s16x4 a = trd(p), b = trd(p + 512);
    return __builtin_shufflevector(a, b, 0, 1, 2, 3, 4, 5, 6, 7);
}
DEV float dot2(unsigned a, unsigned b, float c) { return __builtin_amdgcn_fdot2_f32_bf16(__builtin_bit_cast(bf16x2_t, a), __builtin_bit_cast(bf16x2_t, b), c, false); }
DEV float fsum8(bf16x8 v, float c) { u32x4 u = __builtin_bit_cast(u32x4, v); c = dot2(u[0], 0x3f803f80u, c); c = dot2(u[1], 0x3f803f80u, c); c = dot2(u[2], 0x3f803f80u, c); return dot2(u[3], 0x3f803f80u, c); }
DEV float fsq8(bf16x8 v, float c) { u32x4 u = __builtin_bit_cast(u32x4, v); c = dot2(u[0], u[0], c); c = dot2(u[1], u[1], c); c = dot2(u[2], u[2], c); return dot2(u[3], u[3], c); }
DEV float halves_sum2(float a, float b) {
    auto r = __builtin_amdgcn_permlane32_swap(__float_as_uint(a), __float_as_uint(b), false, false);
    return __uint_as_float(r[0]) + __uint_as_float(r[1]);
}
template <class T> DEV T lds_ld(lds_ptr p) { return *(__attribute__((address_space(3))) T*)p; }
template <class T> DEV void lds_st(lds_ptr p, T v) { *(__attribute__((address_space(3))) T*)p = v; }
DEV void lds_arrive(lds_ptr cnt, int l) {
    asm volatile("s_waitcnt lgkmcnt(0)" ::: "memory");
    if (l == 0) __hip_atomic_fetch_add((__attribute__((address_space(3))) unsigned*)cnt, 1u, __ATOMIC_RELAXED, __HIP_MEMORY_SCOPE_WORKGROUP);
}
DEV void lds_wait(lds_ptr cnt, unsigned target) {
    while (__hip_atomic_load((__attribute__((address_space(3))) unsigned*)cnt, __ATOMIC_RELAXED, __HIP_MEMORY_SCOPE_WORKGROUP) < target) __builtin_amdgcn_s_sleep(1);
    asm volatile("" ::: "memory");
}

constexpr int SLOT = 40960;
constexpr int L_CS = 114688;
constexpr int L_ST = L_CS + 3072;
constexpr int L_CF = 122880;
constexpr int L_CS2 = L_CF + 4096;
constexpr int L_CNT = L_ST + 1024;

struct P1Ctx {
    const char *x, *sS, *sA, *sB, *sC, *sD;
    unsigned strideP;
    int rot, mt, tg, l;
};

DEV void epi_gu(const f32x16& acc, lds_ptr cf, float nmu, float rstd, char* dst) {
    const f32x4 c0 = lds_ld<f32x4>(cf), c1 = lds_ld<f32x4>(cf + 32), c2 = lds_ld<f32x4>(cf + 64), c3 = lds_ld<f32x4>(cf + 96);
    const float C1 = -2.0f * 0.7978845608028654f * 1.4426950408889634f, C2 = C1 * 0.044715f;
    u32x4 o;
#pragma unroll
    for (int j = 0; j < 4; ++j) {
        f32x2 cg = j < 2 ? f32x2{c0[2 * j], c0[2 * j + 1]} : f32x2{c1[2 * j - 4], c1[2 * j - 3]};
        f32x2 cu = j < 2 ? f32x2{c2[2 * j], c2[2 * j + 1]} : f32x2{c3[2 * j - 4], c3[2 * j - 3]};
        f32x2 ag = {acc[2 * j], acc[2 * j + 1]}, au = {acc[8 + 2 * j], acc[9 + 2 * j]};
        f32x2 g = (cg * nmu + ag) * rstd;
        f32x2 u = (cu * nmu + au) * rstd;
        f32x2 z = g * (g * g * C2 + C1);
        f32x2 e = {__builtin_amdgcn_exp2f(z[0]), __builtin_amdgcn_exp2f(z[1])};
        e = e + 1.0f;
        f32x2 r = {__builtin_amdgcn_rcpf(e[0]), __builtin_amdgcn_rcpf(e[1])};
        f32x2 h = g * u * r;
        o[j] = cvtpk(h[0], h[1]);
    }
    *(u32x4*)dst = o;
}
DEV void epi_qk(const f32x16& acc, lds_ptr cf, float nmu, float s0, float s1, char* d0, char* d1) {
    const f32x4 c0 = lds_ld<f32x4>(cf), c1 = lds_ld<f32x4>(cf + 32), c2 = lds_ld<f32x4>(cf + 64), c3 = lds_ld<f32x4>(cf + 96);
    u32x4 o0, o1;
#pragma unroll
    for (int j = 0; j < 4; ++j) {
        f32x2 ca = j < 2 ? f32x2{c0[2 * j], c0[2 * j + 1]} : f32x2{c1[2 * j - 4], c1[2 * j - 3]};
        f32x2 cb = j < 2 ? f32x2{c2[2 * j], c2[2 * j + 1]} : f32x2{c3[2 * j - 4], c3[2 * j - 3]};
        f32x2 a = {acc[2 * j], acc[2 * j + 1]}, b = {acc[8 + 2 * j], acc[9 + 2 * j]};
        a = (ca * nmu + a) * s0; b = (cb * nmu + b) * s1;
        o0[j] = cvtpk(a[0], a[1]); o1[j] = cvtpk(b[0], b[1]);
    }
    *(u32x4*)d0 = o0; *(u32x4*)d1 = o1;
}

template <int ROLE>
DEV void phase1(const P1Ctx& c, lds_ptr lds, char* ws, int g) {
    const int l = c.l, tg = c.tg, r = l & 31, h = l >> 5, blk = (l >> 4) & 1, q = (l >> 2) & 3, p = l & 3;
    const unsigned lds0 = (unsigned)(uintptr_t)lds;
    const unsigned vx0 = (l >> 3) * 896 + (((l & 7) ^ (l >> 4)) << 4), vx1 = vx0 ^ 64;
    const unsigned chA = (((l & 7) ^ (((l >> 4) & 1) << 2)) << 4);
    const unsigned vS = (l >> 3) * 2688 + chA, vA = (l >> 3) * c.strideP + chA, vB = (l >> 3) * c.strideP + (chA ^ 32);
    const unsigned vC = (l >> 3) * 3584 + chA, vD = vC - chA + (chA ^ 32);
    auto kap = [&](int s) { int k = s + c.rot; return k >= 7 ? k - 7 : k; };
    auto issue1 = [&](int s) {
        const int k = kap(s); const unsigned d = lds0 + ((s + 1) & 3) * SLOT;
        const char* xs = c.x + (size_t)(c.mt * 128 + tg * 32) * 896 + k * 128;
#pragma unroll
        for (int e = 0; e < 4; ++e) glds16(xs + e * 7168, (e & 1) ? vx1 : vx0, d + tg * 4096 + e * 1024);
        const int row = k * 64 + tg * 16;
#pragma unroll
        for (int e = 0; e < 2; ++e) {
            glds16(c.sS + (size_t)(row + 8 * e) * 2688, vS, d + 16384 + tg * 2048 + e * 1024);
            glds16(c.sA + (size_t)(row + 8 * e) * c.strideP, vA, d + 24576 + tg * 2048 + e * 1024);
            glds16(c.sB + (size_t)(row + 8 * e) * c.strideP, vB, d + 32768 + tg * 2048 + e * 1024);
        }
    };
    auto issue2 = [&](int u) {
        const int row = kap(u >> 1) * 64 + tg * 16;
#pragma unroll
        for (int e = 0; e < 2; ++e) glds16(((u & 1) ? c.sD : c.sC) + (size_t)(row + 8 * e) * 3584, (u & 1) ? vD : vC, lds0 + u * 8192 + tg * 2048 + e * 1024);
    };
    const int pp = (ROLE == 1) ? p : ((p == 1) ? 2 : (p == 2) ? 1 : p);
    const int pq = (p == 1) ? 2 : (p == 2) ? 1 : p;
    const int rowoff = (8 * h + q) * 128;
    const int sw = (q >> 1) << 2;
    int aS[2], aP[4];
#pragma unroll
    for (int t = 0; t < 2; ++t) aS[t] = 16384 + rowoff + ((((4 * t + 2 * blk + (pp >> 1)) ^ sw) << 4) | ((pp & 1) << 3));
#pragma unroll
    for (int t = 0; t < 4; ++t) aP[t] = blk * 8192 + rowoff + ((((2 * t + (pq >> 1)) ^ sw ^ (blk << 1)) << 4) | ((pq & 1) << 3));
    const int aX = (tg * 32 + r) * 128 + ((h ^ ((r >> 1) & 7)) << 4);

    bf16x8 xf[28];
    f32x16 acc[4];
#pragma unroll
    for (int t = 0; t < 4; ++t) acc[t] = f32x16{};
    float cs[6] = {0.f, 0.f, 0.f, 0.f, 0.f, 0.f}, sum = 0.f, sq = 0.f;

    if (ROLE != 2) { issue1(0); issue1(1); issue1(2); }
#pragma unroll
    for (int s = 0; s < 7; ++s) {
        if (ROLE != 2) asm volatile("s_waitcnt vmcnt(20) lgkmcnt(0)\n\ts_barrier" ::: "memory");
        else asm volatile("s_waitcnt lgkmcnt(0)\n\ts_barrier" ::: "memory");
        if (ROLE != 2) {
            if (s < 4) issue1(s + 3);
            else { for (int u = 5 * (s - 4); u < 5 * (s - 4) + 5 && u < 14; ++u) issue2(u); }
        }
        lds_ptr sl = lds + ((s + 1) & 3) * SLOT;
#pragma unroll
        for (int ks = 0; ks < 4; ++ks) {
            xf[4 * s + ks] = lds_ld<bf16x8>(sl + (aX ^ (ks << 5)));
            if (ROLE == 2) {
#pragma unroll
                for (int t = 0; t < 4; ++t) acc[t] = MFMA(wfrag(sl + 24576 + aP[t] + ks * 2048), xf[4 * s + ks], acc[t]);
                sum = fsum8(xf[4 * s + ks], sum); sq = fsq8(xf[4 * s + ks], sq);
            } else {
#pragma unroll
                for (int t = 0; t < 2; ++t) {
                    bf16x8 wf = wfrag(sl + aS[t] + ks * 2048);
                    acc[t] = (ROLE == 1) ? MFMA(xf[4 * s + ks], wf, acc[t]) : MFMA(wf, xf[4 * s + ks], acc[t]);
                }
            }
        }
        if (ROLE != 2) {
#pragma unroll
            for (int t = 0; t < 2; ++t) cs[t] = fsum8(wfrag(sl + aS[t] + tg * 2048), cs[t]);
#pragma unroll
            for (int t = 0; t < 4; ++t) cs[2 + t] = fsum8(wfrag(sl + 24576 + aP[t] + tg * 2048), cs[2 + t]);
        }
    }
    float nmu, rstd;
    if (ROLE != 2) {
#pragma unroll
        for (int i = 0; i < 3; ++i) lds_st<float>(lds + L_CS + tg * 768 + i * 256 + l * 4, halves_sum2(cs[2 * i], cs[2 * i + 1]));
        asm volatile("s_waitcnt vmcnt(0) lgkmcnt(0)\n\ts_barrier" ::: "memory");
        f32x2 st = lds_ld<f32x2>(lds + L_ST + (tg * 32 + r) * 8); nmu = st[0]; rstd = st[1];
    } else {
        const float ts = halves_sum2(sum, sum), tq = halves_sum2(sq, sq);
        const float mean = ts * (1.0f / 448.0f);
        const float var = fmaxf(tq * (1.0f / 448.0f) - mean * mean, 0.f);
        const float sd = rbf(__builtin_sqrtf(rbf(rbf(var) + 1e-5f)));
        nmu = -rbf(mean); rstd = 1.0f / sd;
        if (l < 32) lds_st<f32x2>(lds + L_ST + (tg * 32 + r) * 8, f32x2{nmu, rstd});
        if (tg == 0 && l < 2) lds_st<unsigned>(lds + L_CNT + l * 4, 0u);
        asm volatile("s_waitcnt lgkmcnt(0)\n\ts_barrier" ::: "memory");
    }
    const int tile = c.mt * 4 + tg;
    const int role = ROLE == 2 ? 1 : 0;
    lds_ptr cf = lds + L_CF + (role * 4 + tg) * 512;
    auto cs_total = [&](int pair) { lds_ptr b = lds + L_CS + pair * 256 + l * 4;
        return (lds_ld<float>(b) + lds_ld<float>(b + 768)) + (lds_ld<float>(b + 1536) + lds_ld<float>(b + 2304)); };
    if (ROLE == 0) {
        if (g <= 12) {
            lds_st<float>(cf + l * 4, cs_total(0));
            const int line = g - 7; const float sc = line < 7 ? rstd * QSCALE : rstd;
            char* d = ws + WS_QK + ((size_t)tile * 112 + line * 8) * 512 + l * 16;
            epi_qk(acc[0], cf + h * 16, nmu, sc, sc, d, d + 1024);
            epi_qk(acc[1], cf + 128 + h * 16, nmu, sc, sc, d + 2048, d + 3072);
        }
    } else if (ROLE == 1) {
        f32x4 st[8];
#pragma unroll
        for (int i = 0; i < 8; ++i) st[i] = lds_ld<f32x4>(lds + L_ST + (tg * 32 + 8 * (i >> 1) + 4 * h + 2 * (i & 1)) * 8);
#pragma unroll
        for (int t = 0; t < 2; ++t) {
            lds_ptr b = lds + L_CS + (32 * t + r) * 4;
            const float cc = (lds_ld<float>(b) + lds_ld<float>(b + 768)) + (lds_ld<float>(b + 1536) + lds_ld<float>(b + 2304));
            const int col = g * 64 + t * 32 + r, hd = col / 28, dd = col - hd * 28;
            char* d = ws + WS_V + ((size_t)hd * 64 + tile) * 2048 + (h * 32 + dd) * 16;
            u32x4 o[2];
#pragma unroll
            for (int i = 0; i < 8; ++i) {
                const float v0 = (cc * st[i][0] + acc[t][2 * i]) * st[i][1], v1 = (cc * st[i][2] + acc[t][2 * i + 1]) * st[i][3];
                o[i >> 2][i & 3] = cvtpk(v0, v1);
            }
            *(u32x4*)d = o[0]; *(u32x4*)(d + 1024) = o[1];
            if (dd >= 24) { const unsigned f = dd == 24 ? 0x3f803f80u : 0u; u32x4 pad = {f, f, f, f}; *(u32x4*)(d + 64) = pad; *(u32x4*)(d + 1024 + 64) = pad; }
        }
    } else {
        lds_st<float>(cf + l * 4, cs_total(1)); lds_st<float>(cf + 256 + l * 4, cs_total(2));
        if (g < 12) {
            char* d = ws + WS_H + ((size_t)tile * 112 + g * 8) * 1024 + l * 16;
#pragma unroll
            for (int t = 0; t < 4; ++t) epi_gu(acc[t], cf + t * 128 + h * 16, nmu, rstd, d + t * 1024);
        } else {
            const int la = 6 + 2 * (g - 12);
            const float s0 = la < 7 ? rstd * QSCALE : rstd;
            char* d = ws + WS_QK + ((size_t)tile * 112 + la * 8) * 512 + l * 16;
#pragma unroll
            for (int t = 0; t < 4; ++t) epi_qk(acc[t], cf + t * 128 + h * 16, nmu, s0, rstd, d + t * 1024, d + 4096 + t * 1024);
        }
    }
    f32x16 b0 = f32x16{}, b1 = f32x16{};
    float c0 = 0.f, c1 = 0.f;
    const int a0 = aP[2 * role], a1 = aP[2 * role + 1];
#pragma unroll
    for (int j = 0; j < 7; ++j) {
        c0 = fsum8(wfrag(lds + a0 + j * 16384 + tg * 2048), c0);
        c1 = fsum8(wfrag(lds + a1 + j * 16384 + tg * 2048), c1);
    }
    lds_st<float>(lds + L_CS2 + role * 1024 + tg * 256 + l * 4, halves_sum2(c0, c1));
    lds_arrive(lds + L_CNT + role * 4, l);
#pragma unroll
    for (int i = 0; i < 28; ++i) {
        b0 = MFMA(wfrag(lds + a0 + (i >> 2) * 16384 + (i & 3) * 2048), xf[i], b0);
        b1 = MFMA(wfrag(lds + a1 + (i >> 2) * 16384 + (i & 3) * 2048), xf[i], b1);
    }
    lds_wait(lds + L_CNT + role * 4, 4);
    { lds_ptr b = lds + L_CS2 + role * 1024 + l * 4;
      lds_st<float>(cf + l * 4, (lds_ld<float>(b) + lds_ld<float>(b + 256)) + (lds_ld<float>(b + 512) + lds_ld<float>(b + 768))); }
    const int u1 = g < 12 ? 2 * g + 1 : 12 + g;
    char* d = ws + WS_H + ((size_t)tile * 112 + u1 * 4 + 2 * role) * 1024 + l * 16;
    epi_gu(b0, cf + h * 16, nmu, rstd, d);
    epi_gu(b1, cf + 128 + h * 16, nmu, rstd, d + 1024);
}

__global__ __launch_bounds__(512, 2) void k1(const char* x, const char* wqkv, const char* wg, const char* wu, char* ws) {
    extern __shared__ __attribute__((aligned(256))) char smem[];
    const int b = blockIdx.x, xcd = b & 7, i = b >> 3;
    const int w = __builtin_amdgcn_readfirstlane(threadIdx.x >> 6);
    P1Ctx c;
    c.mt = (xcd >> 2) * 8 + (i >> 2);
    const int g = (xcd & 3) * 4 + (i & 3);
    c.rot = ((i >> 2) + 2 * (i & 3)) % 7;
    c.tg = w & 3; c.l = threadIdx.x & 63; c.x = x;
    const int sline = g < 7 ? 14 + g : (g <= 12 ? g - 7 : 0);
    c.sS = wqkv + sline * 128;
    if (g < 12) { c.sA = wg + g * 256; c.sB = wu + g * 256; c.strideP = 3584; }
    else { c.sA = wqkv + (6 + 2 * (g - 12)) * 128; c.sB = c.sA + 128; c.strideP = 2688; }
    const int u1 = g < 12 ? 2 * g + 1 : 12 + g;
    c.sC = wg + u1 * 128; c.sD = wu + u1 * 128;
    lds_ptr lds = (lds_ptr)smem;
    if (w >= 4) phase1<2>(c, lds, ws, g);
    else if (g < 7) phase1<1>(c, lds, ws, g);
    else phase1<0>(c, lds, ws, g);
}


typedef __amdgpu_buffer_rsrc_t rsrc_t;
DEV rsrc_t mk_rsrc(const void* p, unsigned n) { return __builtin_amdgcn_make_buffer_rsrc((void*)p, (short)0, n, 0x00020000); }
template <int AUX> DEV bf16x8 bload(rsrc_t rs, unsigned voff, unsigned soff) { return __builtin_bit_cast(bf16x8, __builtin_amdgcn_raw_buffer_load_b128(rs, voff, soff, AUX)); }
constexpr unsigned WS_BYTES = 32u << 20;
constexpr int L2_RED = 0;
constexpr int L2_RING = 65536;
constexpr int RINGK = 8, RINGB = RINGK * 2048;
constexpr int L2_CNT = L2_RING + 4 * RINGB;
constexpr int L2_MAX = L2_CNT + 256;
constexpr int L2_BYTES = L2_MAX + 2048;

#define EXP(v) __builtin_amdgcn_exp2f(v)
template <bool SAFE>
DEV void attn_loop(char* ws, int head, int qt, int w, int l, const bf16x8 (&qf)[4][2], const float (&mx)[4], f32x16 (&o)[4]) {
    const unsigned c0 = (7 * head) >> 1;
    const unsigned kb = (unsigned)WS_QK + (56 + c0) * 512 + w * 16 * 57344, vb = (unsigned)WS_V + (head * 64 + w * 16) * 2048;
    const unsigned vo = l * 16;
    const rsrc_t rs = mk_rsrc(ws, WS_BYTES);
    bf16x8 kf[4][2], vf[4][2];
#pragma unroll
    for (int t = 0; t < 4; ++t) {
        const int j = (t + qt) & 15;
        kf[t][0] = bload<0>(rs, vo, kb + j * 57344); kf[t][1] = bload<0>(rs, vo + 1024, kb + j * 57344);
        if (t < 3) { vf[t][0] = bload<0>(rs, vo, vb + j * 2048); vf[t][1] = bload<0>(rs, vo + 1024, vb + j * 2048); }
    }
    vf[3][0] = bf16x8{}; vf[3][1] = bf16x8{};
#pragma unroll
    for (int i = 0; i < 4; ++i) o[i] = f32x16{};
    f32x16 sa, sb;
    u32x4 p0 = {0, 0, 0, 0}, p1 = {0, 0, 0, 0};
    sa = MFMA(kf[0][0], qf[0][0], f32x16{}); sa = MFMA(kf[0][1], qf[0][1], sa);
#define STEP(CUR, NXT, t, qi) do { \
        constexpr int tn = ((qi) == 3) ? (((t) + 1) & 3) : (t), qn = ((qi) + 1) & 3, tp = ((qi) == 0) ? (((t) + 3) & 3) : (t), qp = ((qi) + 3) & 3; \
        if (SAFE) { _Pragma("unroll") for (int e = 0; e < 16; ++e) CUR[e] -= mx[qi]; } \
        NXT = MFMA(kf[tn][0], qf[qn][0], f32x16{}); \
        CUR[0] = EXP(CUR[0]); CUR[1] = EXP(CUR[1]); CUR[2] = EXP(CUR[2]); CUR[3] = EXP(CUR[3]); CUR[4] = EXP(CUR[4]); PIN(CUR); SBAR(); \
        o[qp] = MFMA(vf[tp][1], __builtin_bit_cast(bf16x8, p1), o[qp]); \
        CUR[5] = EXP(CUR[5]); CUR[6] = EXP(CUR[6]); CUR[7] = EXP(CUR[7]); p0[0] = cvtpk(CUR[0], CUR[1]); p0[1] = cvtpk(CUR[2], CUR[3]); p0[2] = cvtpk(CUR[4], CUR[5]); p0[3] = cvtpk(CUR[6], CUR[7]); PIN(CUR); PIN(p0); SBAR(); \
        NXT = MFMA(kf[tn][1], qf[qn][1], NXT); \
        if ((qi) == 2) { const int jn = (jj * 4 + (t) + 4 + qt) & 15; kf[t][0] = bload<0>(rk, vo, kb + jn * 57344); kf[t][1] = bload<0>(rk, vo + 1024, kb + jn * 57344); } \
        if ((qi) == 0) { const int jn = (jj * 4 + (t) + 3 + qt) & 15; const rsrc_t rv = ((t) == 0) ? rs : rk; vf[tp][0] = bload<0>(rv, vo, vb + jn * 2048); vf[tp][1] = bload<0>(rv, vo + 1024, vb + jn * 2048); } \
        CUR[8] = EXP(CUR[8]); CUR[9] = EXP(CUR[9]); CUR[10] = EXP(CUR[10]); CUR[11] = EXP(CUR[11]); CUR[12] = EXP(CUR[12]); PIN(CUR); SBAR(); \
        o[qi] = MFMA(vf[t][0], __builtin_bit_cast(bf16x8, p0), o[qi]); \
        CUR[13] = EXP(CUR[13]); CUR[14] = EXP(CUR[14]); CUR[15] = EXP(CUR[15]); p1[0] = cvtpk(CUR[8], CUR[9]); p1[1] = cvtpk(CUR[10], CUR[11]); p1[2] = cvtpk(CUR[12], CUR[13]); p1[3] = cvtpk(CUR[14], CUR[15]); PIN(p1); SBAR(); \
    } while (0)
#pragma unroll 1
    for (int jj = 0; jj < 4; ++jj) {
        const rsrc_t rk = mk_rsrc(ws, jj < 3 ? WS_BYTES : 0u);
        STEP(sa, sb, 0, 0); STEP(sb, sa, 0, 1); STEP(sa, sb, 0, 2); STEP(sb, sa, 0, 3);
        STEP(sa, sb, 1, 0); STEP(sb, sa, 1, 1); STEP(sa, sb, 1, 2); STEP(sb, sa, 1, 3);
        STEP(sa, sb, 2, 0); STEP(sb, sa, 2, 1); STEP(sa, sb, 2, 2); STEP(sb, sa, 2, 3);
        STEP(sa, sb, 3, 0); STEP(sb, sa, 3, 1); STEP(sa, sb, 3, 2); STEP(sb, sa, 3, 3);
    }
#undef STEP
    o[3] = MFMA(vf[3][1], __builtin_bit_cast(bf16x8, p1), o[3]);
}

DEV f32x16 attn_reduce(lds_ptr lds, int w, int l, const f32x16 (&o)[4], unsigned target) {
#pragma unroll
    for (int qi = 0; qi < 4; ++qi)
#pragma unroll
        for (int i = 0; i < 4; ++i) lds_st<f32x4>(lds + L2_RED + (w * 4 + qi) * 4096 + i * 1024 + l * 16, f32x4{o[qi][4 * i], o[qi][4 * i + 1], o[qi][4 * i + 2], o[qi][4 * i + 3]});
    lds_arrive(lds + L2_CNT, l);
    lds_wait(lds + L2_CNT, target);
    f32x16 t;
#pragma unroll
    for (int i = 0; i < 4; ++i) {
        lds_ptr b = lds + L2_RED + w * 4096 + i * 1024 + l * 16;
        f32x4 v = (lds_ld<f32x4>(b) + lds_ld<f32x4>(b + 16384)) + (lds_ld<f32x4>(b + 32768) + lds_ld<f32x4>(b + 49152));
        t[4 * i] = v[0]; t[4 * i + 1] = v[1]; t[4 * i + 2] = v[2]; t[4 * i + 3] = v[3];
    }
    return t;
}

DEV void attention(char* ws, lds_ptr lds, int head, int qt, int w, int l) {
    const int r = l & 31, h = l >> 5;
    const unsigned c0 = (7 * head) >> 1;
    const rsrc_t rs = mk_rsrc(ws, WS_BYTES);
    bf16x8 qf[4][2];
#pragma unroll
    for (int qi = 0; qi < 4; ++qi)
#pragma unroll
        for (int s = 0; s < 2; ++s) qf[qi][s] = bload<0>(rs, l * 16 + s * 1024, (unsigned)WS_QK + ((qt * 4 + qi) * 112 + c0) * 512);
    const unsigned mlo = ((head & 1) && h == 0) ? 0u : ~0u, mhi = (!(head & 1) && h == 1) ? 0u : ~0u;
#pragma unroll
    for (int qi = 0; qi < 4; ++qi) {
        u32x4 a = __builtin_bit_cast(u32x4, qf[qi][0]), b = __builtin_bit_cast(u32x4, qf[qi][1]);
        a[0] &= mlo; a[1] &= mlo; b[2] &= mhi; b[3] &= mhi;
        qf[qi][0] = __builtin_bit_cast(bf16x8, a); qf[qi][1] = __builtin_bit_cast(bf16x8, b);
    }
    f32x16 o[4]; float mx[4] = {0.f, 0.f, 0.f, 0.f};
    attn_loop<false>(ws, head, qt, w, l, qf, mx, o);
    f32x16 t = attn_reduce(lds, w, l, o, 4);
    auto lr = __builtin_amdgcn_permlane32_swap(__float_as_uint(t[12]), __float_as_uint(t[12]), false, false);
    float ls = __uint_as_float(lr[1]);
    const bool bad = !(ls >= 0x1p-64f && ls <= 0x1p64f);
    if (__any(bad) && l == 0) __hip_atomic_fetch_add((__attribute__((address_space(3))) unsigned*)(lds + L2_CNT + 8), 1u, __ATOMIC_RELAXED, __HIP_MEMORY_SCOPE_WORKGROUP);
    lds_arrive(lds + L2_CNT, l);
    lds_wait(lds + L2_CNT, 8);
    if (__builtin_expect(lds_ld<unsigned>(lds + L2_CNT + 8) != 0, 0)) {
        const unsigned kb = (unsigned)WS_QK + (56 + c0) * 512 + w * 16 * 57344;
#pragma unroll
        for (int qi = 0; qi < 4; ++qi) mx[qi] = -INFINITY;
#pragma unroll 1
        for (int j = 0; j < 16; ++j) {
            const bf16x8 k0 = bload<0>(rs, l * 16, kb + j * 57344), k1 = bload<0>(rs, l * 16 + 1024, kb + j * 57344);
#pragma unroll
            for (int qi = 0; qi < 4; ++qi) {
                f32x16 s = MFMA(k0, qf[qi][0], f32x16{}); s = MFMA(k1, qf[qi][1], s);
#pragma unroll
                for (int e = 0; e < 16; ++e) mx[qi] = fmaxf(mx[qi], s[e]);
            }
        }
#pragma unroll
        for (int qi = 0; qi < 4; ++qi) {
            auto m2 = __builtin_amdgcn_permlane32_swap(__float_as_uint(mx[qi]), __float_as_uint(mx[qi]), false, false);
            if (l < 32) lds_st<float>(lds + L2_MAX + w * 512 + (qi * 32 + r) * 4, fmaxf(__uint_as_float(m2[0]), __uint_as_float(m2[1])));
        }
        lds_arrive(lds + L2_CNT, l);
        lds_wait(lds + L2_CNT, 12);
#pragma unroll
        for (int qi = 0; qi < 4; ++qi) { lds_ptr b = lds + L2_MAX + (qi * 32 + r) * 4;
            mx[qi] = fmaxf(fmaxf(lds_ld<float>(b), lds_ld<float>(b + 512)), fmaxf(lds_ld<float>(b + 1024), lds_ld<float>(b + 1536))); }
        attn_loop<true>(ws, head, qt, w, l, qf, mx, o);
        t = attn_reduce(lds, w, l, o, 16);
        lr = __builtin_amdgcn_permlane32_swap(__float_as_uint(t[12]), __float_as_uint(t[12]), false, false);
        ls = __uint_as_float(lr[1]);
    }
    const float inv = 1.0f / ls;
    char* ob = ws + WS_O + (size_t)(qt * 4 + w) * 56 * 512 + r * 16;
#pragma unroll
    for (int i = 0; i < 4; ++i) {
        const int grp = 7 * head + 2 * i + h;
        u32x2 v = {cvtpk(t[4 * i] * inv, t[4 * i + 1] * inv), cvtpk(t[4 * i + 2] * inv, t[4 * i + 3] * inv)};
        if (i < 3 || h == 0) *(u32x2*)(ob + (grp >> 1) * 512 + (grp & 1) * 8) = v;
    }
}

DEV void gload_asm(bf16x8& dst, const void* sbase, unsigned voff) {
    asm volatile("global_load_dwordx4 %0, %1, %2" : "=v"(dst) : "v"(voff), "s"(sbase) : "memory");
}
template <int NK>
DEV void gemm64(f32x16 (&acc)[4], lds_ptr ring, const char* wrow, const char* bsrc, size_t tstride, int l) {
    const int h = l >> 5, blk = (l >> 4) & 1, q = (l >> 2) & 3, p = l & 3;
    const int pq = (p == 1) ? 2 : (p == 2) ? 1 : p;
    const unsigned ring0 = (unsigned)(uintptr_t)ring;
    const unsigned vW = (l >> 3) * 896 + (((l & 7) ^ (((l >> 4) & 1) << 2)) << 4);
    const int sw = (q >> 1) << 2;
    int aW[2];
#pragma unroll
    for (int t = 0; t < 2; ++t) aW[t] = (8 * h + q) * 128 + ((((4 * t + 2 * blk + (pq >> 1)) ^ sw) << 4) | ((pq & 1) << 3));
    constexpr int PD = RINGK - 1;
    bf16x8 bf[RINGK][2];
    auto issue = [&](int i) {
        glds16(wrow + (size_t)i * 16 * 896, vW, ring0 + (i % RINGK) * 2048);
        glds16(wrow + (size_t)i * 16 * 896 + 8 * 896, vW, ring0 + (i % RINGK) * 2048 + 1024);
        gload_asm(bf[i % RINGK][0], bsrc + (size_t)i * 1024, l * 16);
        gload_asm(bf[i % RINGK][1], bsrc + tstride + (size_t)i * 1024, l * 16);
    };
#pragma unroll
    for (int i = 0; i < PD && i < NK; ++i) issue(i);
#pragma unroll
    for (int i = 0; i < NK; ++i) {
        const int rem = (NK - 1 - i) < (PD - 1) ? (NK - 1 - i) : (PD - 1);
        asm volatile("s_waitcnt vmcnt(%2)" : "+v"(bf[i % RINGK][0]), "+v"(bf[i % RINGK][1]) : "i"(4 * rem) : "memory");
        const bf16x8 w0 = wfrag(ring + (i % RINGK) * 2048 + aW[0]), w1 = wfrag(ring + (i % RINGK) * 2048 + aW[1]);
        if (i + PD < NK) issue(i + PD);
        acc[0] = MFMA(w0, bf[i % RINGK][0], acc[0]); acc[1] = MFMA(w0, bf[i % RINGK][1], acc[1]);
        acc[2] = MFMA(w1, bf[i % RINGK][0], acc[2]); acc[3] = MFMA(w1, bf[i % RINGK][1], acc[3]);
        SBAR();
    }
}
DEV f32x16 gemm_reduce(lds_ptr rings, lds_ptr cnt, int w, int l, const f32x16 (&acc)[4]) {
#pragma unroll
    for (int t = 0; t < 4; ++t)
#pragma unroll
        for (int i = 0; i < 4; ++i) lds_st<f32x4>(rings + w * RINGB + t * 4096 + i * 1024 + l * 16, f32x4{acc[t][4 * i], acc[t][4 * i + 1], acc[t][4 * i + 2], acc[t][4 * i + 3]});
    lds_arrive(cnt, l);
    lds_wait(cnt, 4);
    f32x16 t;
#pragma unroll
    for (int i = 0; i < 4; ++i) {
        lds_ptr b = rings + w * 4096 + i * 1024 + l * 16;
        f32x4 v = (lds_ld<f32x4>(b) + lds_ld<f32x4>(b + RINGB)) + (lds_ld<f32x4>(b + 2 * RINGB) + lds_ld<f32x4>(b + 3 * RINGB));
        t[4 * i] = v[0]; t[4 * i + 1] = v[1]; t[4 * i + 2] = v[2]; t[4 * i + 3] = v[3];
    }
    return t;
}

__global__ __launch_bounds__(512, 2) void k2(const char* wd, char* ws) {
    extern __shared__ __attribute__((aligned(256))) char smem[];
    lds_ptr lds = (lds_ptr)smem;
    const int b = blockIdx.x, xcd = b & 7, i = b >> 3, l = threadIdx.x & 63;
    const int w = __builtin_amdgcn_readfirstlane(threadIdx.x >> 6);
    if (threadIdx.x < 4) lds_st<unsigned>(lds + L2_CNT + threadIdx.x * 4, 0u);
    __syncthreads();
    if (w < 4) { attention(ws, lds, 2 * xcd + (i >> 4), i & 15, w, l); return; }
    if (i >= 28) return;
    const int g = w - 4, tgp = xcd * 4 + i / 7, cg = i % 7;
    f32x16 acc[4];
#pragma unroll
    for (int t = 0; t < 4; ++t) acc[t] = f32x16{};
    gemm64<28>(acc, lds + L2_RING + g * RINGB, wd + (size_t)g * 448 * 896 + cg * 128, ws + WS_H + ((size_t)tgp * 2 * 112 + g * 28) * 1024, 112 * 1024, l);
    const f32x16 t = gemm_reduce(lds + L2_RING, lds + L2_CNT + 4, g, l, acc);
    char* d = ws + WS_DP + ((size_t)(tgp * 7 + cg) * 4 + g) * 4096 + l * 16;
#pragma unroll
    for (int k = 0; k < 4; ++k) *(f32x4*)(d + k * 1024) = f32x4{t[4 * k], t[4 * k + 1], t[4 * k + 2], t[4 * k + 3]};
}

__global__ __launch_bounds__(256, 2) void k3(const char* x, const char* wo, char* ws, char* out) {
    extern __shared__ __attribute__((aligned(256))) char smem[];
    lds_ptr lds = (lds_ptr)smem;
    const int b = blockIdx.x, xcd = b & 7, i = b >> 3, l = threadIdx.x & 63, r = l & 31, h = l >> 5;
    const int g = __builtin_amdgcn_readfirstlane(threadIdx.x >> 6);
    const int tgp = xcd * 4 + i / 7, cg = i % 7;
    if (threadIdx.x == 0) lds_st<unsigned>(lds + 4 * RINGB, 0u);
    __syncthreads();
    f32x16 acc[4];
#pragma unroll
    for (int t = 0; t < 4; ++t) acc[t] = f32x16{};
    gemm64<7>(acc, lds + g * RINGB, wo + (size_t)g * 112 * 896 + cg * 128, ws + WS_O + ((size_t)tgp * 2 * 28 + g * 7) * 1024, 28 * 1024, l);
    const size_t eo = (size_t)(tgp * 64 + (g & 1) * 32 + r) * 896 + cg * 128 + (g >> 1) * 64 + h * 16;
    const u32x4 x0 = *(const u32x4*)(x + eo), x1 = *(const u32x4*)(x + eo + 32);
    const char* dp = ws + WS_DP + ((size_t)(tgp * 7 + cg) * 4 + g) * 4096 + l * 16;
    f32x4 dv[4];
#pragma unroll
    for (int k = 0; k < 4; ++k) dv[k] = *(const f32x4*)(dp + k * 1024);
    const f32x16 t = gemm_reduce(lds, lds + 4 * RINGB, g, l, acc);
    u32x4 o0, o1;
#pragma unroll
    for (int j = 0; j < 4; ++j) {
        o0[j] = cvtpk(t[2 * j] + dv[j >> 1][(2 * j) & 3] + __uint_as_float(x0[j] << 16), t[2 * j + 1] + dv[j >> 1][(2 * j + 1) & 3] + __uint_as_float(x0[j] & 0xffff0000u));
        o1[j] = cvtpk(t[8 + 2 * j] + dv[2 + (j >> 1)][(2 * j) & 3] + __uint_as_float(x1[j] << 16), t[9 + 2 * j] + dv[2 + (j >> 1)][(2 * j + 1) & 3] + __uint_as_float(x1[j] & 0xffff0000u));
    }
    *(u32x4*)(out + eo) = o0; *(u32x4*)(out + eo + 32) = o1;
}

extern "C" void kernel_launch(void* const* d_in, const int* in_sizes, int n_in, void* d_out, int out_size, void* d_ws, size_t ws_size, hipStream_t stream) {
    const char* x = (const char*)d_in[0]; const char* wqkv = (const char*)d_in[1]; const char* wo = (const char*)d_in[2];
    const char* wg = (const char*)d_in[4]; const char* wu = (const char*)d_in[5]; const char* wd = (const char*)d_in[6];
    char* ws = (char*)d_ws;
    (void)hipFuncSetAttribute((const void*)k1, hipFuncAttributeMaxDynamicSharedMemorySize, 163840);
    (void)hipFuncSetAttribute((const void*)k2, hipFuncAttributeMaxDynamicSharedMemorySize, L2_BYTES);
    (void)hipFuncSetAttribute((const void*)k3, hipFuncAttributeMaxDynamicSharedMemorySize, 4 * RINGB + 256);
    k1<<<256, 512, 163840, stream>>>(x, wqkv, wg, wu, ws);
    k2<<<256, 512, L2_BYTES, stream>>>(wd, ws);
    k3<<<224, 256, 4 * RINGB + 256, stream>>>(x, wo, ws, (char*)d_out);
}
```

```cpp
#include <hip/hip_runtime.h>
#include <stdint.h>
#include <string.h>

struct alignas(64) Args { uint32_t w[800]; };

__global__ __launch_bounds__(512, 2) void fused(Args) {
    auto ka = __builtin_amdgcn_kernarg_segment_ptr();
    unsigned b = blockIdx.x, t = threadIdx.x;
    asm volatile(
    "start_%=:\n"
    "s_load_dwordx8 s[24:31], s[0:1], 0\n"
    "v_readfirstlane_b32 s3, v0\n"
    "s_and_b32 s4, s2, 3\n"
    "s_bfe_u32 s5, s2, 0x20003\n"
    "s_lshl2_add_u32 s22, s5, s4\n"
    "s_lshl2_add_u32 s20, s4, s5\n"
    "s_lshr_b32 s6, s2, 5\n"
    "s_and_b32 s4, s2, 4\n"
    "s_lshl1_add_u32 s23, s4, s6\n"
    "v_and_b32 v64, 63, v0\n"
    "v_lshlrev_b32 v15, 4, v64\n"
    "s_bitcmp1_b32 s3, 8\n"
    "s_cbranch_scc0 a0_%=\n"
    "s_getpc_b64 s[96:97]\n"
    "pf_%=:\n"
    "s_sub_u32 s96, s96, pf_%=-start_%=\n"
    "s_subb_u32 s97, s97, 0\n"
    "s_and_b32 s4, s3, 0xc0\n"
    "s_lshl2_add_u32 s4, s4, s2\n"
    "s_lshr_b32 s4, s4, 3\n"
    "s_lshl_b32 s4, s4, 12\n"
    "v_lshlrev_b32 v77, 6, v64\n"
    "v_add_u32 v77, s4, v77\n"
    "v_min_u32 v77, last_%=-start_%=, v77\n"
    "s_nop 1\n"
    "global_load_dword v29, v77, s[96:97]\n"
    "s_branch g_%=\n"
    "a0_%=:\n"
    "s_lshl_b32 s4, s22, 7\n"
    "s_load_dwordx16 s[32:47], s[0:1], s4 offset:128\n"
    "s_load_dwordx16 s[48:63], s[0:1], s4 offset:192\n"
    "s_mov_b32 s64, 0\n"
    "s_lshl_b32 s67, s3, 5\n"
    "s_lshl_b32 s66, s3, 6\n"
    "s_lshr_b32 s7, s3, 6\n"
    "s_lshl2_add_u32 s8, s23, s7\n"
    "s_mul_i32 s9, s8, 28672\n"
    "s_lshr_b32 s10, s3, 2\n"
    "s_lshl_b32 s11, s22, 7\n"
    "s_add_u32 s11, s11, 1536\n"
    "v_lshrrev_b32 v66, 3, v64\n"
    "v_and_b32 v73, 0x70, v15\n"
    "v_lshlrev_b32 v65, 2, v64\n"
    "v_and_b32 v65, 64, v65\n"
    "v_xor_b32 v65, v65, v73\n"
    "v_and_b32 v74, 0x30, v64\n"
    "v_xor_b32 v74, v74, v73\n"
    "v_mul_u32_u24 v8, 896, v66\n"
    "v_add3_u32 v8, v8, s9, v74\n"
    "v_xor_b32 v9, 64, v8\n"
    "v_add_u32 v66, s10, v66\n"
    "v_mul_u32_u24 v10, 2688, v66\n"
    "v_mul_u32_u24 v13, 3584, v66\n"
    "v_add3_u32 v13, v13, s11, v65\n"
    "v_xor_b32 v14, 32, v13\n"
    "v_xor_b32 v74, 32, v65\n"
    "s_waitcnt lgkmcnt(0)\n"
    "v_mul_u32_u24 v12, s52, v66\n"
    "v_add3_u32 v10, v10, s53, v65\n"
    "v_add3_u32 v11, v12, s54, v65\n"
    "v_add3_u32 v12, v12, s55, v74\n"
    "s_lshl_b32 s65, s52, 6\n"
    "s_lshl_b32 s76, s52, 3\n"
    "s_sub_u32 s76, s76, 1024\n"
    "s_lshl_b32 s72, s64, 7\n"
    "s_add_u32 m0, s66, 40960\n"
    "s_mul_i32 s73, s64, 172032\n"
    "buffer_load_dwordx4 v8, s[28:31], s72 offen lds\n"
    "s_add_u32 s72, s72, 6144\n"
    "buffer_load_dwordx4 v9, s[28:31], s72 offen offset:1024 lds\n"
    "s_add_u32 s72, s72, 6144\n"
    "buffer_load_dwordx4 v8, s[28:31], s72 offen offset:2048 lds\n"
    "s_add_u32 s72, s72, 6144\n"
    "buffer_load_dwordx4 v9, s[28:31], s72 offen offset:3072 lds\n"
    "s_add_u32 m0, s67, 57344\n"
    "s_mul_i32 s74, s64, s65\n"
    "buffer_load_dwordx4 v10, s[32:35], s73 offen lds\n"
    "s_add_u32 s73, s73, 20480\n"
    "buffer_load_dwordx4 v10, s[32:35], s73 offen offset:1024 lds\n"
    "s_add_u32 m0, s67, 65536\n"
    "s_add_u32 s78, s74, s76\n"
    "buffer_load_dwordx4 v11, s[36:39], s74 offen lds\n"
    "buffer_load_dwordx4 v11, s[36:39], s78 offen offset:1024 lds\n"
    "s_add_u32 m0, s67, 73728\n"
    "s_add_u32 s64, s64, 1\n"
    "s_cmp_eq_u32 s64, 7\n"
    "s_cselect_b32 s64, 0, s64\n"
    "buffer_load_dwordx4 v12, s[40:43], s74 offen lds\n"
    "buffer_load_dwordx4 v12, s[40:43], s78 offen offset:1024 lds\n"
    "s_lshl4_add_u32 s4, s23, s22\n"
    "s_lshl2_add_u32 s4, s4, s7\n"
    "v_lshrrev_b32 v70, 5, v64\n"
    "v_lshl_add_u32 v79, s4, 1, v70\n"
    "v_and_b32 v69, 0x1f0, v15\n"
    "v_cmp_gt_u32 vcc, 448, v69\n"
    "v_mul_u32_u24 v74, 896, v79\n"
    "v_add_u32 v74, v74, v69\n"
    "v_bfrev_b32 v78, 2\n"
    "v_cndmask_b32 v40, v78, v74, vcc\n"
    "buffer_load_dwordx4 v[32:35], v40, s[28:31], 0 offen\n"
    "buffer_load_dwordx4 v[36:39], v40, s[28:31], 0 offen offset:448\n"
    "v_cmp_eq_u32 vcc, 0x1f0, v69\n"
    "v_lshlrev_b32 v79, 3, v79\n"
    "v_add_u32 v79, 16777216, v79\n"
    "v_cndmask_b32 v24, v78, v79, vcc\n"
    "s_mov_b32 s4, 17055744\n"
    "buffer_load_dword v48, off, s[24:27], s4 sc1\n"
    "s_and_b32 s12, s7, 1\n"
    "s_lshr_b32 s13, s7, 1\n"
    "v_and_b32 v67, 44, v64\n"
    "v_lshlrev_b32 v67, 5, v67\n"
    "v_and_b32 v73, 8, v64\n"
    "v_lshl_or_b32 v67, v73, 3, v67\n"
    "v_and_b32 v74, 16, v64\n"
    "v_lshl_or_b32 v67, v74, 1, v67\n"
    "v_and_b32 v68, 3, v64\n"
    "v_mul_u32_u24 v73, 20, v68\n"
    "v_and_or_b32 v75, v73, 24, v67\n"
    "v_lshl_or_b32 v76, v74, 9, v75\n"
    "v_and_b32 v69, 0x1f0, v15\n"
    "v_lshrrev_b32 v70, 5, v64\n"
    "v_bfe_u32 v73, v64, 1, 3\n"
    "v_xor_b32 v73, v73, v70\n"
    "v_lshlrev_b32 v73, 4, v73\n"
    "v_lshl_or_b32 v73, v69, 3, v73\n"
    "s_lshl_b32 s4, s7, 12\n"
    "s_lshl_b32 s5, s13, 5\n"
    "s_or_b32 s4, s4, s5\n"
    "v_xor_b32 v0, s4, v73\n"
    "v_xor_b32 v1, 32, v0\n"
    "v_xor_b32 v2, 64, v0\n"
    "v_xor_b32 v3, 0x60, v0\n"
    "v_and_b32 v71, 1, v64\n"
    "v_lshlrev_b32 v71, 8, v71\n"
    "v_and_b32 v73, 48, v64\n"
    "v_lshl_or_b32 v71, v73, 2, v71\n"
    "v_and_b32 v73, 6, v64\n"
    "v_lshl_or_b32 v71, v73, 3, v71\n"
    "v_lshl_or_b32 v72, v70, 3, v69\n"
    "s_lshl_b32 s14, s13, 11\n"
    "s_lshl_b32 s4, s12, 5\n"
    "s_or_b32 s4, s4, s14\n"
    "v_xor_b32 v16, s4, v76\n"
    "v_xor_b32 v17, 32, v16\n"
    "v_xor_b32 v18, 0x800, v16\n"
    "v_xor_b32 v19, 0x800, v17\n"
    "s_lshl_b32 s4, s12, 9\n"
    "s_add_u32 s4, s4, 117760\n"
    "s_lshl3_add_u32 s5, s13, s4\n"
    "v_add_u32 v21, s5, v71\n"
    "v_add_u32 v23, s4, v72\n"
    "s_lshl_b32 s15, s7, 9\n"
    "s_lshl_b32 s4, s8, 8\n"
    "s_add_u32 s4, s4, 16777216\n"
    "v_lshrrev_b32 v73, 1, v69\n"
    "v_add_u32 v25, s4, v73\n"
    "s_lshl_b32 s4, s12, 10\n"
    "v_add_u32 v27, s4, v15\n"
    "v_lshrrev_b32 v73, 4, v64\n"
    "v_xor_b32 v73, v73, v64\n"
    "v_and_b32 v73, 1, v73\n"
    "v_add_u32 v73, -1, v73\n"
    "v_and_b32 v244, 0x3f803f80, v73\n"
    "s_mov_b32 s21, 122380\n"
    "v_mul_u32_u24 v73, s63, v68\n"
    "v_and_or_b32 v73, v73, 24, v67\n"
    "s_lshl_b32 s4, s12, 6\n"
    "s_or_b32 s4, s4, s14\n"
    "s_or_b32 s4, s4, 0x4000\n"
    "v_xor_b32 v4, s4, v73\n"
    "v_xor_b32 v5, 64, v4\n"
    "v_xor_b32 v6, 0x800, v4\n"
    "v_xor_b32 v7, 0x800, v5\n"
    "v_subrev_u32 v20, 0x400, v21\n"
    "v_subrev_u32 v22, 0x400, v23\n"
    "s_lshl_b32 s4, s12, 11\n"
    "v_add_u32 v26, s4, v15\n"
    "s_lshl_b32 s9, s8, 17\n"
    "s_add_u32 s56, s56, s9\n"
    "s_lshl_b32 s4, s22, 7\n"
    "s_lshl2_add_u32 s70, s23, s4\n"
    "s_lshl_b32 s70, s70, 5\n"
    "s_add_u32 s70, s70, 16793600\n"
    "v_and_b32 v73, 28, v64\n"
    "v_bfe_u32 v74, s62, v73, 4\n"
    "v_lshl_add_u32 v77, s7, 2, v68\n"
    "v_mov_b32 v78, s23\n"
    "v_cmp_gt_u32 vcc, 16, v73\n"
    "s_nop 1\n"
    "v_cndmask_b32 v77, v78, v77, vcc\n"
    "v_lshlrev_b32 v74, 12, v74\n"
    "v_lshl_add_u32 v77, v77, 7, v74\n"
    "v_add_u32 v30, 16793600, v77\n"
    "s_lshl_b32 s16, s7, 21\n"
    "s_add_u32 s18, s9, s61\n"
    "s_sub_u32 s18, s18, 28672\n"
    "s_lshl_b32 s19, s20, 11\n"
    "s_add_u32 s19, s19, 57344\n"
    "s_lshr_b32 s4, s20, 1\n"
    "s_mul_i32 s4, s4, 3584\n"
    "s_and_b32 s20, s20, 1\n"
    "s_add_u32 s4, s4, s9\n"
    "s_add_u32 s4, s4, 90112\n"
    "s_lshl3_add_u32 s68, s20, s4\n"
    "s_lshl_b32 s5, s20, 11\n"
    "s_add_u32 s71, s4, s5\n"
    "s_mov_b32 s77, 0xffff0000\n"
    "s_mov_b32 s80, 0xcccccccc\n"
    "s_mov_b32 s81, 0xcccccccc\n"
    "s_mov_b32 s82, 0xf0f0f0f0\n"
    "s_mov_b32 s83, 0xf0f0f0f0\n"
    "v_mov_b32 v245, v244\n"
    "v_mov_b32 v246, v244\n"
    "v_mov_b32 v247, v244\n"
    "v_mov_b32 v250, 0\n"
    "v_mov_b32 v251, 0\n"
    "s_lshl_b32 s72, s64, 7\n"
    "s_add_u32 m0, s66, 81920\n"
    "s_mul_i32 s73, s64, 172032\n"
    "buffer_load_dwordx4 v8, s[28:31], s72 offen lds\n"
    "s_add_u32 s72, s72, 6144\n"
    "buffer_load_dwordx4 v9, s[28:31], s72 offen offset:1024 lds\n"
    "s_add_u32 s72, s72, 6144\n"
    "buffer_load_dwordx4 v8, s[28:31], s72 offen offset:2048 lds\n"
    "s_add_u32 s72, s72, 6144\n"
    "buffer_load_dwordx4 v9, s[28:31], s72 offen offset:3072 lds\n"
    "s_add_u32 m0, s67, 98304\n"
    "s_mul_i32 s74, s64, s65\n"
    "buffer_load_dwordx4 v10, s[32:35], s73 offen lds\n"
    "s_add_u32 s73, s73, 20480\n"
    "buffer_load_dwordx4 v10, s[32:35], s73 offen offset:1024 lds\n"
    "s_add_u32 m0, s67, 106496\n"
    "s_add_u32 s78, s74, s76\n"
    "buffer_load_dwordx4 v11, s[36:39], s74 offen lds\n"
    "buffer_load_dwordx4 v11, s[36:39], s78 offen offset:1024 lds\n"
    "s_add_u32 m0, s67, 114688\n"
    "s_add_u32 s64, s64, 1\n"
    "s_cmp_eq_u32 s64, 7\n"
    "s_cselect_b32 s64, 0, s64\n"
    "buffer_load_dwordx4 v12, s[40:43], s74 offen lds\n"
    "buffer_load_dwordx4 v12, s[40:43], s78 offen offset:1024 lds\n"
    "s_cmp_eq_u32 s57, 1\n"
    "s_cbranch_scc1 av_%=\n"
    "s_waitcnt vmcnt(13)\n"
    "s_barrier\n"
    "ds_read_b128 v[96:99], v0 offset:40960\n"
    "ds_read_b64_tr_b16 v[208:209], v4 offset:40960\n"
    "ds_read_b64_tr_b16 v[210:211], v4 offset:41472\n"
    "ds_read_b64_tr_b16 v[212:213], v5 offset:40960\n"
    "ds_read_b64_tr_b16 v[214:215], v5 offset:41472\n"
    "ds_read_b128 v[100:103], v1 offset:40960\n"
    "ds_read_b64_tr_b16 v[216:217], v6 offset:40960\n"
    "ds_read_b64_tr_b16 v[218:219], v6 offset:41472\n"
    "ds_read_b64_tr_b16 v[220:221], v7 offset:40960\n"
    "ds_read_b64_tr_b16 v[222:223], v7 offset:41472\n"
    "s_waitcnt lgkmcnt(7)\n"
    "v_mfma_f32_32x32x16_bf16 v[144:159], v[208:211], v[96:99], 0\n"
    "ds_read_b128 v[104:107], v2 offset:40960\n"
    "ds_read_b64_tr_b16 v[224:225], v4 offset:45056\n"
    "ds_read_b64_tr_b16 v[226:227], v4 offset:45568\n"
    "ds_read_b64_tr_b16 v[228:229], v5 offset:45056\n"
    "ds_read_b64_tr_b16 v[230:231], v5 offset:45568\n"
    "v_mfma_f32_16x16x32_bf16 v[240:243], v[208:211], v[244:247], 0\n"
    "s_lshl_b32 s72, s64, 7\n"
    "s_add_u32 m0, s66, 122880\n"
    "s_mul_i32 s73, s64, 172032\n"
    "buffer_load_dwordx4 v8, s[28:31], s72 offen lds\n"
    "s_add_u32 s72, s72, 6144\n"
    "buffer_load_dwordx4 v9, s[28:31], s72 offen offset:1024 lds\n"
    "s_waitcnt vmcnt(14)\n"
    "v_lshlrev_b32 v44, 16, v32\n"
    "v_and_b32 v45, s77, v32\n"
    "v_mov_b32 v40, v44\n"
    "v_mul_f32 v41, v44, v44\n"
    "v_mov_b32 v42, v45\n"
    "v_mul_f32 v43, v45, v45\n"
    "v_lshlrev_b32 v44, 16, v33\n"
    "v_and_b32 v45, s77, v33\n"
    "v_add_f32 v40, v40, v44\n"
    "v_fmac_f32 v41, v44, v44\n"
    "v_add_f32 v42, v42, v45\n"
    "v_fmac_f32 v43, v45, v45\n"
    "v_lshlrev_b32 v44, 16, v34\n"
    "v_and_b32 v45, s77, v34\n"
    "v_add_f32 v40, v40, v44\n"
    "v_fmac_f32 v41, v44, v44\n"
    "v_add_f32 v42, v42, v45\n"
    "v_fmac_f32 v43, v45, v45\n"
    "s_waitcnt lgkmcnt(10)\n"
    "v_mfma_f32_32x32x16_bf16 v[160:175], v[212:215], v[96:99], 0\n"
    "ds_read_b128 v[108:111], v3 offset:40960\n"
    "ds_read_b64_tr_b16 v[232:233], v6 offset:45056\n"
    "ds_read_b64_tr_b16 v[234:235], v6 offset:45568\n"
    "ds_read_b64_tr_b16 v[236:237], v7 offset:45056\n"
    "ds_read_b64_tr_b16 v[238:239], v7 offset:45568\n"
    "s_add_u32 s72, s72, 6144\n"
    "buffer_load_dwordx4 v8, s[28:31], s72 offen offset:2048 lds\n"
    "s_add_u32 s72, s72, 6144\n"
    "buffer_load_dwordx4 v9, s[28:31], s72 offen offset:3072 lds\n"
    "s_add_u32 m0, s67, 139264\n"
    "s_mul_i32 s74, s64, s65\n"
    "buffer_load_dwordx4 v10, s[32:35], s73 offen lds\n"
    "v_lshlrev_b32 v44, 16, v35\n"
    "v_and_b32 v45, s77, v35\n"
    "v_add_f32 v40, v40, v44\n"
    "v_fmac_f32 v41, v44, v44\n"
    "v_add_f32 v42, v42, v45\n"
    "v_fmac_f32 v43, v45, v45\n"
    "s_waitcnt vmcnt(16)\n"
    "v_lshlrev_b32 v44, 16, v36\n"
    "v_and_b32 v45, s77, v36\n"
    "v_add_f32 v40, v40, v44\n"
    "v_fmac_f32 v41, v44, v44\n"
    "v_add_f32 v42, v42, v45\n"
    "v_fmac_f32 v43, v45, v45\n"
    "v_lshlrev_b32 v44, 16, v37\n"
    "v_and_b32 v45, s77, v37\n"
    "v_add_f32 v40, v40, v44\n"
    "v_fmac_f32 v41, v44, v44\n"
    "v_add_f32 v42, v42, v45\n"
    "v_fmac_f32 v43, v45, v45\n"
    "s_waitcnt lgkmcnt(12)\n"
    "v_mfma_f32_32x32x16_bf16 v[144:159], v[216:219], v[100:103], v[144:159]\n"
    "v_add_u32 v0, 0x14000, v0\n"
    "v_add_u32 v1, 0x14000, v1\n"
    "v_add_u32 v2, 0x14000, v2\n"
    "v_add_u32 v3, 0x14000, v3\n"
    "v_add_u32 v4, 0x14000, v4\n"
    "s_add_u32 s73, s73, 20480\n"
    "buffer_load_dwordx4 v10, s[32:35], s73 offen offset:1024 lds\n"
    "s_add_u32 m0, s67, 147456\n"
    "s_add_u32 s78, s74, s76\n"
    "buffer_load_dwordx4 v11, s[36:39], s74 offen lds\n"
    "v_lshlrev_b32 v44, 16, v38\n"
    "v_and_b32 v45, s77, v38\n"
    "v_add_f32 v40, v40, v44\n"
    "v_fmac_f32 v41, v44, v44\n"
    "v_add_f32 v42, v42, v45\n"
    "v_fmac_f32 v43, v45, v45\n"
    "v_lshlrev_b32 v44, 16, v39\n"
    "v_and_b32 v45, s77, v39\n"
    "v_add_f32 v40, v40, v44\n"
    "v_fmac_f32 v41, v44, v44\n"
    "v_add_f32 v42, v42, v45\n"
    "v_fmac_f32 v43, v45, v45\n"
    "v_add_f32 v40, v40, v42\n"
    "v_add_f32 v41, v41, v43\n"
    "s_waitcnt vmcnt(17)\n"
    "v_readfirstlane_b32 s69, v48\n"
    "v_add_f32_dpp v40, v40, v40 row_shr:1 row_mask:0xf bank_mask:0xf\n"
    "v_add_f32_dpp v41, v41, v41 row_shr:1 row_mask:0xf bank_mask:0xf\n"
    "s_nop 0\n"
    "v_add_f32_dpp v40, v40, v40 row_shr:2 row_mask:0xf bank_mask:0xf\n"
    "s_waitcnt lgkmcnt(10)\n"
    "v_mfma_f32_32x32x16_bf16 v[160:175], v[220:223], v[100:103], v[160:175]\n"
    "v_add_u32 v5, 0x14000, v5\n"
    "v_add_u32 v6, 0x14000, v6\n"
    "v_add_u32 v7, 0x14000, v7\n"
    "buffer_load_dwordx4 v11, s[36:39], s78 offen offset:1024 lds\n"
    "s_add_u32 m0, s67, 155648\n"
    "s_add_u32 s64, s64, 1\n"
    "s_cmp_eq_u32 s64, 7\n"
    "s_cselect_b32 s64, 0, s64\n"
    "buffer_load_dwordx4 v12, s[40:43], s74 offen lds\n"
    "buffer_load_dwordx4 v12, s[40:43], s78 offen offset:1024 lds\n"
    "v_add_f32_dpp v41, v41, v41 row_shr:2 row_mask:0xf bank_mask:0xf\n"
    "v_add_f32_dpp v40, v40, v40 row_shr:4 row_mask:0xf bank_mask:0xf\n"
    "s_nop 0\n"
    "v_add_f32_dpp v41, v41, v41 row_shr:4 row_mask:0xf bank_mask:0xf\n"
    "v_add_f32_dpp v40, v40, v40 row_shr:8 row_mask:0xf bank_mask:0xf\n"
    "s_nop 0\n"
    "v_add_f32_dpp v41, v41, v41 row_shr:8 row_mask:0xf bank_mask:0xf\n"
    "v_add_f32_dpp v40, v40, v40 row_bcast:15 row_mask:0xa bank_mask:0xf\n"
    "s_nop 0\n"
    "v_add_f32_dpp v41, v41, v41 row_bcast:15 row_mask:0xa bank_mask:0xf\n"
    "s_add_u32 s69, s69, 1\n"
    "v_mul_f32 v40, 0x3b124925, v40\n"
    "v_mul_f32 v41, 0x3b124925, v41\n"
    "v_fma_f32 v41, -v40, v40, v41\n"
    "v_max_f32 v41, 0, v41\n"
    "v_cvt_pk_bf16_f32 v41, 0, v41\n"
    "v_add_f32 v41, 0x3727c5ac, v41\n"
    "v_cvt_pk_bf16_f32 v41, 0, v41\n"
    "v_sqrt_f32 v41, v41\n"
    "v_mov_b32 v43, s69\n"
    "v_cvt_pk_bf16_f32 v42, -v40, v41\n"
    "buffer_store_dwordx2 v[42:43], v24, s[24:27], 0 offen sc1\n"
    "s_waitcnt lgkmcnt(7)\n"
    "v_mfma_f32_32x32x16_bf16 v[144:159], v[224:227], v[104:107], v[144:159]\n"
    "s_waitcnt vmcnt(11) lgkmcnt(0)\n"
    "s_barrier\n"
    "ds_read_b128 v[112:115], v0 offset:0\n"
    "ds_read_b64_tr_b16 v[208:209], v4 offset:0\n"
    "ds_read_b64_tr_b16 v[210:211], v4 offset:512\n"
    "ds_read_b64_tr_b16 v[212:213], v5 offset:0\n"
    "ds_read_b64_tr_b16 v[214:215], v5 offset:512\n"
    "v_mfma_f32_16x16x32_bf16 v[240:243], v[224:227], v[244:247], v[240:243]\n"
    "s_lshl_b32 s72, s64, 7\n"
    "s_add_u32 m0, s66, 0\n"
    "s_mul_i32 s73, s64, 172032\n"
    "buffer_load_dwordx4 v8, s[28:31], s72 offen lds\n"
    "v_mfma_f32_32x32x16_bf16 v[160:175], v[228:231], v[104:107], v[160:175]\n"
    "ds_read_b128 v[116:119], v1 offset:0\n"
    "ds_read_b64_tr_b16 v[216:217], v6 offset:0\n"
    "ds_read_b64_tr_b16 v[218:219], v6 offset:512\n"
    "ds_read_b64_tr_b16 v[220:221], v7 offset:0\n"
    "ds_read_b64_tr_b16 v[222:223], v7 offset:512\n"
    "s_add_u32 s72, s72, 6144\n"
    "buffer_load_dwordx4 v9, s[28:31], s72 offen offset:1024 lds\n"
    "s_add_u32 s72, s72, 6144\n"
    "buffer_load_dwordx4 v8, s[28:31], s72 offen offset:2048 lds\n"
    "v_mfma_f32_32x32x16_bf16 v[144:159], v[232:235], v[108:111], v[144:159]\n"
    "ds_read_b128 v[120:123], v2 offset:0\n"
    "ds_read_b64_tr_b16 v[224:225], v4 offset:4096\n"
    "ds_read_b64_tr_b16 v[226:227], v4 offset:4608\n"
    "s_add_u32 s72, s72, 6144\n"
    "buffer_load_dwordx4 v9, s[28:31], s72 offen offset:3072 lds\n"
    "s_add_u32 m0, s67, 16384\n"
    "s_mul_i32 s74, s64, s65\n"
    "buffer_load_dwordx4 v10, s[32:35], s73 offen lds\n"
    "v_mfma_f32_32x32x16_bf16 v[160:175], v[236:239], v[108:111], v[160:175]\n"
    "ds_read_b64_tr_b16 v[228:229], v5 offset:4096\n"
    "ds_read_b64_tr_b16 v[230:231], v5 offset:4608\n"
    "s_add_u32 s73, s73, 20480\n"
    "buffer_load_dwordx4 v10, s[32:35], s73 offen offset:1024 lds\n"
    "s_waitcnt lgkmcnt(12)\n"
    "v_mfma_f32_32x32x16_bf16 v[144:159], v[208:211], v[112:115], v[144:159]\n"
    "ds_read_b128 v[124:127], v3 offset:0\n"
    "ds_read_b64_tr_b16 v[232:233], v6 offset:4096\n"
    "ds_read_b64_tr_b16 v[234:235], v6 offset:4608\n"
    "v_mfma_f32_16x16x32_bf16 v[240:243], v[208:211], v[244:247], v[240:243]\n"
    "s_add_u32 m0, s67, 24576\n"
    "s_add_u32 s78, s74, s76\n"
    "buffer_load_dwordx4 v11, s[36:39], s74 offen lds\n"
    "buffer_load_dwordx4 v11, s[36:39], s78 offen offset:1024 lds\n"
    "s_waitcnt lgkmcnt(13)\n"
    "v_mfma_f32_32x32x16_bf16 v[160:175], v[212:215], v[112:115], v[160:175]\n"
    "ds_read_b64_tr_b16 v[236:237], v7 offset:4096\n"
    "ds_read_b64_tr_b16 v[238:239], v7 offset:4608\n"
    "s_add_u32 m0, s67, 32768\n"
    "s_add_u32 s64, s64, 1\n"
    "s_cmp_eq_u32 s64, 7\n"
    "s_cselect_b32 s64, 0, s64\n"
    "buffer_load_dwordx4 v12, s[40:43], s74 offen lds\n"
    "buffer_load_dwordx4 v12, s[40:43], s78 offen offset:1024 lds\n"
    "s_waitcnt lgkmcnt(12)\n"
    "v_mfma_f32_32x32x16_bf16 v[144:159], v[216:219], v[116:119], v[144:159]\n"
    "s_lshl_b32 s72, s64, 7\n"
    "s_add_u32 m0, s66, 40960\n"
    "s_mul_i32 s73, s64, 172032\n"
    "buffer_load_dwordx4 v8, s[28:31], s72 offen lds\n"
    "s_add_u32 s72, s72, 6144\n"
    "buffer_load_dwordx4 v9, s[28:31], s72 offen offset:1024 lds\n"
    "s_waitcnt lgkmcnt(10)\n"
    "v_mfma_f32_32x32x16_bf16 v[160:175], v[220:223], v[116:119], v[160:175]\n"
    "s_waitcnt lgkmcnt(7)\n"
    "v_mfma_f32_32x32x16_bf16 v[144:159], v[224:227], v[120:123], v[144:159]\n"
    "s_waitcnt vmcnt(13) lgkmcnt(0)\n"
    "s_barrier\n"
    "ds_read_b128 v[128:131], v0 offset:40960\n"
    "ds_read_b64_tr_b16 v[208:209], v4 offset:40960\n"
    "ds_read_b64_tr_b16 v[210:211], v4 offset:41472\n"
    "ds_read_b64_tr_b16 v[212:213], v5 offset:40960\n"
    "ds_read_b64_tr_b16 v[214:215], v5 offset:41472\n"
    "v_mfma_f32_16x16x32_bf16 v[240:243], v[224:227], v[244:247], v[240:243]\n"
    "s_add_u32 s72, s72, 6144\n"
    "buffer_load_dwordx4 v8, s[28:31], s72 offen offset:2048 lds\n"
    "v_mfma_f32_32x32x16_bf16 v[160:175], v[228:231], v[120:123], v[160:175]\n"
    "ds_read_b128 v[132:135], v1 offset:40960\n"
    "ds_read_b64_tr_b16 v[216:217], v6 offset:40960\n"
    "ds_read_b64_tr_b16 v[218:219], v6 offset:41472\n"
    "ds_read_b64_tr_b16 v[220:221], v7 offset:40960\n"
    "ds_read_b64_tr_b16 v[222:223], v7 offset:41472\n"
    "s_add_u32 s72, s72, 6144\n"
    "buffer_load_dwordx4 v9, s[28:31], s72 offen offset:3072 lds\n"
    "s_add_u32 m0, s67, 57344\n"
    "s_mul_i32 s74, s64, s65\n"
    "buffer_load_dwordx4 v10, s[32:35], s73 offen lds\n"
    "v_mfma_f32_32x32x16_bf16 v[144:159], v[232:235], v[124:127], v[144:159]\n"
    "ds_read_b128 v[136:139], v2 offset:40960\n"
    "ds_read_b64_tr_b16 v[224:225], v4 offset:45056\n"
    "ds_read_b64_tr_b16 v[226:227], v4 offset:45568\n"
    "s_add_u32 s73, s73, 20480\n"
    "buffer_load_dwordx4 v10, s[32:35], s73 offen offset:1024 lds\n"
    "s_add_u32 m0, s67, 65536\n"
    "s_add_u32 s78, s74, s76\n"
    "buffer_load_dwordx4 v11, s[36:39], s74 offen lds\n"
    "v_mfma_f32_32x32x16_bf16 v[160:175], v[236:239], v[124:127], v[160:175]\n"
    "ds_read_b64_tr_b16 v[228:229], v5 offset:45056\n"
    "ds_read_b64_tr_b16 v[230:231], v5 offset:45568\n"
    "buffer_load_dwordx4 v11, s[36:39], s78 offen offset:1024 lds\n"
    "s_waitcnt lgkmcnt(12)\n"
    "v_mfma_f32_32x32x16_bf16 v[144:159], v[208:211], v[128:131], v[144:159]\n"
    "ds_read_b128 v[140:143], v3 offset:40960\n"
    "ds_read_b64_tr_b16 v[232:233], v6 offset:45056\n"
    "ds_read_b64_tr_b16 v[234:235], v6 offset:45568\n"
    "v_mfma_f32_16x16x32_bf16 v[240:243], v[208:211], v[244:247], v[240:243]\n"
    "s_add_u32 m0, s67, 73728\n"
    "s_add_u32 s64, s64, 1\n"
    "s_cmp_eq_u32 s64, 7\n"
    "s_cselect_b32 s64, 0, s64\n"
    "buffer_load_dwordx4 v12, s[40:43], s74 offen lds\n"
    "buffer_load_dwordx4 v12, s[40:43], s78 offen offset:1024 lds\n"
    "s_waitcnt lgkmcnt(13)\n"
    "v_mfma_f32_32x32x16_bf16 v[160:175], v[212:215], v[128:131], v[160:175]\n"
    "ds_read_b64_tr_b16 v[236:237], v7 offset:45056\n"
    "ds_read_b64_tr_b16 v[238:239], v7 offset:45568\n"
    "s_lshl_b32 s72, s64, 7\n"
    "s_add_u32 m0, s66, 81920\n"
    "s_mul_i32 s73, s64, 172032\n"
    "buffer_load_dwordx4 v8, s[28:31], s72 offen lds\n"
    "s_add_u32 s72, s72, 6144\n"
    "buffer_load_dwordx4 v9, s[28:31], s72 offen offset:1024 lds\n"
    "s_waitcnt lgkmcnt(12)\n"
    "v_mfma_f32_32x32x16_bf16 v[144:159], v[216:219], v[132:135], v[144:159]\n"
    "v_add_u32 v0, 0xfffec000, v0\n"
    "v_add_u32 v1, 0xfffec000, v1\n"
    "v_add_u32 v2, 0xfffec000, v2\n"
    "v_add_u32 v3, 0xfffec000, v3\n"
    "v_add_u32 v4, 0xfffec000, v4\n"
    "s_add_u32 s72, s72, 6144\n"
    "buffer_load_dwordx4 v8, s[28:31], s72 offen offset:2048 lds\n"
    "s_add_u32 s72, s72, 6144\n"
    "buffer_load_dwordx4 v9, s[28:31], s72 offen offset:3072 lds\n"
    "s_waitcnt lgkmcnt(10)\n"
    "v_mfma_f32_32x32x16_bf16 v[160:175], v[220:223], v[132:135], v[160:175]\n"
    "v_add_u32 v5, 0xfffec000, v5\n"
    "v_add_u32 v6, 0xfffec000, v6\n"
    "v_add_u32 v7, 0xfffec000, v7\n"
    "s_waitcnt lgkmcnt(7)\n"
    "v_mfma_f32_32x32x16_bf16 v[144:159], v[224:227], v[136:139], v[144:159]\n"
    "s_waitcnt vmcnt(14) lgkmcnt(0)\n"
    "s_barrier\n"
    "ds_read_b128 v[64:67], v0 offset:0\n"
    "ds_read_b64_tr_b16 v[208:209], v4 offset:0\n"
    "ds_read_b64_tr_b16 v[210:211], v4 offset:512\n"
    "ds_read_b64_tr_b16 v[212:213], v5 offset:0\n"
    "ds_read_b64_tr_b16 v[214:215], v5 offset:512\n"
    "v_mfma_f32_16x16x32_bf16 v[240:243], v[224:227], v[244:247], v[240:243]\n"
    "s_add_u32 m0, s67, 98304\n"
    "s_mul_i32 s74, s64, s65\n"
    "buffer_load_dwordx4 v10, s[32:35], s73 offen lds\n"
    "v_mfma_f32_32x32x16_bf16 v[160:175], v[228:231], v[136:139], v[160:175]\n"
    "ds_read_b128 v[68:71], v1 offset:0\n"
    "ds_read_b64_tr_b16 v[216:217], v6 offset:0\n"
    "ds_read_b64_tr_b16 v[218:219], v6 offset:512\n"
    "ds_read_b64_tr_b16 v[220:221], v7 offset:0\n"
    "ds_read_b64_tr_b16 v[222:223], v7 offset:512\n"
    "s_add_u32 s73, s73, 20480\n"
    "buffer_load_dwordx4 v10, s[32:35], s73 offen offset:1024 lds\n"
    "s_add_u32 m0, s67, 106496\n"
    "s_add_u32 s78, s74, s76\n"
    "buffer_load_dwordx4 v11, s[36:39], s74 offen lds\n"
    "v_mfma_f32_32x32x16_bf16 v[144:159], v[232:235], v[140:143], v[144:159]\n"
    "ds_read_b128 v[72:75], v2 offset:0\n"
    "ds_read_b64_tr_b16 v[224:225], v4 offset:4096\n"
    "ds_read_b64_tr_b16 v[226:227], v4 offset:4608\n"
    "buffer_load_dwordx4 v11, s[36:39], s78 offen offset:1024 lds\n"
    "s_add_u32 m0, s67, 114688\n"
    "s_add_u32 s64, s64, 1\n"
    "s_cmp_eq_u32 s64, 7\n"
    "s_cselect_b32 s64, 0, s64\n"
    "buffer_load_dwordx4 v12, s[40:43], s74 offen lds\n"
    "v_mfma_f32_32x32x16_bf16 v[160:175], v[236:239], v[140:143], v[160:175]\n"
    "ds_read_b64_tr_b16 v[228:229], v5 offset:4096\n"
    "ds_read_b64_tr_b16 v[230:231], v5 offset:4608\n"
    "buffer_load_dwordx4 v12, s[40:43], s78 offen offset:1024 lds\n"
    "s_waitcnt lgkmcnt(12)\n"
    "v_mfma_f32_32x32x16_bf16 v[144:159], v[208:211], v[64:67], v[144:159]\n"
    "ds_read_b128 v[76:79], v3 offset:0\n"
    "ds_read_b64_tr_b16 v[232:233], v6 offset:4096\n"
    "ds_read_b64_tr_b16 v[234:235], v6 offset:4608\n"
    "v_mfma_f32_16x16x32_bf16 v[240:243], v[208:211], v[244:247], v[240:243]\n"
    "s_lshl_b32 s72, s64, 7\n"
    "s_add_u32 m0, s66, 122880\n"
    "s_mul_i32 s73, s64, 172032\n"
    "buffer_load_dwordx4 v8, s[28:31], s72 offen lds\n"
    "s_add_u32 s72, s72, 6144\n"
    "buffer_load_dwordx4 v9, s[28:31], s72 offen offset:1024 lds\n"
    "s_waitcnt lgkmcnt(13)\n"
    "v_mfma_f32_32x32x16_bf16 v[160:175], v[212:215], v[64:67], v[160:175]\n"
    "ds_read_b64_tr_b16 v[236:237], v7 offset:4096\n"
    "ds_read_b64_tr_b16 v[238:239], v7 offset:4608\n"
    "s_add_u32 s72, s72, 6144\n"
    "buffer_load_dwordx4 v8, s[28:31], s72 offen offset:2048 lds\n"
    "s_add_u32 s72, s72, 6144\n"
    "buffer_load_dwordx4 v9, s[28:31], s72 offen offset:3072 lds\n"
    "s_waitcnt lgkmcnt(12)\n"
    "v_mfma_f32_32x32x16_bf16 v[144:159], v[216:219], v[68:71], v[144:159]\n"
    "s_add_u32 m0, s67, 139264\n"
    "s_mul_i32 s74, s64, s65\n"
    "buffer_load_dwordx4 v10, s[32:35], s73 offen lds\n"
    "s_add_u32 s73, s73, 20480\n"
    "buffer_load_dwordx4 v10, s[32:35], s73 offen offset:1024 lds\n"
    "s_waitcnt lgkmcnt(10)\n"
    "v_mfma_f32_32x32x16_bf16 v[160:175], v[220:223], v[68:71], v[160:175]\n"
    "s_waitcnt lgkmcnt(7)\n"
    "v_mfma_f32_32x32x16_bf16 v[144:159], v[224:227], v[72:75], v[144:159]\n"
    "s_waitcnt vmcnt(16) lgkmcnt(0)\n"
    "s_barrier\n"
    "ds_read_b128 v[80:83], v0 offset:40960\n"
    "ds_read_b64_tr_b16 v[208:209], v4 offset:40960\n"
    "ds_read_b64_tr_b16 v[210:211], v4 offset:41472\n"
    "ds_read_b64_tr_b16 v[212:213], v5 offset:40960\n"
    "ds_read_b64_tr_b16 v[214:215], v5 offset:41472\n"
    "v_mfma_f32_16x16x32_bf16 v[240:243], v[224:227], v[244:247], v[240:243]\n"
    "s_add_u32 m0, s67, 147456\n"
    "s_add_u32 s78, s74, s76\n"
    "buffer_load_dwordx4 v11, s[36:39], s74 offen lds\n"
    "v_mfma_f32_32x32x16_bf16 v[160:175], v[228:231], v[72:75], v[160:175]\n"
    "ds_read_b128 v[40:43], v1 offset:40960\n"
    "ds_read_b64_tr_b16 v[216:217], v6 offset:40960\n"
    "ds_read_b64_tr_b16 v[218:219], v6 offset:41472\n"
    "ds_read_b64_tr_b16 v[220:221], v7 offset:40960\n"
    "ds_read_b64_tr_b16 v[222:223], v7 offset:41472\n"
    "buffer_load_dwordx4 v11, s[36:39], s78 offen offset:1024 lds\n"
    "s_add_u32 m0, s67, 155648\n"
    "s_add_u32 s64, s64, 1\n"
    "s_cmp_eq_u32 s64, 7\n"
    "s_cselect_b32 s64, 0, s64\n"
    "buffer_load_dwordx4 v12, s[40:43], s74 offen lds\n"
    "v_mfma_f32_32x32x16_bf16 v[144:159], v[232:235], v[76:79], v[144:159]\n"
    "ds_read_b128 v[84:87], v2 offset:40960\n"
    "ds_read_b64_tr_b16 v[224:225], v4 offset:45056\n"
    "ds_read_b64_tr_b16 v[226:227], v4 offset:45568\n"
    "buffer_load_dwordx4 v12, s[40:43], s78 offen offset:1024 lds\n"
    "s_mul_i32 s74, s64, 229376\n"
    "s_add_u32 m0, s67, 0\n"
    "s_add_u32 s78, s74, 27648\n"
    "buffer_load_dwordx4 v13, s[44:47], s74 offen lds\n"
    "v_mfma_f32_32x32x16_bf16 v[160:175], v[236:239], v[76:79], v[160:175]\n"
    "ds_read_b64_tr_b16 v[228:229], v5 offset:45056\n"
    "ds_read_b64_tr_b16 v[230:231], v5 offset:45568\n"
    "buffer_load_dwordx4 v13, s[44:47], s78 offen offset:1024 lds\n"
    "s_waitcnt lgkmcnt(12)\n"
    "v_mfma_f32_32x32x16_bf16 v[144:159], v[208:211], v[80:83], v[144:159]\n"
    "ds_read_b128 v[44:47], v3 offset:40960\n"
    "ds_read_b64_tr_b16 v[232:233], v6 offset:45056\n"
    "ds_read_b64_tr_b16 v[234:235], v6 offset:45568\n"
    "v_mfma_f32_16x16x32_bf16 v[240:243], v[208:211], v[244:247], v[240:243]\n"
    "s_add_u32 m0, s67, 8192\n"
    "s_add_u32 s64, s64, 1\n"
    "s_cmp_eq_u32 s64, 7\n"
    "s_cselect_b32 s64, 0, s64\n"
    "buffer_load_dwordx4 v14, s[48:51], s74 offen lds\n"
    "buffer_load_dwordx4 v14, s[48:51], s78 offen offset:1024 lds\n"
    "s_waitcnt lgkmcnt(13)\n"
    "v_mfma_f32_32x32x16_bf16 v[160:175], v[212:215], v[80:83], v[160:175]\n"
    "ds_read_b64_tr_b16 v[236:237], v7 offset:45056\n"
    "ds_read_b64_tr_b16 v[238:239], v7 offset:45568\n"
    "s_mul_i32 s74, s64, 229376\n"
    "s_add_u32 m0, s67, 16384\n"
    "s_add_u32 s78, s74, 27648\n"
    "buffer_load_dwordx4 v13, s[44:47], s74 offen lds\n"
    "buffer_load_dwordx4 v13, s[44:47], s78 offen offset:1024 lds\n"
    "s_waitcnt lgkmcnt(12)\n"
    "v_mfma_f32_32x32x16_bf16 v[144:159], v[216:219], v[40:43], v[144:159]\n"
    "v_add_u32 v0, 0x14000, v0\n"
    "v_add_u32 v1, 0x14000, v1\n"
    "v_add_u32 v2, 0x14000, v2\n"
    "v_add_u32 v3, 0x14000, v3\n"
    "v_add_u32 v4, 0x14000, v4\n"
    "s_add_u32 m0, s67, 24576\n"
    "s_add_u32 s64, s64, 1\n"
    "s_cmp_eq_u32 s64, 7\n"
    "s_cselect_b32 s64, 0, s64\n"
    "buffer_load_dwordx4 v14, s[48:51], s74 offen lds\n"
    "buffer_load_dwordx4 v14, s[48:51], s78 offen offset:1024 lds\n"
    "buffer_load_dwordx2 v[252:253], v25, s[24:27], 0 offen sc1\n"
    "s_waitcnt lgkmcnt(10)\n"
    "v_mfma_f32_32x32x16_bf16 v[160:175], v[220:223], v[40:43], v[160:175]\n"
    "v_add_u32 v5, 0x14000, v5\n"
    "v_add_u32 v6, 0x14000, v6\n"
    "v_add_u32 v7, 0x14000, v7\n"
    "s_waitcnt lgkmcnt(7)\n"
    "v_mfma_f32_32x32x16_bf16 v[144:159], v[224:227], v[84:87], v[144:159]\n"
    "s_waitcnt vmcnt(19) lgkmcnt(0)\n"
    "s_barrier\n"
    "ds_read_b128 v[88:91], v0 offset:0\n"
    "ds_read_b64_tr_b16 v[208:209], v4 offset:0\n"
    "ds_read_b64_tr_b16 v[210:211], v4 offset:512\n"
    "ds_read_b64_tr_b16 v[212:213], v5 offset:0\n"
    "ds_read_b64_tr_b16 v[214:215], v5 offset:512\n"
    "v_mfma_f32_16x16x32_bf16 v[240:243], v[224:227], v[244:247], v[240:243]\n"
    "s_mul_i32 s74, s64, 229376\n"
    "s_add_u32 m0, s67, 32768\n"
    "s_add_u32 s78, s74, 27648\n"
    "buffer_load_dwordx4 v13, s[44:47], s74 offen lds\n"
    "v_mfma_f32_32x32x16_bf16 v[160:175], v[228:231], v[84:87], v[160:175]\n"
    "ds_read_b128 v[48:51], v1 offset:0\n"
    "ds_read_b64_tr_b16 v[216:217], v6 offset:0\n"
    "ds_read_b64_tr_b16 v[218:219], v6 offset:512\n"
    "ds_read_b64_tr_b16 v[220:221], v7 offset:0\n"
    "ds_read_b64_tr_b16 v[222:223], v7 offset:512\n"
    "buffer_load_dwordx4 v13, s[44:47], s78 offen offset:1024 lds\n"
    "s_add_u32 m0, s67, 40960\n"
    "s_add_u32 s64, s64, 1\n"
    "s_cmp_eq_u32 s64, 7\n"
    "s_cselect_b32 s64, 0, s64\n"
    "buffer_load_dwordx4 v14, s[48:51], s74 offen lds\n"
    "v_mfma_f32_32x32x16_bf16 v[144:159], v[232:235], v[44:47], v[144:159]\n"
    "ds_read_b128 v[92:95], v2 offset:0\n"
    "ds_read_b64_tr_b16 v[224:225], v4 offset:4096\n"
    "ds_read_b64_tr_b16 v[226:227], v4 offset:4608\n"
    "buffer_load_dwordx4 v14, s[48:51], s78 offen offset:1024 lds\n"
    "s_mul_i32 s74, s64, 229376\n"
    "s_add_u32 m0, s67, 49152\n"
    "s_add_u32 s78, s74, 27648\n"
    "buffer_load_dwordx4 v13, s[44:47], s74 offen lds\n"
    "v_mfma_f32_32x32x16_bf16 v[160:175], v[236:239], v[44:47], v[160:175]\n"
    "ds_read_b64_tr_b16 v[228:229], v5 offset:4096\n"
    "ds_read_b64_tr_b16 v[230:231], v5 offset:4608\n"
    "buffer_load_dwordx4 v13, s[44:47], s78 offen offset:1024 lds\n"
    "s_waitcnt vmcnt(6)\n"
    "v_cmp_ne_u32 vcc, s69, v253\n"
    "s_mov_b32 s79, 0x40000\n"
    "s_cmp_eq_u64 vcc, 0\n"
    "s_cbranch_scc1 pok1_%=\n"
    "psp2_%=:\n"
    "buffer_load_dwordx2 v[252:253], v25, s[24:27], 0 offen sc1\n"
    "s_sub_u32 s79, s79, 1\n"
    "s_cbranch_scc1 pok1_%=\n"
    "s_waitcnt vmcnt(0)\n"
    "v_cmp_ne_u32 vcc, s69, v253\n"
    "s_sleep 1\n"
    "s_cmp_eq_u64 vcc, 0\n"
    "s_cbranch_scc0 psp2_%=\n"
    "pok1_%=:\n"
    "s_waitcnt lgkmcnt(12)\n"
    "v_mfma_f32_32x32x16_bf16 v[144:159], v[208:211], v[88:91], v[144:159]\n"
    "ds_read_b128 v[52:55], v3 offset:0\n"
    "ds_read_b64_tr_b16 v[232:233], v6 offset:4096\n"
    "ds_read_b64_tr_b16 v[234:235], v6 offset:4608\n"
    "v_mfma_f32_16x16x32_bf16 v[240:243], v[208:211], v[244:247], v[240:243]\n"
    "s_add_u32 m0, s67, 57344\n"
    "s_add_u32 s64, s64, 1\n"
    "s_cmp_eq_u32 s64, 7\n"
    "s_cselect_b32 s64, 0, s64\n"
    "buffer_load_dwordx4 v14, s[48:51], s74 offen lds\n"
    "buffer_load_dwordx4 v14, s[48:51], s78 offen offset:1024 lds\n"
    "v_and_b32 v254, s77, v252\n"
    "v_and_b32 v248, 0xffff, v252\n"
    "v_rcp_f32 v255, v254\n"
    "s_waitcnt lgkmcnt(13)\n"
    "v_mfma_f32_32x32x16_bf16 v[160:175], v[212:215], v[88:91], v[160:175]\n"
    "ds_read_b64_tr_b16 v[236:237], v7 offset:4096\n"
    "ds_read_b64_tr_b16 v[238:239], v7 offset:4608\n"
    "s_mul_i32 s74, s64, 229376\n"
    "s_add_u32 m0, s67, 65536\n"
    "s_add_u32 s78, s74, 27648\n"
    "buffer_load_dwordx4 v13, s[44:47], s74 offen lds\n"
    "buffer_load_dwordx4 v13, s[44:47], s78 offen offset:1024 lds\n"
    "v_lshl_or_b32 v248, v252, 16, v248\n"
    "v_mul_f32 v254, v254, v254\n"
    "v_mov_b32 v249, v248\n"
    "s_waitcnt lgkmcnt(12)\n"
    "v_mfma_f32_32x32x16_bf16 v[144:159], v[216:219], v[48:51], v[144:159]\n"
    "s_add_u32 m0, s67, 73728\n"
    "s_add_u32 s64, s64, 1\n"
    "s_cmp_eq_u32 s64, 7\n"
    "s_cselect_b32 s64, 0, s64\n"
    "buffer_load_dwordx4 v14, s[48:51], s74 offen lds\n"
    "buffer_load_dwordx4 v14, s[48:51], s78 offen offset:1024 lds\n"
    "v_mul_f32 v252, 0xc0135761, v255\n"
    "v_mul_f32 v253, v255, v255\n"
    "v_mul_f32 v253, v253, v252\n"
    "v_mul_f32 v253, 0x3d372713, v253\n"
    "s_waitcnt lgkmcnt(10)\n"
    "v_mfma_f32_32x32x16_bf16 v[160:175], v[220:223], v[48:51], v[160:175]\n"
    "s_waitcnt lgkmcnt(7)\n"
    "v_mfma_f32_32x32x16_bf16 v[144:159], v[224:227], v[92:95], v[144:159]\n"
    "s_waitcnt lgkmcnt(0)\n"
    "s_barrier\n"
    "ds_read_b128 v[32:35], v0 offset:40960\n"
    "ds_read_b64_tr_b16 v[208:209], v4 offset:40960\n"
    "ds_read_b64_tr_b16 v[210:211], v4 offset:41472\n"
    "ds_read_b128 v[36:39], v2 offset:40960\n"
    "ds_read_b64_tr_b16 v[212:213], v4 offset:45056\n"
    "ds_read_b64_tr_b16 v[214:215], v4 offset:45568\n"
    "v_mfma_f32_16x16x32_bf16 v[240:243], v[224:227], v[244:247], v[240:243]\n"
    "s_mul_i32 s74, s64, 229376\n"
    "s_add_u32 m0, s67, 81920\n"
    "s_add_u32 s78, s74, 27648\n"
    "buffer_load_dwordx4 v13, s[44:47], s74 offen lds\n"
    "v_mfma_f32_32x32x16_bf16 v[160:175], v[228:231], v[92:95], v[160:175]\n"
    "ds_read_b64_tr_b16 v[216:217], v5 offset:40960\n"
    "ds_read_b64_tr_b16 v[218:219], v5 offset:41472\n"
    "ds_read_b64_tr_b16 v[220:221], v5 offset:45056\n"
    "ds_read_b64_tr_b16 v[222:223], v5 offset:45568\n"
    "buffer_load_dwordx4 v13, s[44:47], s78 offen offset:1024 lds\n"
    "v_mfma_f32_32x32x16_bf16 v[144:159], v[232:235], v[52:55], v[144:159]\n"
    "ds_read_b128 v[56:59], v1 offset:40960\n"
    "ds_read_b64_tr_b16 v[224:225], v6 offset:40960\n"
    "ds_read_b64_tr_b16 v[226:227], v6 offset:41472\n"
    "s_add_u32 m0, s67, 90112\n"
    "s_add_u32 s64, s64, 1\n"
    "s_cmp_eq_u32 s64, 7\n"
    "s_cselect_b32 s64, 0, s64\n"
    "buffer_load_dwordx4 v14, s[48:51], s74 offen lds\n"
    "buffer_load_dwordx4 v14, s[48:51], s78 offen offset:1024 lds\n"
    "v_mul_f32 v192, s58, v255\n"
    "v_mfma_f32_32x32x16_bf16 v[160:175], v[236:239], v[52:55], v[160:175]\n"
    "ds_read_b64_tr_b16 v[228:229], v7 offset:40960\n"
    "ds_read_b64_tr_b16 v[230:231], v7 offset:41472\n"
    "s_mul_i32 s74, s64, 229376\n"
    "s_add_u32 m0, s67, 98304\n"
    "s_add_u32 s78, s74, 27648\n"
    "buffer_load_dwordx4 v13, s[44:47], s74 offen lds\n"
    "s_waitcnt lgkmcnt(12)\n"
    "v_mfma_f32_32x32x16_bf16 v[144:159], v[208:211], v[32:35], v[144:159]\n"
    "ds_read_b128 v[60:63], v3 offset:40960\n"
    "ds_read_b64_tr_b16 v[232:233], v6 offset:45056\n"
    "ds_read_b64_tr_b16 v[234:235], v6 offset:45568\n"
    "v_mfma_f32_16x16x32_bf16 v[240:243], v[208:211], v[244:247], v[240:243]\n"
    "buffer_load_dwordx4 v13, s[44:47], s78 offen offset:1024 lds\n"
    "v_xor_b32 v193, 2048, v26\n"
    "s_waitcnt lgkmcnt(12)\n"
    "v_mfma_f32_32x32x16_bf16 v[144:159], v[212:215], v[36:39], v[144:159]\n"
    "ds_read_b64_tr_b16 v[236:237], v7 offset:45056\n"
    "ds_read_b64_tr_b16 v[238:239], v7 offset:45568\n"
    "v_mfma_f32_16x16x32_bf16 v[240:243], v[212:215], v[244:247], v[240:243]\n"
    "s_add_u32 m0, s67, 106496\n"
    "s_add_u32 s64, s64, 1\n"
    "s_cmp_eq_u32 s64, 7\n"
    "s_cselect_b32 s64, 0, s64\n"
    "buffer_load_dwordx4 v14, s[48:51], s74 offen lds\n"
    "buffer_load_dwordx4 v14, s[48:51], s78 offen offset:1024 lds\n"
    "s_waitcnt lgkmcnt(12)\n"
    "v_mfma_f32_32x32x16_bf16 v[160:175], v[216:219], v[32:35], v[160:175]\n"
    "s_waitcnt lgkmcnt(10)\n"
    "v_mfma_f32_32x32x16_bf16 v[160:175], v[220:223], v[36:39], v[160:175]\n"
    "v_cndmask_b32 v240, v240, v241, s[80:81]\n"
    "v_cndmask_b32 v242, v242, v243, s[80:81]\n"
    "v_cndmask_b32 v240, v240, v242, s[82:83]\n"
    "v_and_b32 v241, s77, v240\n"
    "v_sub_f32 v241, v240, v241\n"
    "s_waitcnt lgkmcnt(7)\n"
    "v_mfma_f32_32x32x16_bf16 v[144:159], v[224:227], v[56:59], v[144:159]\n"
    "v_and_b32 v242, s77, v241\n"
    "v_sub_f32 v243, v241, v242\n"
    "v_lshrrev_b32 v240, 16, v240\n"
    "v_or_b32 v242, v240, v242\n"
    "v_lshrrev_b32 v243, 16, v243\n"
    "ds_write_b64 v20, v[242:243]\n"
    "s_waitcnt lgkmcnt(6)\n"
    "v_mfma_f32_32x32x16_bf16 v[160:175], v[228:231], v[56:59], v[160:175]\n"
    "s_waitcnt lgkmcnt(3)\n"
    "v_mfma_f32_32x32x16_bf16 v[144:159], v[232:235], v[60:63], v[144:159]\n"
    "s_waitcnt lgkmcnt(1)\n"
    "v_mfma_f32_32x32x16_bf16 v[160:175], v[236:239], v[60:63], v[160:175]\n"
    "s_waitcnt vmcnt(8) lgkmcnt(0)\n"
    "s_barrier\n"
    "ds_read_b64 v[208:209], v22\n"
    "v_xor_b32 v1, 512, v22\n"
    "ds_read_b64 v[212:213], v1\n"
    "s_waitcnt lgkmcnt(1)\n"
    "v_mfma_f32_32x32x16_bf16 v[144:159], v[208:211], v[248:251], v[144:159]\n"
    "s_waitcnt lgkmcnt(0)\n"
    "v_mfma_f32_32x32x16_bf16 v[160:175], v[212:215], v[248:251], v[160:175]\n"
    "s_nop 9\n"
    "v_mul_f32 v144, v192, v144\n"
    "v_mul_f32 v145, v192, v145\n"
    "v_mul_f32 v146, v192, v146\n"
    "v_mul_f32 v147, v192, v147\n"
    "v_mul_f32 v148, v192, v148\n"
    "v_mul_f32 v149, v192, v149\n"
    "v_mul_f32 v150, v192, v150\n"
    "v_mul_f32 v151, v192, v151\n"
    "v_cvt_pk_bf16_f32 v224, v144, v145\n"
    "v_cvt_pk_bf16_f32 v225, v146, v147\n"
    "v_cvt_pk_bf16_f32 v226, v148, v149\n"
    "v_cvt_pk_bf16_f32 v227, v150, v151\n"
    "buffer_store_dwordx4 v[224:227], v26, s[24:27], s56 offen sc1\n"
    "v_mul_f32 v152, v192, v152\n"
    "v_mul_f32 v153, v192, v153\n"
    "v_mul_f32 v154, v192, v154\n"
    "v_mul_f32 v155, v192, v155\n"
    "v_mul_f32 v156, v192, v156\n"
    "v_mul_f32 v157, v192, v157\n"
    "v_mul_f32 v158, v192, v158\n"
    "v_mul_f32 v159, v192, v159\n"
    "v_cvt_pk_bf16_f32 v228, v152, v153\n"
    "v_cvt_pk_bf16_f32 v229, v154, v155\n"
    "v_cvt_pk_bf16_f32 v230, v156, v157\n"
    "v_cvt_pk_bf16_f32 v231, v158, v159\n"
    "buffer_store_dwordx4 v[228:231], v26, s[24:27], s56 offen offset:1024 sc1\n"
    "v_mul_f32 v160, v192, v160\n"
    "v_mul_f32 v161, v192, v161\n"
    "v_mul_f32 v162, v192, v162\n"
    "v_mul_f32 v163, v192, v163\n"
    "v_mul_f32 v164, v192, v164\n"
    "v_mul_f32 v165, v192, v165\n"
    "v_mul_f32 v166, v192, v166\n"
    "v_mul_f32 v167, v192, v167\n"
    "v_cvt_pk_bf16_f32 v232, v160, v161\n"
    "v_cvt_pk_bf16_f32 v233, v162, v163\n"
    "v_cvt_pk_bf16_f32 v234, v164, v165\n"
    "v_cvt_pk_bf16_f32 v235, v166, v167\n"
    "buffer_store_dwordx4 v[232:235], v193, s[24:27], s56 offen sc1\n"
    "v_mul_f32 v168, v192, v168\n"
    "v_mul_f32 v169, v192, v169\n"
    "v_mul_f32 v170, v192, v170\n"
    "v_mul_f32 v171, v192, v171\n"
    "v_mul_f32 v172, v192, v172\n"
    "v_mul_f32 v173, v192, v173\n"
    "v_mul_f32 v174, v192, v174\n"
    "v_mul_f32 v175, v192, v175\n"
    "v_cvt_pk_bf16_f32 v236, v168, v169\n"
    "v_cvt_pk_bf16_f32 v237, v170, v171\n"
    "v_cvt_pk_bf16_f32 v238, v172, v173\n"
    "v_cvt_pk_bf16_f32 v239, v174, v175\n"
    "buffer_store_dwordx4 v[236:239], v193, s[24:27], s56 offen offset:1024 sc1\n"
    "s_branch a2_%=\n"
    "av_%=:\n"
    "s_lshl1_add_u32 s4, s22, s12\n"
    "s_lshl_b32 s4, s4, 9\n"
    "s_movk_i32 s6, 1600\n"
    "v_add_u32 v73, s4, v69\n"
    "v_mul_u32_u24 v73, 2341, v73\n"
    "v_lshrrev_b32 v73, 20, v73\n"
    "s_add_u32 s5, s4, 57344\n"
    "v_add_u32 v74, s5, v15\n"
    "v_mad_u32_u24 v28, v73, s6, v74\n"
    "s_xor_b32 s4, s4, 512\n"
    "v_add_u32 v73, s4, v69\n"
    "v_mul_u32_u24 v73, 2341, v73\n"
    "v_lshrrev_b32 v73, 20, v73\n"
    "s_add_u32 s5, s4, 57344\n"
    "v_add_u32 v74, s5, v15\n"
    "v_mad_u32_u24 v29, v73, s6, v74\n"
    "s_waitcnt vmcnt(13)\n"
    "s_barrier\n"
    "ds_read_b128 v[96:99], v0 offset:40960\n"
    "ds_read_b64_tr_b16 v[208:209], v4 offset:40960\n"
    "ds_read_b64_tr_b16 v[210:211], v4 offset:41472\n"
    "ds_read_b64_tr_b16 v[212:213], v5 offset:40960\n"
    "ds_read_b64_tr_b16 v[214:215], v5 offset:41472\n"
    "ds_read_b128 v[100:103], v1 offset:40960\n"
    "ds_read_b64_tr_b16 v[216:217], v6 offset:40960\n"
    "ds_read_b64_tr_b16 v[218:219], v6 offset:41472\n"
    "ds_read_b64_tr_b16 v[220:221], v7 offset:40960\n"
    "ds_read_b64_tr_b16 v[222:223], v7 offset:41472\n"
    "s_waitcnt lgkmcnt(7)\n"
    "v_mfma_f32_32x32x16_bf16 v[144:159], v[96:99], v[208:211], 0\n"
    "ds_read_b128 v[104:107], v2 offset:40960\n"
    "ds_read_b64_tr_b16 v[224:225], v4 offset:45056\n"
    "ds_read_b64_tr_b16 v[226:227], v4 offset:45568\n"
    "ds_read_b64_tr_b16 v[228:229], v5 offset:45056\n"
    "ds_read_b64_tr_b16 v[230:231], v5 offset:45568\n"
    "v_mfma_f32_16x16x32_bf16 v[240:243], v[208:211], v[244:247], 0\n"
    "s_lshl_b32 s72, s64, 7\n"
    "s_add_u32 m0, s66, 122880\n"
    "s_mul_i32 s73, s64, 172032\n"
    "buffer_load_dwordx4 v8, s[28:31], s72 offen lds\n"
    "s_add_u32 s72, s72, 6144\n"
    "buffer_load_dwordx4 v9, s[28:31], s72 offen offset:1024 lds\n"
    "s_waitcnt vmcnt(14)\n"
    "v_lshlrev_b32 v44, 16, v32\n"
    "v_and_b32 v45, s77, v32\n"
    "v_mov_b32 v40, v44\n"
    "v_mul_f32 v41, v44, v44\n"
    "v_mov_b32 v42, v45\n"
    "v_mul_f32 v43, v45, v45\n"
    "v_lshlrev_b32 v44, 16, v33\n"
    "v_and_b32 v45, s77, v33\n"
    "v_add_f32 v40, v40, v44\n"
    "v_fmac_f32 v41, v44, v44\n"
    "v_add_f32 v42, v42, v45\n"
    "v_fmac_f32 v43, v45, v45\n"
    "v_lshlrev_b32 v44, 16, v34\n"
    "v_and_b32 v45, s77, v34\n"
    "v_add_f32 v40, v40, v44\n"
    "v_fmac_f32 v41, v44, v44\n"
    "v_add_f32 v42, v42, v45\n"
    "v_fmac_f32 v43, v45, v45\n"
    "s_waitcnt lgkmcnt(10)\n"
    "v_mfma_f32_32x32x16_bf16 v[160:175], v[96:99], v[212:215], 0\n"
    "ds_read_b128 v[108:111], v3 offset:40960\n"
    "ds_read_b64_tr_b16 v[232:233], v6 offset:45056\n"
    "ds_read_b64_tr_b16 v[234:235], v6 offset:45568\n"
    "ds_read_b64_tr_b16 v[236:237], v7 offset:45056\n"
    "ds_read_b64_tr_b16 v[238:239], v7 offset:45568\n"
    "s_add_u32 s72, s72, 6144\n"
    "buffer_load_dwordx4 v8, s[28:31], s72 offen offset:2048 lds\n"
    "s_add_u32 s72, s72, 6144\n"
    "buffer_load_dwordx4 v9, s[28:31], s72 offen offset:3072 lds\n"
    "s_add_u32 m0, s67, 139264\n"
    "s_mul_i32 s74, s64, s65\n"
    "buffer_load_dwordx4 v10, s[32:35], s73 offen lds\n"
    "v_lshlrev_b32 v44, 16, v35\n"
    "v_and_b32 v45, s77, v35\n"
    "v_add_f32 v40, v40, v44\n"
    "v_fmac_f32 v41, v44, v44\n"
    "v_add_f32 v42, v42, v45\n"
    "v_fmac_f32 v43, v45, v45\n"
    "s_waitcnt vmcnt(16)\n"
    "v_lshlrev_b32 v44, 16, v36\n"
    "v_and_b32 v45, s77, v36\n"
    "v_add_f32 v40, v40, v44\n"
    "v_fmac_f32 v41, v44, v44\n"
    "v_add_f32 v42, v42, v45\n"
    "v_fmac_f32 v43, v45, v45\n"
    "v_lshlrev_b32 v44, 16, v37\n"
    "v_and_b32 v45, s77, v37\n"
    "v_add_f32 v40, v40, v44\n"
    "v_fmac_f32 v41, v44, v44\n"
    "v_add_f32 v42, v42, v45\n"
    "v_fmac_f32 v43, v45, v45\n"
    "s_waitcnt lgkmcnt(12)\n"
    "v_mfma_f32_32x32x16_bf16 v[144:159], v[100:103], v[216:219], v[144:159]\n"
    "v_add_u32 v0, 0x14000, v0\n"
    "v_add_u32 v1, 0x14000, v1\n"
    "v_add_u32 v2, 0x14000, v2\n"
    "v_add_u32 v3, 0x14000, v3\n"
    "v_add_u32 v4, 0x14000, v4\n"
    "s_add_u32 s73, s73, 20480\n"
    "buffer_load_dwordx4 v10, s[32:35], s73 offen offset:1024 lds\n"
    "s_add_u32 m0, s67, 147456\n"
    "s_add_u32 s78, s74, s76\n"
    "buffer_load_dwordx4 v11, s[36:39], s74 offen lds\n"
    "v_lshlrev_b32 v44, 16, v38\n"
    "v_and_b32 v45, s77, v38\n"
    "v_add_f32 v40, v40, v44\n"
    "v_fmac_f32 v41, v44, v44\n"
    "v_add_f32 v42, v42, v45\n"
    "v_fmac_f32 v43, v45, v45\n"
    "v_lshlrev_b32 v44, 16, v39\n"
    "v_and_b32 v45, s77, v39\n"
    "v_add_f32 v40, v40, v44\n"
    "v_fmac_f32 v41, v44, v44\n"
    "v_add_f32 v42, v42, v45\n"
    "v_fmac_f32 v43, v45, v45\n"
    "v_add_f32 v40, v40, v42\n"
    "v_add_f32 v41, v41, v43\n"
    "s_waitcnt vmcnt(17)\n"
    "v_readfirstlane_b32 s69, v48\n"
    "v_add_f32_dpp v40, v40, v40 row_shr:1 row_mask:0xf bank_mask:0xf\n"
    "v_add_f32_dpp v41, v41, v41 row_shr:1 row_mask:0xf bank_mask:0xf\n"
    "s_nop 0\n"
    "v_add_f32_dpp v40, v40, v40 row_shr:2 row_mask:0xf bank_mask:0xf\n"
    "s_waitcnt lgkmcnt(10)\n"
    "v_mfma_f32_32x32x16_bf16 v[160:175], v[100:103], v[220:223], v[160:175]\n"
    "v_add_u32 v5, 0x14000, v5\n"
    "v_add_u32 v6, 0x14000, v6\n"
    "v_add_u32 v7, 0x14000, v7\n"
    "buffer_load_dwordx4 v11, s[36:39], s78 offen offset:1024 lds\n"
    "s_add_u32 m0, s67, 155648\n"
    "s_add_u32 s64, s64, 1\n"
    "s_cmp_eq_u32 s64, 7\n"
    "s_cselect_b32 s64, 0, s64\n"
    "buffer_load_dwordx4 v12, s[40:43], s74 offen lds\n"
    "buffer_load_dwordx4 v12, s[40:43], s78 offen offset:1024 lds\n"
    "v_add_f32_dpp v41, v41, v41 row_shr:2 row_mask:0xf bank_mask:0xf\n"
    "v_add_f32_dpp v40, v40, v40 row_shr:4 row_mask:0xf bank_mask:0xf\n"
    "s_nop 0\n"
    "v_add_f32_dpp v41, v41, v41 row_shr:4 row_mask:0xf bank_mask:0xf\n"
    "v_add_f32_dpp v40, v40, v40 row_shr:8 row_mask:0xf bank_mask:0xf\n"
    "s_nop 0\n"
    "v_add_f32_dpp v41, v41, v41 row_shr:8 row_mask:0xf bank_mask:0xf\n"
    "v_add_f32_dpp v40, v40, v40 row_bcast:15 row_mask:0xa bank_mask:0xf\n"
    "s_nop 0\n"
    "v_add_f32_dpp v41, v41, v41 row_bcast:15 row_mask:0xa bank_mask:0xf\n"
    "s_add_u32 s69, s69, 1\n"
    "v_mul_f32 v40, 0x3b124925, v40\n"
    "v_mul_f32 v41, 0x3b124925, v41\n"
    "v_fma_f32 v41, -v40, v40, v41\n"
    "v_max_f32 v41, 0, v41\n"
    "v_cvt_pk_bf16_f32 v41, 0, v41\n"
    "v_add_f32 v41, 0x3727c5ac, v41\n"
    "v_cvt_pk_bf16_f32 v41, 0, v41\n"
    "v_sqrt_f32 v41, v41\n"
    "v_mov_b32 v43, s69\n"
    "v_cvt_pk_bf16_f32 v42, -v40, v41\n"
    "buffer_store_dwordx2 v[42:43], v24, s[24:27], 0 offen sc1\n"
    "s_waitcnt lgkmcnt(7)\n"
    "v_mfma_f32_32x32x16_bf16 v[144:159], v[104:107], v[224:227], v[144:159]\n"
    "s_waitcnt vmcnt(11) lgkmcnt(0)\n"
    "s_barrier\n"
    "ds_read_b128 v[112:115], v0 offset:0\n"
    "ds_read_b64_tr_b16 v[208:209], v4 offset:0\n"
    "ds_read_b64_tr_b16 v[210:211], v4 offset:512\n"
    "ds_read_b64_tr_b16 v[212:213], v5 offset:0\n"
    "ds_read_b64_tr_b16 v[214:215], v5 offset:512\n"
    "v_mfma_f32_16x16x32_bf16 v[240:243], v[224:227], v[244:247], v[240:243]\n"
    "s_lshl_b32 s72, s64, 7\n"
    "s_add_u32 m0, s66, 0\n"
    "s_mul_i32 s73, s64, 172032\n"
    "buffer_load_dwordx4 v8, s[28:31], s72 offen lds\n"
    "v_mfma_f32_32x32x16_bf16 v[160:175], v[104:107], v[228:231], v[160:175]\n"
    "ds_read_b128 v[116:119], v1 offset:0\n"
    "ds_read_b64_tr_b16 v[216:217], v6 offset:0\n"
    "ds_read_b64_tr_b16 v[218:219], v6 offset:512\n"
    "ds_read_b64_tr_b16 v[220:221], v7 offset:0\n"
    "ds_read_b64_tr_b16 v[222:223], v7 offset:512\n"
    "s_add_u32 s72, s72, 6144\n"
    "buffer_load_dwordx4 v9, s[28:31], s72 offen offset:1024 lds\n"
    "s_add_u32 s72, s72, 6144\n"
    "buffer_load_dwordx4 v8, s[28:31], s72 offen offset:2048 lds\n"
    "v_mfma_f32_32x32x16_bf16 v[144:159], v[108:111], v[232:235], v[144:159]\n"
    "ds_read_b128 v[120:123], v2 offset:0\n"
    "ds_read_b64_tr_b16 v[224:225], v4 offset:4096\n"
    "ds_read_b64_tr_b16 v[226:227], v4 offset:4608\n"
    "s_add_u32 s72, s72, 6144\n"
    "buffer_load_dwordx4 v9, s[28:31], s72 offen offset:3072 lds\n"
    "s_add_u32 m0, s67, 16384\n"
    "s_mul_i32 s74, s64, s65\n"
    "buffer_load_dwordx4 v10, s[32:35], s73 offen lds\n"
    "v_mfma_f32_32x32x16_bf16 v[160:175], v[108:111], v[236:239], v[160:175]\n"
    "ds_read_b64_tr_b16 v[228:229], v5 offset:4096\n"
    "ds_read_b64_tr_b16 v[230:231], v5 offset:4608\n"
    "s_add_u32 s73, s73, 20480\n"
    "buffer_load_dwordx4 v10, s[32:35], s73 offen offset:1024 lds\n"
    "s_waitcnt lgkmcnt(12)\n"
    "v_mfma_f32_32x32x16_bf16 v[144:159], v[112:115], v[208:211], v[144:159]\n"
    "ds_read_b128 v[124:127], v3 offset:0\n"
    "ds_read_b64_tr_b16 v[232:233], v6 offset:4096\n"
    "ds_read_b64_tr_b16 v[234:235], v6 offset:4608\n"
    "v_mfma_f32_16x16x32_bf16 v[240:243], v[208:211], v[244:247], v[240:243]\n"
    "s_add_u32 m0, s67, 24576\n"
    "s_add_u32 s78, s74, s76\n"
    "buffer_load_dwordx4 v11, s[36:39], s74 offen lds\n"
    "buffer_load_dwordx4 v11, s[36:39], s78 offen offset:1024 lds\n"
    "s_waitcnt lgkmcnt(13)\n"
    "v_mfma_f32_32x32x16_bf16 v[160:175], v[112:115], v[212:215], v[160:175]\n"
    "ds_read_b64_tr_b16 v[236:237], v7 offset:4096\n"
    "ds_read_b64_tr_b16 v[238:239], v7 offset:4608\n"
    "s_add_u32 m0, s67, 32768\n"
    "s_add_u32 s64, s64, 1\n"
    "s_cmp_eq_u32 s64, 7\n"
    "s_cselect_b32 s64, 0, s64\n"
    "buffer_load_dwordx4 v12, s[40:43], s74 offen lds\n"
    "buffer_load_dwordx4 v12, s[40:43], s78 offen offset:1024 lds\n"
    "s_waitcnt lgkmcnt(12)\n"
    "v_mfma_f32_32x32x16_bf16 v[144:159], v[116:119], v[216:219], v[144:159]\n"
    "s_lshl_b32 s72, s64, 7\n"
    "s_add_u32 m0, s66, 40960\n"
    "s_mul_i32 s73, s64, 172032\n"
    "buffer_load_dwordx4 v8, s[28:31], s72 offen lds\n"
    "s_add_u32 s72, s72, 6144\n"
    "buffer_load_dwordx4 v9, s[28:31], s72 offen offset:1024 lds\n"
    "s_waitcnt lgkmcnt(10)\n"
    "v_mfma_f32_32x32x16_bf16 v[160:175], v[116:119], v[220:223], v[160:175]\n"
    "s_waitcnt lgkmcnt(7)\n"
    "v_mfma_f32_32x32x16_bf16 v[144:159], v[120:123], v[224:227], v[144:159]\n"
    "s_waitcnt vmcnt(13) lgkmcnt(0)\n"
    "s_barrier\n"
    "ds_read_b128 v[128:131], v0 offset:40960\n"
    "ds_read_b64_tr_b16 v[208:209], v4 offset:40960\n"
    "ds_read_b64_tr_b16 v[210:211], v4 offset:41472\n"
    "ds_read_b64_tr_b16 v[212:213], v5 offset:40960\n"
    "ds_read_b64_tr_b16 v[214:215], v5 offset:41472\n"
    "v_mfma_f32_16x16x32_bf16 v[240:243], v[224:227], v[244:247], v[240:243]\n"
    "s_add_u32 s72, s72, 6144\n"
    "buffer_load_dwordx4 v8, s[28:31], s72 offen offset:2048 lds\n"
    "v_mfma_f32_32x32x16_bf16 v[160:175], v[120:123], v[228:231], v[160:175]\n"
    "ds_read_b128 v[132:135], v1 offset:40960\n"
    "ds_read_b64_tr_b16 v[216:217], v6 offset:40960\n"
    "ds_read_b64_tr_b16 v[218:219], v6 offset:41472\n"
    "ds_read_b64_tr_b16 v[220:221], v7 offset:40960\n"
    "ds_read_b64_tr_b16 v[222:223], v7 offset:41472\n"
    "s_add_u32 s72, s72, 6144\n"
    "buffer_load_dwordx4 v9, s[28:31], s72 offen offset:3072 lds\n"
    "s_add_u32 m0, s67, 57344\n"
    "s_mul_i32 s74, s64, s65\n"
    "buffer_load_dwordx4 v10, s[32:35], s73 offen lds\n"
    "v_mfma_f32_32x32x16_bf16 v[144:159], v[124:127], v[232:235], v[144:159]\n"
    "ds_read_b128 v[136:139], v2 offset:40960\n"
    "ds_read_b64_tr_b16 v[224:225], v4 offset:45056\n"
    "ds_read_b64_tr_b16 v[226:227], v4 offset:45568\n"
    "s_add_u32 s73, s73, 20480\n"
    "buffer_load_dwordx4 v10, s[32:35], s73 offen offset:1024 lds\n"
    "s_add_u32 m0, s67, 65536\n"
    "s_add_u32 s78, s74, s76\n"
    "buffer_load_dwordx4 v11, s[36:39], s74 offen lds\n"
    "v_mfma_f32_32x32x16_bf16 v[160:175], v[124:127], v[236:239], v[160:175]\n"
    "ds_read_b64_tr_b16 v[228:229], v5 offset:45056\n"
    "ds_read_b64_tr_b16 v[230:231], v5 offset:45568\n"
    "buffer_load_dwordx4 v11, s[36:39], s78 offen offset:1024 lds\n"
    "s_waitcnt lgkmcnt(12)\n"
    "v_mfma_f32_32x32x16_bf16 v[144:159], v[128:131], v[208:211], v[144:159]\n"
    "ds_read_b128 v[140:143], v3 offset:40960\n"
    "ds_read_b64_tr_b16 v[232:233], v6 offset:45056\n"
    "ds_read_b64_tr_b16 v[234:235], v6 offset:45568\n"
    "v_mfma_f32_16x16x32_bf16 v[240:243], v[208:211], v[244:247], v[240:243]\n"
    "s_add_u32 m0, s67, 73728\n"
    "s_add_u32 s64, s64, 1\n"
    "s_cmp_eq_u32 s64, 7\n"
    "s_cselect_b32 s64, 0, s64\n"
    "buffer_load_dwordx4 v12, s[40:43], s74 offen lds\n"
    "buffer_load_dwordx4 v12, s[40:43], s78 offen offset:1024 lds\n"
    "s_waitcnt lgkmcnt(13)\n"
    "v_mfma_f32_32x32x16_bf16 v[160:175], v[128:131], v[212:215], v[160:175]\n"
    "ds_read_b64_tr_b16 v[236:237], v7 offset:45056\n"
    "ds_read_b64_tr_b16 v[238:239], v7 offset:45568\n"
    "s_lshl_b32 s72, s64, 7\n"
    "s_add_u32 m0, s66, 81920\n"
    "s_mul_i32 s73, s64, 172032\n"
    "buffer_load_dwordx4 v8, s[28:31], s72 offen lds\n"
    "s_add_u32 s72, s72, 6144\n"
    "buffer_load_dwordx4 v9, s[28:31], s72 offen offset:1024 lds\n"
    "s_waitcnt lgkmcnt(12)\n"
    "v_mfma_f32_32x32x16_bf16 v[144:159], v[132:135], v[216:219], v[144:159]\n"
    "v_add_u32 v0, 0xfffec000, v0\n"
    "v_add_u32 v1, 0xfffec000, v1\n"
    "v_add_u32 v2, 0xfffec000, v2\n"
    "v_add_u32 v3, 0xfffec000, v3\n"
    "v_add_u32 v4, 0xfffec000, v4\n"
    "s_add_u32 s72, s72, 6144\n"
    "buffer_load_dwordx4 v8, s[28:31], s72 offen offset:2048 lds\n"
    "s_add_u32 s72, s72, 6144\n"
    "buffer_load_dwordx4 v9, s[28:31], s72 offen offset:3072 lds\n"
    "s_waitcnt lgkmcnt(10)\n"
    "v_mfma_f32_32x32x16_bf16 v[160:175], v[132:135], v[220:223], v[160:175]\n"
    "v_add_u32 v5, 0xfffec000, v5\n"
    "v_add_u32 v6, 0xfffec000, v6\n"
    "v_add_u32 v7, 0xfffec000, v7\n"
    "s_waitcnt lgkmcnt(7)\n"
    "v_mfma_f32_32x32x16_bf16 v[144:159], v[136:139], v[224:227], v[144:159]\n"
    "s_waitcnt vmcnt(14) lgkmcnt(0)\n"
    "s_barrier\n"
    "ds_read_b128 v[64:67], v0 offset:0\n"
    "ds_read_b64_tr_b16 v[208:209], v4 offset:0\n"
    "ds_read_b64_tr_b16 v[210:211], v4 offset:512\n"
    "ds_read_b64_tr_b16 v[212:213], v5 offset:0\n"
    "ds_read_b64_tr_b16 v[214:215], v5 offset:512\n"
    "v_mfma_f32_16x16x32_bf16 v[240:243], v[224:227], v[244:247], v[240:243]\n"
    "s_add_u32 m0, s67, 98304\n"
    "s_mul_i32 s74, s64, s65\n"
    "buffer_load_dwordx4 v10, s[32:35], s73 offen lds\n"
    "v_mfma_f32_32x32x16_bf16 v[160:175], v[136:139], v[228:231], v[160:175]\n"
    "ds_read_b128 v[68:71], v1 offset:0\n"
    "ds_read_b64_tr_b16 v[216:217], v6 offset:0\n"
    "ds_read_b64_tr_b16 v[218:219], v6 offset:512\n"
    "ds_read_b64_tr_b16 v[220:221], v7 offset:0\n"
    "ds_read_b64_tr_b16 v[222:223], v7 offset:512\n"
    "s_add_u32 s73, s73, 20480\n"
    "buffer_load_dwordx4 v10, s[32:35], s73 offen offset:1024 lds\n"
    "s_add_u32 m0, s67, 106496\n"
    "s_add_u32 s78, s74, s76\n"
    "buffer_load_dwordx4 v11, s[36:39], s74 offen lds\n"
    "v_mfma_f32_32x32x16_bf16 v[144:159], v[140:143], v[232:235], v[144:159]\n"
    "ds_read_b128 v[72:75], v2 offset:0\n"
    "ds_read_b64_tr_b16 v[224:225], v4 offset:4096\n"
    "ds_read_b64_tr_b16 v[226:227], v4 offset:4608\n"
    "buffer_load_dwordx4 v11, s[36:39], s78 offen offset:1024 lds\n"
    "s_add_u32 m0, s67, 114688\n"
    "s_add_u32 s64, s64, 1\n"
    "s_cmp_eq_u32 s64, 7\n"
    "s_cselect_b32 s64, 0, s64\n"
    "buffer_load_dwordx4 v12, s[40:43], s74 offen lds\n"
    "v_mfma_f32_32x32x16_bf16 v[160:175], v[140:143], v[236:239], v[160:175]\n"
    "ds_read_b64_tr_b16 v[228:229], v5 offset:4096\n"
    "ds_read_b64_tr_b16 v[230:231], v5 offset:4608\n"
    "buffer_load_dwordx4 v12, s[40:43], s78 offen offset:1024 lds\n"
    "s_waitcnt lgkmcnt(12)\n"
    "v_mfma_f32_32x32x16_bf16 v[144:159], v[64:67], v[208:211], v[144:159]\n"
    "ds_read_b128 v[76:79], v3 offset:0\n"
    "ds_read_b64_tr_b16 v[232:233], v6 offset:4096\n"
    "ds_read_b64_tr_b16 v[234:235], v6 offset:4608\n"
    "v_mfma_f32_16x16x32_bf16 v[240:243], v[208:211], v[244:247], v[240:243]\n"
    "s_lshl_b32 s72, s64, 7\n"
    "s_add_u32 m0, s66, 122880\n"
    "s_mul_i32 s73, s64, 172032\n"
    "buffer_load_dwordx4 v8, s[28:31], s72 offen lds\n"
    "s_add_u32 s72, s72, 6144\n"
    "buffer_load_dwordx4 v9, s[28:31], s72 offen offset:1024 lds\n"
    "s_waitcnt lgkmcnt(13)\n"
    "v_mfma_f32_32x32x16_bf16 v[160:175], v[64:67], v[212:215], v[160:175]\n"
    "ds_read_b64_tr_b16 v[236:237], v7 offset:4096\n"
    "ds_read_b64_tr_b16 v[238:239], v7 offset:4608\n"
    "s_add_u32 s72, s72, 6144\n"
    "buffer_load_dwordx4 v8, s[28:31], s72 offen offset:2048 lds\n"
    "s_add_u32 s72, s72, 6144\n"
    "buffer_load_dwordx4 v9, s[28:31], s72 offen offset:3072 lds\n"
    "s_waitcnt lgkmcnt(12)\n"
    "v_mfma_f32_32x32x16_bf16 v[144:159], v[68:71], v[216:219], v[144:159]\n"
    "s_add_u32 m0, s67, 139264\n"
    "s_mul_i32 s74, s64, s65\n"
    "buffer_load_dwordx4 v10, s[32:35], s73 offen lds\n"
    "s_add_u32 s73, s73, 20480\n"
    "buffer_load_dwordx4 v10, s[32:35], s73 offen offset:1024 lds\n"
    "s_waitcnt lgkmcnt(10)\n"
    "v_mfma_f32_32x32x16_bf16 v[160:175], v[68:71], v[220:223], v[160:175]\n"
    "s_waitcnt lgkmcnt(7)\n"
    "v_mfma_f32_32x32x16_bf16 v[144:159], v[72:75], v[224:227], v[144:159]\n"
    "s_waitcnt vmcnt(16) lgkmcnt(0)\n"
    "s_barrier\n"
    "ds_read_b128 v[80:83], v0 offset:40960\n"
    "ds_read_b64_tr_b16 v[208:209], v4 offset:40960\n"
    "ds_read_b64_tr_b16 v[210:211], v4 offset:41472\n"
    "ds_read_b64_tr_b16 v[212:213], v5 offset:40960\n"
    "ds_read_b64_tr_b16 v[214:215], v5 offset:41472\n"
    "v_mfma_f32_16x16x32_bf16 v[240:243], v[224:227], v[244:247], v[240:243]\n"
    "s_add_u32 m0, s67, 147456\n"
    "s_add_u32 s78, s74, s76\n"
    "buffer_load_dwordx4 v11, s[36:39], s74 offen lds\n"
    "v_mfma_f32_32x32x16_bf16 v[160:175], v[72:75], v[228:231], v[160:175]\n"
    "ds_read_b128 v[40:43], v1 offset:40960\n"
    "ds_read_b64_tr_b16 v[216:217], v6 offset:40960\n"
    "ds_read_b64_tr_b16 v[218:219], v6 offset:41472\n"
    "ds_read_b64_tr_b16 v[220:221], v7 offset:40960\n"
    "ds_read_b64_tr_b16 v[222:223], v7 offset:41472\n"
    "buffer_load_dwordx4 v11, s[36:39], s78 offen offset:1024 lds\n"
    "s_add_u32 m0, s67, 155648\n"
    "s_add_u32 s64, s64, 1\n"
    "s_cmp_eq_u32 s64, 7\n"
    "s_cselect_b32 s64, 0, s64\n"
    "buffer_load_dwordx4 v12, s[40:43], s74 offen lds\n"
    "v_mfma_f32_32x32x16_bf16 v[144:159], v[76:79], v[232:235], v[144:159]\n"
    "ds_read_b128 v[84:87], v2 offset:40960\n"
    "ds_read_b64_tr_b16 v[224:225], v4 offset:45056\n"
    "ds_read_b64_tr_b16 v[226:227], v4 offset:45568\n"
    "buffer_load_dwordx4 v12, s[40:43], s78 offen offset:1024 lds\n"
    "s_mul_i32 s74, s64, 229376\n"
    "s_add_u32 m0, s67, 0\n"
    "s_add_u32 s78, s74, 27648\n"
    "buffer_load_dwordx4 v13, s[44:47], s74 offen lds\n"
    "v_mfma_f32_32x32x16_bf16 v[160:175], v[76:79], v[236:239], v[160:175]\n"
    "ds_read_b64_tr_b16 v[228:229], v5 offset:45056\n"
    "ds_read_b64_tr_b16 v[230:231], v5 offset:45568\n"
    "buffer_load_dwordx4 v13, s[44:47], s78 offen offset:1024 lds\n"
    "s_waitcnt lgkmcnt(12)\n"
    "v_mfma_f32_32x32x16_bf16 v[144:159], v[80:83], v[208:211], v[144:159]\n"
    "ds_read_b128 v[44:47], v3 offset:40960\n"
    "ds_read_b64_tr_b16 v[232:233], v6 offset:45056\n"
    "ds_read_b64_tr_b16 v[234:235], v6 offset:45568\n"
    "v_mfma_f32_16x16x32_bf16 v[240:243], v[208:211], v[244:247], v[240:243]\n"
    "s_add_u32 m0, s67, 8192\n"
    "s_add_u32 s64, s64, 1\n"
    "s_cmp_eq_u32 s64, 7\n"
    "s_cselect_b32 s64, 0, s64\n"
    "buffer_load_dwordx4 v14, s[48:51], s74 offen lds\n"
    "buffer_load_dwordx4 v14, s[48:51], s78 offen offset:1024 lds\n"
    "s_waitcnt lgkmcnt(13)\n"
    "v_mfma_f32_32x32x16_bf16 v[160:175], v[80:83], v[212:215], v[160:175]\n"
    "ds_read_b64_tr_b16 v[236:237], v7 offset:45056\n"
    "ds_read_b64_tr_b16 v[238:239], v7 offset:45568\n"
    "s_mul_i32 s74, s64, 229376\n"
    "s_add_u32 m0, s67, 16384\n"
    "s_add_u32 s78, s74, 27648\n"
    "buffer_load_dwordx4 v13, s[44:47], s74 offen lds\n"
    "buffer_load_dwordx4 v13, s[44:47], s78 offen offset:1024 lds\n"
    "s_waitcnt lgkmcnt(12)\n"
    "v_mfma_f32_32x32x16_bf16 v[144:159], v[40:43], v[216:219], v[144:159]\n"
    "v_add_u32 v0, 0x14000, v0\n"
    "v_add_u32 v1, 0x14000, v1\n"
    "v_add_u32 v2, 0x14000, v2\n"
    "v_add_u32 v3, 0x14000, v3\n"
    "v_add_u32 v4, 0x14000, v4\n"
    "s_add_u32 m0, s67, 24576\n"
    "s_add_u32 s64, s64, 1\n"
    "s_cmp_eq_u32 s64, 7\n"
    "s_cselect_b32 s64, 0, s64\n"
    "buffer_load_dwordx4 v14, s[48:51], s74 offen lds\n"
    "buffer_load_dwordx4 v14, s[48:51], s78 offen offset:1024 lds\n"
    "buffer_load_dwordx2 v[252:253], v25, s[24:27], 0 offen sc1\n"
    "s_waitcnt lgkmcnt(10)\n"
    "v_mfma_f32_32x32x16_bf16 v[160:175], v[40:43], v[220:223], v[160:175]\n"
    "v_add_u32 v5, 0x14000, v5\n"
    "v_add_u32 v6, 0x14000, v6\n"
    "v_add_u32 v7, 0x14000, v7\n"
    "s_waitcnt lgkmcnt(7)\n"
    "v_mfma_f32_32x32x16_bf16 v[144:159], v[84:87], v[224:227], v[144:159]\n"
    "s_waitcnt vmcnt(19) lgkmcnt(0)\n"
    "s_barrier\n"
    "ds_read_b128 v[88:91], v0 offset:0\n"
    "ds_read_b64_tr_b16 v[208:209], v4 offset:0\n"
    "ds_read_b64_tr_b16 v[210:211], v4 offset:512\n"
    "ds_read_b64_tr_b16 v[212:213], v5 offset:0\n"
    "ds_read_b64_tr_b16 v[214:215], v5 offset:512\n"
    "v_mfma_f32_16x16x32_bf16 v[240:243], v[224:227], v[244:247], v[240:243]\n"
    "s_mul_i32 s74, s64, 229376\n"
    "s_add_u32 m0, s67, 32768\n"
    "s_add_u32 s78, s74, 27648\n"
    "buffer_load_dwordx4 v13, s[44:47], s74 offen lds\n"
    "v_mfma_f32_32x32x16_bf16 v[160:175], v[84:87], v[228:231], v[160:175]\n"
    "ds_read_b128 v[48:51], v1 offset:0\n"
    "ds_read_b64_tr_b16 v[216:217], v6 offset:0\n"
    "ds_read_b64_tr_b16 v[218:219], v6 offset:512\n"
    "ds_read_b64_tr_b16 v[220:221], v7 offset:0\n"
    "ds_read_b64_tr_b16 v[222:223], v7 offset:512\n"
    "buffer_load_dwordx4 v13, s[44:47], s78 offen offset:1024 lds\n"
    "s_add_u32 m0, s67, 40960\n"
    "s_add_u32 s64, s64, 1\n"
    "s_cmp_eq_u32 s64, 7\n"
    "s_cselect_b32 s64, 0, s64\n"
    "buffer_load_dwordx4 v14, s[48:51], s74 offen lds\n"
    "v_mfma_f32_32x32x16_bf16 v[144:159], v[44:47], v[232:235], v[144:159]\n"
    "ds_read_b128 v[92:95], v2 offset:0\n"
    "ds_read_b64_tr_b16 v[224:225], v4 offset:4096\n"
    "ds_read_b64_tr_b16 v[226:227], v4 offset:4608\n"
    "buffer_load_dwordx4 v14, s[48:51], s78 offen offset:1024 lds\n"
    "s_mul_i32 s74, s64, 229376\n"
    "s_add_u32 m0, s67, 49152\n"
    "s_add_u32 s78, s74, 27648\n"
    "buffer_load_dwordx4 v13, s[44:47], s74 offen lds\n"
    "v_mfma_f32_32x32x16_bf16 v[160:175], v[44:47], v[236:239], v[160:175]\n"
    "ds_read_b64_tr_b16 v[228:229], v5 offset:4096\n"
    "ds_read_b64_tr_b16 v[230:231], v5 offset:4608\n"
    "buffer_load_dwordx4 v13, s[44:47], s78 offen offset:1024 lds\n"
    "s_waitcnt vmcnt(6)\n"
    "v_cmp_ne_u32 vcc, s69, v253\n"
    "s_mov_b32 s79, 0x40000\n"
    "s_cmp_eq_u64 vcc, 0\n"
    "s_cbranch_scc1 pok3_%=\n"
    "psp4_%=:\n"
    "buffer_load_dwordx2 v[252:253], v25, s[24:27], 0 offen sc1\n"
    "s_sub_u32 s79, s79, 1\n"
    "s_cbranch_scc1 pok3_%=\n"
    "s_waitcnt vmcnt(0)\n"
    "v_cmp_ne_u32 vcc, s69, v253\n"
    "s_sleep 1\n"
    "s_cmp_eq_u64 vcc, 0\n"
    "s_cbranch_scc0 psp4_%=\n"
    "pok3_%=:\n"
    "s_waitcnt lgkmcnt(12)\n"
    "v_mfma_f32_32x32x16_bf16 v[144:159], v[88:91], v[208:211], v[144:159]\n"
    "ds_read_b128 v[52:55], v3 offset:0\n"
    "ds_read_b64_tr_b16 v[232:233], v6 offset:4096\n"
    "ds_read_b64_tr_b16 v[234:235], v6 offset:4608\n"
    "v_mfma_f32_16x16x32_bf16 v[240:243], v[208:211], v[244:247], v[240:243]\n"
    "s_add_u32 m0, s67, 57344\n"
    "s_add_u32 s64, s64, 1\n"
    "s_cmp_eq_u32 s64, 7\n"
    "s_cselect_b32 s64, 0, s64\n"
    "buffer_load_dwordx4 v14, s[48:51], s74 offen lds\n"
    "buffer_load_dwordx4 v14, s[48:51], s78 offen offset:1024 lds\n"
    "v_and_b32 v254, s77, v252\n"
    "v_and_b32 v248, 0xffff, v252\n"
    "v_rcp_f32 v255, v254\n"
    "s_waitcnt lgkmcnt(13)\n"
    "v_mfma_f32_32x32x16_bf16 v[160:175], v[88:91], v[212:215], v[160:175]\n"
    "ds_read_b64_tr_b16 v[236:237], v7 offset:4096\n"
    "ds_read_b64_tr_b16 v[238:239], v7 offset:4608\n"
    "s_mul_i32 s74, s64, 229376\n"
    "s_add_u32 m0, s67, 65536\n"
    "s_add_u32 s78, s74, 27648\n"
    "buffer_load_dwordx4 v13, s[44:47], s74 offen lds\n"
    "buffer_load_dwordx4 v13, s[44:47], s78 offen offset:1024 lds\n"
    "v_lshl_or_b32 v248, v252, 16, v248\n"
    "v_mul_f32 v254, v254, v254\n"
    "v_mov_b32 v249, v248\n"
    "s_waitcnt lgkmcnt(12)\n"
    "v_mfma_f32_32x32x16_bf16 v[144:159], v[48:51], v[216:219], v[144:159]\n"
    "s_add_u32 m0, s67, 73728\n"
    "s_add_u32 s64, s64, 1\n"
    "s_cmp_eq_u32 s64, 7\n"
    "s_cselect_b32 s64, 0, s64\n"
    "buffer_load_dwordx4 v14, s[48:51], s74 offen lds\n"
    "buffer_load_dwordx4 v14, s[48:51], s78 offen offset:1024 lds\n"
    "v_mul_f32 v252, 0xc0135761, v255\n"
    "v_mul_f32 v253, v255, v255\n"
    "v_mul_f32 v253, v253, v252\n"
    "v_mul_f32 v253, 0x3d372713, v253\n"
    "s_waitcnt lgkmcnt(10)\n"
    "v_mfma_f32_32x32x16_bf16 v[160:175], v[48:51], v[220:223], v[160:175]\n"
    "s_waitcnt lgkmcnt(7)\n"
    "v_mfma_f32_32x32x16_bf16 v[144:159], v[92:95], v[224:227], v[144:159]\n"
    "s_waitcnt lgkmcnt(0)\n"
    "s_barrier\n"
    "ds_read_b128 v[32:35], v0 offset:40960\n"
    "ds_read_b64_tr_b16 v[208:209], v4 offset:40960\n"
    "ds_read_b64_tr_b16 v[210:211], v4 offset:41472\n"
    "ds_read_b128 v[36:39], v2 offset:40960\n"
    "ds_read_b64_tr_b16 v[212:213], v4 offset:45056\n"
    "ds_read_b64_tr_b16 v[214:215], v4 offset:45568\n"
    "v_mfma_f32_16x16x32_bf16 v[240:243], v[224:227], v[244:247], v[240:243]\n"
    "s_mul_i32 s74, s64, 229376\n"
    "s_add_u32 m0, s67, 81920\n"
    "s_add_u32 s78, s74, 27648\n"
    "buffer_load_dwordx4 v13, s[44:47], s74 offen lds\n"
    "s_lshr_b32 s75, s66, 5\n"
    "s_add_u32 s75, s75, 121856\n"
    "v_mfma_f32_32x32x16_bf16 v[160:175], v[92:95], v[228:231], v[160:175]\n"
    "ds_read_b64_tr_b16 v[216:217], v5 offset:40960\n"
    "ds_read_b64_tr_b16 v[218:219], v5 offset:41472\n"
    "ds_read_b64_tr_b16 v[220:221], v5 offset:45056\n"
    "ds_read_b64_tr_b16 v[222:223], v5 offset:45568\n"
    "buffer_load_dwordx4 v13, s[44:47], s78 offen offset:1024 lds\n"
    "v_bfe_u32 v192, v15, 4, 5\n"
    "v_lshl_add_u32 v192, v192, 2, s75\n"
    "v_mfma_f32_32x32x16_bf16 v[144:159], v[52:55], v[232:235], v[144:159]\n"
    "ds_read_b128 v[56:59], v1 offset:40960\n"
    "ds_read_b64_tr_b16 v[224:225], v6 offset:40960\n"
    "ds_read_b64_tr_b16 v[226:227], v6 offset:41472\n"
    "s_add_u32 m0, s67, 90112\n"
    "s_add_u32 s64, s64, 1\n"
    "s_cmp_eq_u32 s64, 7\n"
    "s_cselect_b32 s64, 0, s64\n"
    "buffer_load_dwordx4 v14, s[48:51], s74 offen lds\n"
    "buffer_load_dwordx4 v14, s[48:51], s78 offen offset:1024 lds\n"
    "ds_write_b32 v192, v255\n"
    "v_lshrrev_b32 v193, 9, v15\n"
    "v_mfma_f32_32x32x16_bf16 v[160:175], v[52:55], v[236:239], v[160:175]\n"
    "ds_read_b64_tr_b16 v[228:229], v7 offset:40960\n"
    "ds_read_b64_tr_b16 v[230:231], v7 offset:41472\n"
    "s_mul_i32 s74, s64, 229376\n"
    "s_add_u32 m0, s67, 98304\n"
    "s_add_u32 s78, s74, 27648\n"
    "buffer_load_dwordx4 v13, s[44:47], s74 offen lds\n"
    "v_lshl_add_u32 v193, v193, 4, s75\n"
    "ds_read_b128 v[176:179], v193 offset:0\n"
    "s_waitcnt lgkmcnt(14)\n"
    "v_mfma_f32_32x32x16_bf16 v[144:159], v[32:35], v[208:211], v[144:159]\n"
    "ds_read_b128 v[60:63], v3 offset:40960\n"
    "ds_read_b64_tr_b16 v[232:233], v6 offset:45056\n"
    "ds_read_b64_tr_b16 v[234:235], v6 offset:45568\n"
    "v_mfma_f32_16x16x32_bf16 v[240:243], v[208:211], v[244:247], v[240:243]\n"
    "buffer_load_dwordx4 v13, s[44:47], s78 offen offset:1024 lds\n"
    "ds_read_b128 v[180:183], v193 offset:32\n"
    "ds_read_b128 v[184:187], v193 offset:64\n"
    "ds_read_b128 v[188:191], v193 offset:96\n"
    "s_waitcnt lgkmcnt(15)\n"
    "v_mfma_f32_32x32x16_bf16 v[144:159], v[36:39], v[212:215], v[144:159]\n"
    "ds_read_b64_tr_b16 v[236:237], v7 offset:45056\n"
    "ds_read_b64_tr_b16 v[238:239], v7 offset:45568\n"
    "v_mfma_f32_16x16x32_bf16 v[240:243], v[212:215], v[244:247], v[240:243]\n"
    "s_add_u32 m0, s67, 106496\n"
    "s_add_u32 s64, s64, 1\n"
    "s_cmp_eq_u32 s64, 7\n"
    "s_cselect_b32 s64, 0, s64\n"
    "buffer_load_dwordx4 v14, s[48:51], s74 offen lds\n"
    "buffer_load_dwordx4 v14, s[48:51], s78 offen offset:1024 lds\n"
    "v_mfma_f32_32x32x16_bf16 v[160:175], v[32:35], v[216:219], v[160:175]\n"
    "s_waitcnt lgkmcnt(15)\n"
    "v_mfma_f32_32x32x16_bf16 v[160:175], v[36:39], v[220:223], v[160:175]\n"
    "v_cndmask_b32 v240, v240, v241, s[80:81]\n"
    "v_cndmask_b32 v242, v242, v243, s[80:81]\n"
    "v_cndmask_b32 v240, v240, v242, s[82:83]\n"
    "v_and_b32 v241, s77, v240\n"
    "v_sub_f32 v241, v240, v241\n"
    "s_waitcnt lgkmcnt(12)\n"
    "v_mfma_f32_32x32x16_bf16 v[144:159], v[56:59], v[224:227], v[144:159]\n"
    "v_and_b32 v242, s77, v241\n"
    "v_sub_f32 v243, v241, v242\n"
    "v_lshrrev_b32 v240, 16, v240\n"
    "v_or_b32 v242, v240, v242\n"
    "v_lshrrev_b32 v243, 16, v243\n"
    "ds_write_b64 v20, v[242:243]\n"
    "s_waitcnt lgkmcnt(10)\n"
    "v_mfma_f32_32x32x16_bf16 v[160:175], v[56:59], v[228:231], v[160:175]\n"
    "s_waitcnt lgkmcnt(6)\n"
    "v_mfma_f32_32x32x16_bf16 v[144:159], v[60:63], v[232:235], v[144:159]\n"
    "s_waitcnt lgkmcnt(1)\n"
    "v_mfma_f32_32x32x16_bf16 v[160:175], v[60:63], v[236:239], v[160:175]\n"
    "s_waitcnt vmcnt(8) lgkmcnt(0)\n"
    "s_barrier\n"
    "ds_read_b64 v[208:209], v22\n"
    "v_xor_b32 v1, 512, v22\n"
    "ds_read_b64 v[212:213], v1\n"
    "s_waitcnt lgkmcnt(1)\n"
    "v_mfma_f32_32x32x16_bf16 v[144:159], v[248:251], v[208:211], v[144:159]\n"
    "s_waitcnt lgkmcnt(0)\n"
    "v_mfma_f32_32x32x16_bf16 v[160:175], v[248:251], v[212:215], v[160:175]\n"
    "s_nop 9\n"
    "v_mul_f32 v144, v176, v144\n"
    "v_mul_f32 v145, v177, v145\n"
    "v_mul_f32 v146, v178, v146\n"
    "v_mul_f32 v147, v179, v147\n"
    "v_mul_f32 v148, v180, v148\n"
    "v_mul_f32 v149, v181, v149\n"
    "v_mul_f32 v150, v182, v150\n"
    "v_mul_f32 v151, v183, v151\n"
    "v_cvt_pk_bf16_f32 v224, v144, v145\n"
    "v_cvt_pk_bf16_f32 v225, v146, v147\n"
    "v_cvt_pk_bf16_f32 v226, v148, v149\n"
    "v_cvt_pk_bf16_f32 v227, v150, v151\n"
    "buffer_store_dwordx4 v[224:227], v28, s[24:27], s56 offen sc1\n"
    "v_mul_f32 v152, v184, v152\n"
    "v_mul_f32 v153, v185, v153\n"
    "v_mul_f32 v154, v186, v154\n"
    "v_mul_f32 v155, v187, v155\n"
    "v_mul_f32 v156, v188, v156\n"
    "v_mul_f32 v157, v189, v157\n"
    "v_mul_f32 v158, v190, v158\n"
    "v_mul_f32 v159, v191, v159\n"
    "v_cvt_pk_bf16_f32 v228, v152, v153\n"
    "v_cvt_pk_bf16_f32 v229, v154, v155\n"
    "v_cvt_pk_bf16_f32 v230, v156, v157\n"
    "v_cvt_pk_bf16_f32 v231, v158, v159\n"
    "buffer_store_dwordx4 v[228:231], v28, s[24:27], s56 offen offset:1024 sc1\n"
    "v_mul_f32 v160, v176, v160\n"
    "v_mul_f32 v161, v177, v161\n"
    "v_mul_f32 v162, v178, v162\n"
    "v_mul_f32 v163, v179, v163\n"
    "v_mul_f32 v164, v180, v164\n"
    "v_mul_f32 v165, v181, v165\n"
    "v_mul_f32 v166, v182, v166\n"
    "v_mul_f32 v167, v183, v167\n"
    "v_cvt_pk_bf16_f32 v232, v160, v161\n"
    "v_cvt_pk_bf16_f32 v233, v162, v163\n"
    "v_cvt_pk_bf16_f32 v234, v164, v165\n"
    "v_cvt_pk_bf16_f32 v235, v166, v167\n"
    "buffer_store_dwordx4 v[232:235], v29, s[24:27], s56 offen sc1\n"
    "v_mul_f32 v168, v184, v168\n"
    "v_mul_f32 v169, v185, v169\n"
    "v_mul_f32 v170, v186, v170\n"
    "v_mul_f32 v171, v187, v171\n"
    "v_mul_f32 v172, v188, v172\n"
    "v_mul_f32 v173, v189, v173\n"
    "v_mul_f32 v174, v190, v174\n"
    "v_mul_f32 v175, v191, v175\n"
    "v_cvt_pk_bf16_f32 v236, v168, v169\n"
    "v_cvt_pk_bf16_f32 v237, v170, v171\n"
    "v_cvt_pk_bf16_f32 v238, v172, v173\n"
    "v_cvt_pk_bf16_f32 v239, v174, v175\n"
    "buffer_store_dwordx4 v[236:239], v29, s[24:27], s56 offen offset:1024 sc1\n"
    "a2_%=:\n"
    "s_waitcnt vmcnt(4)\n"
    "v_mov_b32 v24, 122376\n"
    "v_mov_b32 v25, 1\n"
    "s_mov_b64 exec, 1\n"
    "ds_add_u32 v24, v25\n"
    "s_mov_b64 exec, -1\n"
    "s_setprio 3\n"
    "ds_read_b64_tr_b16 v[208:209], v16 offset:0\n"
    "ds_read_b64_tr_b16 v[210:211], v16 offset:512\n"
    "ds_read_b64_tr_b16 v[212:213], v17 offset:0\n"
    "ds_read_b64_tr_b16 v[214:215], v17 offset:512\n"
    "ds_read_b64_tr_b16 v[216:217], v16 offset:4096\n"
    "ds_read_b64_tr_b16 v[218:219], v16 offset:4608\n"
    "ds_read_b64_tr_b16 v[220:221], v17 offset:4096\n"
    "ds_read_b64_tr_b16 v[222:223], v17 offset:4608\n"
    "ds_read_b64_tr_b16 v[224:225], v18 offset:0\n"
    "ds_read_b64_tr_b16 v[226:227], v18 offset:512\n"
    "ds_read_b64_tr_b16 v[228:229], v19 offset:0\n"
    "ds_read_b64_tr_b16 v[230:231], v19 offset:512\n"
    "s_waitcnt lgkmcnt(10)\n"
    "v_mfma_f32_32x32x16_bf16 v[144:159], v[208:211], v[96:99], 0\n"
    "ds_read_b64_tr_b16 v[232:233], v18 offset:4096\n"
    "ds_read_b64_tr_b16 v[234:235], v18 offset:4608\n"
    "v_mfma_f32_16x16x32_bf16 v[240:243], v[208:211], v[244:247], 0\n"
    "s_waitcnt lgkmcnt(10)\n"
    "v_mfma_f32_32x32x16_bf16 v[160:175], v[212:215], v[96:99], 0\n"
    "ds_read_b64_tr_b16 v[236:237], v19 offset:4096\n"
    "ds_read_b64_tr_b16 v[238:239], v19 offset:4608\n"
    "s_waitcnt lgkmcnt(10)\n"
    "v_mfma_f32_32x32x16_bf16 v[144:159], v[216:219], v[104:107], v[144:159]\n"
    "ds_read_b64_tr_b16 v[208:209], v16 offset:16384\n"
    "ds_read_b64_tr_b16 v[210:211], v16 offset:16896\n"
    "v_mfma_f32_16x16x32_bf16 v[240:243], v[216:219], v[244:247], v[240:243]\n"
    "s_waitcnt lgkmcnt(10)\n"
    "v_mfma_f32_32x32x16_bf16 v[160:175], v[220:223], v[104:107], v[160:175]\n"
    "ds_read_b64_tr_b16 v[212:213], v17 offset:16384\n"
    "ds_read_b64_tr_b16 v[214:215], v17 offset:16896\n"
    "s_waitcnt lgkmcnt(10)\n"
    "v_mfma_f32_32x32x16_bf16 v[144:159], v[224:227], v[100:103], v[144:159]\n"
    "ds_read_b64_tr_b16 v[216:217], v16 offset:20480\n"
    "ds_read_b64_tr_b16 v[218:219], v16 offset:20992\n"
    "s_waitcnt lgkmcnt(10)\n"
    "v_mfma_f32_32x32x16_bf16 v[160:175], v[228:231], v[100:103], v[160:175]\n"
    "ds_read_b64_tr_b16 v[220:221], v17 offset:20480\n"
    "ds_read_b64_tr_b16 v[222:223], v17 offset:20992\n"
    "s_waitcnt lgkmcnt(10)\n"
    "v_mfma_f32_32x32x16_bf16 v[144:159], v[232:235], v[108:111], v[144:159]\n"
    "ds_read_b64_tr_b16 v[224:225], v18 offset:16384\n"
    "ds_read_b64_tr_b16 v[226:227], v18 offset:16896\n"
    "s_waitcnt lgkmcnt(10)\n"
    "v_mfma_f32_32x32x16_bf16 v[160:175], v[236:239], v[108:111], v[160:175]\n"
    "ds_read_b64_tr_b16 v[228:229], v19 offset:16384\n"
    "ds_read_b64_tr_b16 v[230:231], v19 offset:16896\n"
    "s_waitcnt lgkmcnt(10)\n"
    "v_mfma_f32_32x32x16_bf16 v[144:159], v[208:211], v[112:115], v[144:159]\n"
    "ds_read_b64_tr_b16 v[232:233], v18 offset:20480\n"
    "ds_read_b64_tr_b16 v[234:235], v18 offset:20992\n"
    "v_mfma_f32_16x16x32_bf16 v[240:243], v[208:211], v[244:247], v[240:243]\n"
    "s_waitcnt lgkmcnt(10)\n"
    "v_mfma_f32_32x32x16_bf16 v[160:175], v[212:215], v[112:115], v[160:175]\n"
    "ds_read_b64_tr_b16 v[236:237], v19 offset:20480\n"
    "ds_read_b64_tr_b16 v[238:239], v19 offset:20992\n"
    "s_waitcnt lgkmcnt(10)\n"
    "v_mfma_f32_32x32x16_bf16 v[144:159], v[216:219], v[120:123], v[144:159]\n"
    "ds_read_b64_tr_b16 v[208:209], v16 offset:32768\n"
    "ds_read_b64_tr_b16 v[210:211], v16 offset:33280\n"
    "v_mfma_f32_16x16x32_bf16 v[240:243], v[216:219], v[244:247], v[240:243]\n"
    "s_waitcnt lgkmcnt(10)\n"
    "v_mfma_f32_32x32x16_bf16 v[160:175], v[220:223], v[120:123], v[160:175]\n"
    "ds_read_b64_tr_b16 v[212:213], v17 offset:32768\n"
    "ds_read_b64_tr_b16 v[214:215], v17 offset:33280\n"
    "s_waitcnt lgkmcnt(10)\n"
    "v_mfma_f32_32x32x16_bf16 v[144:159], v[224:227], v[116:119], v[144:159]\n"
    "ds_read_b64_tr_b16 v[216:217], v16 offset:36864\n"
    "ds_read_b64_tr_b16 v[218:219], v16 offset:37376\n"
    "s_waitcnt lgkmcnt(10)\n"
    "v_mfma_f32_32x32x16_bf16 v[160:175], v[228:231], v[116:119], v[160:175]\n"
    "ds_read_b64_tr_b16 v[220:221], v17 offset:36864\n"
    "ds_read_b64_tr_b16 v[222:223], v17 offset:37376\n"
    "s_waitcnt lgkmcnt(10)\n"
    "v_mfma_f32_32x32x16_bf16 v[144:159], v[232:235], v[124:127], v[144:159]\n"
    "ds_read_b64_tr_b16 v[224:225], v18 offset:32768\n"
    "ds_read_b64_tr_b16 v[226:227], v18 offset:33280\n"
    "s_waitcnt lgkmcnt(10)\n"
    "v_mfma_f32_32x32x16_bf16 v[160:175], v[236:239], v[124:127], v[160:175]\n"
    "ds_read_b64_tr_b16 v[228:229], v19 offset:32768\n"
    "ds_read_b64_tr_b16 v[230:231], v19 offset:33280\n"
    "s_waitcnt lgkmcnt(10)\n"
    "v_mfma_f32_32x32x16_bf16 v[144:159], v[208:211], v[128:131], v[144:159]\n"
    "ds_read_b64_tr_b16 v[232:233], v18 offset:36864\n"
    "ds_read_b64_tr_b16 v[234:235], v18 offset:37376\n"
    "v_mfma_f32_16x16x32_bf16 v[240:243], v[208:211], v[244:247], v[240:243]\n"
    "s_waitcnt lgkmcnt(10)\n"
    "v_mfma_f32_32x32x16_bf16 v[160:175], v[212:215], v[128:131], v[160:175]\n"
    "ds_read_b64_tr_b16 v[236:237], v19 offset:36864\n"
    "ds_read_b64_tr_b16 v[238:239], v19 offset:37376\n"
    "v_mov_b32 v176, 0\n"
    "v_mov_b32 v177, 0\n"
    "s_waitcnt lgkmcnt(10)\n"
    "v_mfma_f32_32x32x16_bf16 v[144:159], v[216:219], v[136:139], v[144:159]\n"
    "ds_read_b64_tr_b16 v[208:209], v16 offset:49152\n"
    "ds_read_b64_tr_b16 v[210:211], v16 offset:49664\n"
    "v_mfma_f32_16x16x32_bf16 v[240:243], v[216:219], v[244:247], v[240:243]\n"
    "s_waitcnt lgkmcnt(10)\n"
    "v_mfma_f32_32x32x16_bf16 v[160:175], v[220:223], v[136:139], v[160:175]\n"
    "ds_read_b64_tr_b16 v[212:213], v17 offset:49152\n"
    "ds_read_b64_tr_b16 v[214:215], v17 offset:49664\n"
    "v_mov_b32 v178, 0\n"
    "v_mov_b32 v179, 0\n"
    "s_waitcnt lgkmcnt(10)\n"
    "v_mfma_f32_32x32x16_bf16 v[144:159], v[224:227], v[132:135], v[144:159]\n"
    "ds_read_b64_tr_b16 v[216:217], v16 offset:53248\n"
    "ds_read_b64_tr_b16 v[218:219], v16 offset:53760\n"
    "v_mov_b32 v180, 0\n"
    "s_waitcnt lgkmcnt(10)\n"
    "v_mfma_f32_32x32x16_bf16 v[160:175], v[228:231], v[132:135], v[160:175]\n"
    "ds_read_b64_tr_b16 v[220:221], v17 offset:53248\n"
    "ds_read_b64_tr_b16 v[222:223], v17 offset:53760\n"
    "v_mov_b32 v181, 0\n"
    "s_waitcnt vmcnt(0)\n"
    "v_mov_b32 v24, s60\n"
    "v_mov_b32 v25, 1\n"
    "s_mov_b64 exec, 1\n"
    "ds_add_rtn_u32 v10, v24, v25\n"
    "s_mov_b64 exec, -1\n"
    "s_waitcnt lgkmcnt(11)\n"
    "v_mfma_f32_32x32x16_bf16 v[144:159], v[232:235], v[140:143], v[144:159]\n"
    "ds_read_b64_tr_b16 v[224:225], v18 offset:49152\n"
    "ds_read_b64_tr_b16 v[226:227], v18 offset:49664\n"
    "v_mov_b32 v182, 0\n"
    "s_waitcnt lgkmcnt(11)\n"
    "v_mfma_f32_32x32x16_bf16 v[160:175], v[236:239], v[140:143], v[160:175]\n"
    "ds_read_b64_tr_b16 v[228:229], v19 offset:49152\n"
    "ds_read_b64_tr_b16 v[230:231], v19 offset:49664\n"
    "v_mov_b32 v183, 0\n"
    "s_waitcnt lgkmcnt(11)\n"
    "v_mfma_f32_32x32x16_bf16 v[144:159], v[208:211], v[64:67], v[144:159]\n"
    "ds_read_b64_tr_b16 v[232:233], v18 offset:53248\n"
    "ds_read_b64_tr_b16 v[234:235], v18 offset:53760\n"
    "v_mfma_f32_16x16x32_bf16 v[240:243], v[208:211], v[244:247], v[240:243]\n"
    "s_waitcnt lgkmcnt(11)\n"
    "v_mfma_f32_32x32x16_bf16 v[160:175], v[212:215], v[64:67], v[160:175]\n"
    "ds_read_b64_tr_b16 v[236:237], v19 offset:53248\n"
    "ds_read_b64_tr_b16 v[238:239], v19 offset:53760\n"
    "v_mov_b32 v184, 0\n"
    "v_mov_b32 v185, 0\n"
    "s_waitcnt lgkmcnt(8)\n"
    "v_readfirstlane_b32 s75, v10\n"
    "v_mov_b32 v25, s69\n"
    "v_mov_b32 v24, 0\n"
    "s_cmp_eq_u32 s75, s59\n"
    "s_cselect_b32 s75, s70, 0x40000000\n"
    "s_mov_b64 exec, 1\n"
    "buffer_store_dword v25, v24, s[24:27], s75 offen sc1\n"
    "s_mov_b64 exec, -1\n"
    "v_mfma_f32_32x32x16_bf16 v[144:159], v[216:219], v[72:75], v[144:159]\n"
    "v_add_u32 v16, 0x10000, v16\n"
    "v_add_u32 v17, 0x10000, v17\n"
    "ds_read_b64_tr_b16 v[208:209], v16 offset:0\n"
    "ds_read_b64_tr_b16 v[210:211], v16 offset:512\n"
    "v_mov_b32 v186, 0\n"
    "v_mfma_f32_16x16x32_bf16 v[240:243], v[216:219], v[244:247], v[240:243]\n"
    "v_mfma_f32_32x32x16_bf16 v[160:175], v[220:223], v[72:75], v[160:175]\n"
    "ds_read_b64_tr_b16 v[212:213], v17 offset:0\n"
    "ds_read_b64_tr_b16 v[214:215], v17 offset:512\n"
    "v_mov_b32 v187, 0\n"
    "v_mov_b32 v0, -1\n"
    "v_mov_b32 v1, -1\n"
    "v_mov_b32 v2, -1\n"
    "v_mov_b32 v3, -1\n"
    "s_waitcnt lgkmcnt(10)\n"
    "v_mfma_f32_32x32x16_bf16 v[144:159], v[224:227], v[68:71], v[144:159]\n"
    "ds_read_b64_tr_b16 v[216:217], v16 offset:4096\n"
    "ds_read_b64_tr_b16 v[218:219], v16 offset:4608\n"
    "v_mov_b32 v188, 0\n"
    "s_waitcnt lgkmcnt(10)\n"
    "v_mfma_f32_32x32x16_bf16 v[160:175], v[228:231], v[68:71], v[160:175]\n"
    "ds_read_b64_tr_b16 v[220:221], v17 offset:4096\n"
    "ds_read_b64_tr_b16 v[222:223], v17 offset:4608\n"
    "v_mov_b32 v189, 0\n"
    "buffer_store_dwordx4 v[0:3], v15, s[24:27], s71 offen sc1\n"
    "v_mov_b32 v9, 122376\n"
    "ds_read_b32 v8, v9\n"
    "s_waitcnt lgkmcnt(11)\n"
    "v_mfma_f32_32x32x16_bf16 v[144:159], v[232:235], v[76:79], v[144:159]\n"
    "v_add_u32 v18, 0x10000, v18\n"
    "v_add_u32 v19, 0x10000, v19\n"
    "ds_read_b64_tr_b16 v[224:225], v18 offset:0\n"
    "ds_read_b64_tr_b16 v[226:227], v18 offset:512\n"
    "v_mov_b32 v190, 0\n"
    "s_waitcnt lgkmcnt(11)\n"
    "v_mfma_f32_32x32x16_bf16 v[160:175], v[236:239], v[76:79], v[160:175]\n"
    "ds_read_b64_tr_b16 v[228:229], v19 offset:0\n"
    "ds_read_b64_tr_b16 v[230:231], v19 offset:512\n"
    "v_mov_b32 v191, 0\n"
    "s_mov_b32 exec_hi, 0\n"
    "buffer_store_dwordx4 v[0:3], v15, s[24:27], s71 offen offset:1024 sc1\n"
    "s_not_b64 exec, exec\n"
    "buffer_store_dwordx2 v[0:1], v15, s[24:27], s68 offen offset:1024 sc1\n"
    "s_mov_b32 exec_lo, -1\n"
    "s_waitcnt lgkmcnt(11)\n"
    "v_mfma_f32_32x32x16_bf16 v[144:159], v[208:211], v[80:83], v[144:159]\n"
    "ds_read_b64_tr_b16 v[232:233], v18 offset:4096\n"
    "ds_read_b64_tr_b16 v[234:235], v18 offset:4608\n"
    "v_mfma_f32_16x16x32_bf16 v[240:243], v[208:211], v[244:247], v[240:243]\n"
    "s_waitcnt lgkmcnt(11)\n"
    "v_mfma_f32_32x32x16_bf16 v[160:175], v[212:215], v[80:83], v[160:175]\n"
    "ds_read_b64_tr_b16 v[236:237], v19 offset:4096\n"
    "ds_read_b64_tr_b16 v[238:239], v19 offset:4608\n"
    "v_mov_b32 v192, 0\n"
    "v_mov_b32 v193, 0\n"
    "s_waitcnt lgkmcnt(8)\n"
    "v_readfirstlane_b32 s75, v8\n"
    "s_nop 0\n"
    "s_cmp_ge_u32 s75, 4\n"
    "s_cbranch_scc1 cok5_%=\n"
    "s_mov_b32 s79, 0x100000\n"
    "csp6_%=:\n"
    "ds_read_b32 v8, v9\n"
    "s_sub_u32 s79, s79, 1\n"
    "s_cbranch_scc1 cok5_%=\n"
    "s_sleep 1\n"
    "s_waitcnt lgkmcnt(0)\n"
    "v_readfirstlane_b32 s75, v8\n"
    "s_nop 0\n"
    "s_cmp_ge_u32 s75, 4\n"
    "s_cbranch_scc0 csp6_%=\n"
    "cok5_%=:\n"
    "v_mfma_f32_32x32x16_bf16 v[144:159], v[216:219], v[84:87], v[144:159]\n"
    "ds_read_b64_tr_b16 v[208:209], v16 offset:16384\n"
    "ds_read_b64_tr_b16 v[210:211], v16 offset:16896\n"
    "v_mfma_f32_16x16x32_bf16 v[240:243], v[216:219], v[244:247], v[240:243]\n"
    "v_mfma_f32_32x32x16_bf16 v[160:175], v[220:223], v[84:87], v[160:175]\n"
    "ds_read_b64_tr_b16 v[212:213], v17 offset:16384\n"
    "ds_read_b64_tr_b16 v[214:215], v17 offset:16896\n"
    "v_mov_b32 v194, 0\n"
    "v_mov_b32 v195, 0\n"
    "s_waitcnt lgkmcnt(10)\n"
    "v_mfma_f32_32x32x16_bf16 v[144:159], v[224:227], v[40:43], v[144:159]\n"
    "ds_read_b64_tr_b16 v[216:217], v16 offset:20480\n"
    "ds_read_b64_tr_b16 v[218:219], v16 offset:20992\n"
    "v_mov_b32 v196, 0\n"
    "s_waitcnt lgkmcnt(10)\n"
    "v_mfma_f32_32x32x16_bf16 v[160:175], v[228:231], v[40:43], v[160:175]\n"
    "ds_read_b64_tr_b16 v[220:221], v17 offset:20480\n"
    "ds_read_b64_tr_b16 v[222:223], v17 offset:20992\n"
    "v_mov_b32 v197, 0\n"
    "s_waitcnt lgkmcnt(10)\n"
    "v_mfma_f32_32x32x16_bf16 v[144:159], v[232:235], v[44:47], v[144:159]\n"
    "ds_read_b64_tr_b16 v[224:225], v16 offset:32768\n"
    "ds_read_b64_tr_b16 v[226:227], v16 offset:33280\n"
    "v_mov_b32 v198, 0\n"
    "s_waitcnt lgkmcnt(10)\n"
    "v_mfma_f32_32x32x16_bf16 v[160:175], v[236:239], v[44:47], v[160:175]\n"
    "ds_read_b64_tr_b16 v[228:229], v17 offset:32768\n"
    "ds_read_b64_tr_b16 v[230:231], v17 offset:33280\n"
    "v_mov_b32 v199, 0\n"
    "s_waitcnt lgkmcnt(10)\n"
    "v_mfma_f32_32x32x16_bf16 v[144:159], v[208:211], v[88:91], v[144:159]\n"
    "ds_read_b64_tr_b16 v[232:233], v16 offset:36864\n"
    "ds_read_b64_tr_b16 v[234:235], v16 offset:37376\n"
    "v_mfma_f32_16x16x32_bf16 v[240:243], v[208:211], v[244:247], v[240:243]\n"
    "s_waitcnt lgkmcnt(10)\n"
    "v_mfma_f32_32x32x16_bf16 v[160:175], v[212:215], v[88:91], v[160:175]\n"
    "ds_read_b64_tr_b16 v[236:237], v17 offset:36864\n"
    "ds_read_b64_tr_b16 v[238:239], v17 offset:37376\n"
    "v_mov_b32 v200, 0\n"
    "v_mov_b32 v201, 0\n"
    "s_waitcnt lgkmcnt(10)\n"
    "v_mfma_f32_32x32x16_bf16 v[144:159], v[216:219], v[92:95], v[144:159]\n"
    "ds_read_b64_tr_b16 v[208:209], v18 offset:16384\n"
    "ds_read_b64_tr_b16 v[210:211], v18 offset:16896\n"
    "v_mfma_f32_16x16x32_bf16 v[240:243], v[216:219], v[244:247], v[240:243]\n"
    "s_waitcnt lgkmcnt(10)\n"
    "v_mfma_f32_32x32x16_bf16 v[160:175], v[220:223], v[92:95], v[160:175]\n"
    "ds_read_b64_tr_b16 v[212:213], v19 offset:16384\n"
    "ds_read_b64_tr_b16 v[214:215], v19 offset:16896\n"
    "v_mov_b32 v202, 0\n"
    "v_mov_b32 v203, 0\n"
    "s_waitcnt lgkmcnt(10)\n"
    "v_mfma_f32_32x32x16_bf16 v[144:159], v[224:227], v[32:35], v[144:159]\n"
    "ds_read_b64_tr_b16 v[216:217], v18 offset:20480\n"
    "ds_read_b64_tr_b16 v[218:219], v18 offset:20992\n"
    "v_mfma_f32_16x16x32_bf16 v[240:243], v[224:227], v[244:247], v[240:243]\n"
    "s_waitcnt lgkmcnt(10)\n"
    "v_mfma_f32_32x32x16_bf16 v[160:175], v[228:231], v[32:35], v[160:175]\n"
    "ds_read_b64_tr_b16 v[220:221], v19 offset:20480\n"
    "ds_read_b64_tr_b16 v[222:223], v19 offset:20992\n"
    "v_mov_b32 v204, 0\n"
    "v_mov_b32 v205, 0\n"
    "s_waitcnt lgkmcnt(10)\n"
    "v_mfma_f32_32x32x16_bf16 v[144:159], v[232:235], v[36:39], v[144:159]\n"
    "ds_read_b64_tr_b16 v[224:225], v18 offset:32768\n"
    "ds_read_b64_tr_b16 v[226:227], v18 offset:33280\n"
    "v_mfma_f32_16x16x32_bf16 v[240:243], v[232:235], v[244:247], v[240:243]\n"
    "s_waitcnt lgkmcnt(10)\n"
    "v_mfma_f32_32x32x16_bf16 v[160:175], v[236:239], v[36:39], v[160:175]\n"
    "ds_read_b64_tr_b16 v[228:229], v19 offset:32768\n"
    "ds_read_b64_tr_b16 v[230:231], v19 offset:33280\n"
    "v_mov_b32 v206, 0\n"
    "v_mov_b32 v207, 0\n"
    "s_waitcnt lgkmcnt(10)\n"
    "v_mfma_f32_32x32x16_bf16 v[144:159], v[208:211], v[48:51], v[144:159]\n"
    "ds_read_b64_tr_b16 v[232:233], v18 offset:36864\n"
    "ds_read_b64_tr_b16 v[234:235], v18 offset:37376\n"
    "s_waitcnt lgkmcnt(10)\n"
    "v_mfma_f32_32x32x16_bf16 v[160:175], v[212:215], v[48:51], v[160:175]\n"
    "ds_read_b64_tr_b16 v[236:237], v19 offset:36864\n"
    "ds_read_b64_tr_b16 v[238:239], v19 offset:37376\n"
    "v_cndmask_b32 v240, v240, v241, s[80:81]\n"
    "v_cndmask_b32 v242, v242, v243, s[80:81]\n"
    "v_cndmask_b32 v240, v240, v242, s[82:83]\n"
    "v_and_b32 v241, s77, v240\n"
    "v_sub_f32 v241, v240, v241\n"
    "v_and_b32 v242, s77, v241\n"
    "v_sub_f32 v243, v241, v242\n"
    "v_lshrrev_b32 v240, 16, v240\n"
    "v_or_b32 v242, v240, v242\n"
    "v_lshrrev_b32 v243, 16, v243\n"
    "ds_write_b64 v21, v[242:243]\n"
    "v_mov_b32 v24, s21\n"
    "v_mov_b32 v25, 1\n"
    "s_mov_b64 exec, 1\n"
    "ds_add_u32 v24, v25\n"
    "s_mov_b64 exec, -1\n"
    "v_mov_b32 v242, 0\n"
    "v_mov_b32 v243, 0\n"
    "v_xor_b32 v22, 512, v23\n"
    "v_mov_b32 v24, 122388\n"
    "v_mov_b32 v25, 1\n"
    "s_mov_b64 exec, 1\n"
    "ds_add_u32 v24, v25\n"
    "s_mov_b64 exec, -1\n"
    "s_waitcnt lgkmcnt(13)\n"
    "v_mfma_f32_32x32x16_bf16 v[144:159], v[216:219], v[52:55], v[144:159]\n"
    "s_waitcnt lgkmcnt(11)\n"
    "v_mfma_f32_32x32x16_bf16 v[160:175], v[220:223], v[52:55], v[160:175]\n"
    "v_mov_b32 v13, 122432\n"
    "ds_read_b32 v12, v13\n"
    "s_waitcnt lgkmcnt(10)\n"
    "v_mfma_f32_32x32x16_bf16 v[144:159], v[224:227], v[56:59], v[144:159]\n"
    "s_waitcnt lgkmcnt(8)\n"
    "v_mfma_f32_32x32x16_bf16 v[160:175], v[228:231], v[56:59], v[160:175]\n"
    "v_mov_b32 v9, s21\n"
    "ds_read_b32 v8, v9\n"
    "ds_read_b64 v[240:241], v23\n"
    "ds_read_b64 v[244:245], v22\n"
    "s_waitcnt lgkmcnt(9)\n"
    "v_mfma_f32_32x32x16_bf16 v[144:159], v[232:235], v[60:63], v[144:159]\n"
    "s_waitcnt lgkmcnt(7)\n"
    "v_mfma_f32_32x32x16_bf16 v[160:175], v[236:239], v[60:63], v[160:175]\n"
    "buffer_load_dword v11, v30, s[24:27], 0 offen sc1\n"
    "s_waitcnt lgkmcnt(2)\n"
    "v_readfirstlane_b32 s75, v8\n"
    "s_nop 0\n"
    "s_cmp_ge_u32 s75, 4\n"
    "s_cbranch_scc1 cok7_%=\n"
    "s_mov_b32 s79, 0x100000\n"
    "csp8_%=:\n"
    "ds_read_b32 v8, v9\n"
    "s_sub_u32 s79, s79, 1\n"
    "s_cbranch_scc1 cok7_%=\n"
    "s_sleep 1\n"
    "s_waitcnt lgkmcnt(0)\n"
    "v_readfirstlane_b32 s75, v8\n"
    "s_nop 0\n"
    "s_cmp_ge_u32 s75, 4\n"
    "s_cbranch_scc0 csp8_%=\n"
    "ds_read_b64 v[240:241], v23\n"
    "ds_read_b64 v[244:245], v22\n"
    "s_waitcnt lgkmcnt(0)\n"
    "cok7_%=:\n"
    "s_waitcnt lgkmcnt(1)\n"
    "v_mfma_f32_32x32x16_bf16 v[144:159], v[240:243], v[248:251], v[144:159]\n"
    "s_waitcnt lgkmcnt(0)\n"
    "v_mfma_f32_32x32x16_bf16 v[160:175], v[244:247], v[248:251], v[160:175]\n"
    "s_setprio 3\n"
    "v_readfirstlane_b32 s75, v12\n"
    "s_nop 0\n"
    "s_cmp_ge_u32 s75, 4\n"
    "s_cbranch_scc1 cok9_%=\n"
    "s_mov_b32 s79, 0x100000\n"
    "csp10_%=:\n"
    "ds_read_b32 v12, v13\n"
    "s_sub_u32 s79, s79, 1\n"
    "s_cbranch_scc1 cok9_%=\n"
    "s_sleep 1\n"
    "s_waitcnt lgkmcnt(0)\n"
    "v_readfirstlane_b32 s75, v12\n"
    "s_nop 0\n"
    "s_cmp_ge_u32 s75, 4\n"
    "s_cbranch_scc0 csp10_%=\n"
    "cok9_%=:\n"
    "v_add_u32 v27, s67, v15\n"
    "s_nop 3\n"
    "v_mul_f32 v208, v144, v144\n"
    "v_mul_f32 v209, v145, v145\n"
    "v_mul_f32 v210, v146, v146\n"
    "v_mul_f32 v211, v147, v147\n"
    "v_mul_f32 v212, v148, v148\n"
    "v_mul_f32 v213, v149, v149\n"
    "v_mul_f32 v214, v150, v150\n"
    "v_mul_f32 v215, v151, v151\n"
    "v_fma_f32 v208, v208, v253, v252\n"
    "v_fma_f32 v209, v209, v253, v252\n"
    "v_fma_f32 v210, v210, v253, v252\n"
    "v_fma_f32 v211, v211, v253, v252\n"
    "v_fma_f32 v212, v212, v253, v252\n"
    "v_fma_f32 v213, v213, v253, v252\n"
    "v_fma_f32 v214, v214, v253, v252\n"
    "v_fma_f32 v215, v215, v253, v252\n"
    "v_mul_f32 v208, v208, v144\n"
    "v_mul_f32 v209, v209, v145\n"
    "v_mul_f32 v210, v210, v146\n"
    "v_mul_f32 v211, v211, v147\n"
    "v_mul_f32 v212, v212, v148\n"
    "v_mul_f32 v213, v213, v149\n"
    "v_mul_f32 v214, v214, v150\n"
    "v_mul_f32 v215, v215, v151\n"
    "v_exp_f32 v208, v208\n"
    "v_mul_f32 v152, v144, v152\n"
    "v_exp_f32 v209, v209\n"
    "v_mul_f32 v153, v145, v153\n"
    "v_exp_f32 v210, v210\n"
    "v_mul_f32 v154, v146, v154\n"
    "v_exp_f32 v211, v211\n"
    "v_mul_f32 v155, v147, v155\n"
    "v_exp_f32 v212, v212\n"
    "v_mul_f32 v156, v148, v156\n"
    "v_exp_f32 v213, v213\n"
    "v_mul_f32 v157, v149, v157\n"
    "v_exp_f32 v214, v214\n"
    "v_mul_f32 v158, v150, v158\n"
    "v_exp_f32 v215, v215\n"
    "v_mul_f32 v159, v151, v159\n"
    "v_fma_f32 v208, v208, v254, v254\n"
    "v_fma_f32 v209, v209, v254, v254\n"
    "v_fma_f32 v210, v210, v254, v254\n"
    "v_fma_f32 v211, v211, v254, v254\n"
    "v_fma_f32 v212, v212, v254, v254\n"
    "v_fma_f32 v213, v213, v254, v254\n"
    "v_fma_f32 v214, v214, v254, v254\n"
    "v_fma_f32 v215, v215, v254, v254\n"
    "v_rcp_f32 v208, v208\n"
    "v_rcp_f32 v209, v209\n"
    "v_rcp_f32 v210, v210\n"
    "v_rcp_f32 v211, v211\n"
    "v_rcp_f32 v212, v212\n"
    "v_rcp_f32 v213, v213\n"
    "v_rcp_f32 v214, v214\n"
    "v_rcp_f32 v215, v215\n"
    "v_mul_f32 v152, v152, v208\n"
    "v_mul_f32 v153, v153, v209\n"
    "v_mul_f32 v154, v154, v210\n"
    "v_mul_f32 v155, v155, v211\n"
    "v_mul_f32 v156, v156, v212\n"
    "v_mul_f32 v157, v157, v213\n"
    "v_mul_f32 v158, v158, v214\n"
    "v_mul_f32 v159, v159, v215\n"
    "v_cvt_pk_bf16_f32 v216, v152, v153\n"
    "v_cvt_pk_bf16_f32 v217, v154, v155\n"
    "v_cvt_pk_bf16_f32 v218, v156, v157\n"
    "v_cvt_pk_bf16_f32 v219, v158, v159\n"
    "ds_write_b128 v27, v[216:219]\n"
    "s_waitcnt vmcnt(0)\n"
    "v_cmp_ne_u32 vcc, s69, v11\n"
    "s_mov_b32 s79, 0x40000\n"
    "s_cmp_eq_u64 vcc, 0\n"
    "s_cbranch_scc1 pok11_%=\n"
    "psp12_%=:\n"
    "buffer_load_dword v11, v30, s[24:27], 0 offen sc1\n"
    "s_sub_u32 s79, s79, 1\n"
    "s_cbranch_scc1 pok11_%=\n"
    "s_waitcnt vmcnt(0)\n"
    "v_cmp_ne_u32 vcc, s69, v11\n"
    "s_sleep 1\n"
    "s_cmp_eq_u64 vcc, 0\n"
    "s_cbranch_scc0 psp12_%=\n"
    "pok11_%=:\n"
    "v_add_u32 v8, s61, v15\n"
    "v_add_u32 v9, s19, v15\n"
    "s_mov_b32 s73, s16\n"
    "buffer_load_dwordx4 v[64:67], v15, s[24:27], s18 offen sc1\n"
    "buffer_load_dwordx4 v[68:71], v15, s[24:27], s18 offen offset:1024 sc1\n"
    "buffer_load_dwordx4 v[96:99], v8, s[24:27], s73 offen sc1\n"
    "buffer_load_dwordx4 v[100:103], v8, s[24:27], s73 offen offset:1024 sc1\n"
    "s_xor_b32 s75, s18, 131072\n"
    "buffer_load_dwordx4 v[72:75], v15, s[24:27], s75 offen sc1\n"
    "buffer_load_dwordx4 v[76:79], v15, s[24:27], s75 offen offset:1024 sc1\n"
    "s_xor_b32 s75, s18, 262144\n"
    "buffer_load_dwordx4 v[80:83], v15, s[24:27], s75 offen sc1\n"
    "buffer_load_dwordx4 v[84:87], v15, s[24:27], s75 offen offset:1024 sc1\n"
    "s_xor_b32 s75, s18, 393216\n"
    "buffer_load_dwordx4 v[88:91], v15, s[24:27], s75 offen sc1\n"
    "buffer_load_dwordx4 v[92:95], v15, s[24:27], s75 offen offset:1024 sc1\n"
    "buffer_load_dwordx4 v[128:131], v9, s[24:27], s73 offen sc1\n"
    "buffer_load_dwordx4 v[132:135], v9, s[24:27], s73 offen offset:1024 sc1\n"
    "s_add_u32 s73, s73, 131072\n"
    "buffer_load_dwordx4 v[104:107], v8, s[24:27], s73 offen sc1\n"
    "buffer_load_dwordx4 v[108:111], v8, s[24:27], s73 offen offset:1024 sc1\n"
    "buffer_load_dwordx4 v[136:139], v9, s[24:27], s73 offen sc1\n"
    "buffer_load_dwordx4 v[140:143], v9, s[24:27], s73 offen offset:1024 sc1\n"
    "s_add_u32 s73, s73, 131072\n"
    "buffer_load_dwordx4 v[112:115], v8, s[24:27], s73 offen sc1\n"
    "buffer_load_dwordx4 v[116:119], v8, s[24:27], s73 offen offset:1024 sc1\n"
    "buffer_load_dwordx4 v[224:227], v9, s[24:27], s73 offen sc1\n"
    "buffer_load_dwordx4 v[228:231], v9, s[24:27], s73 offen offset:1024 sc1\n"
    "s_add_u32 s73, s73, 131072\n"
    "buffer_load_dwordx4 v[120:123], v8, s[24:27], s73 offen sc1\n"
    "buffer_load_dwordx4 v[124:127], v8, s[24:27], s73 offen offset:1024 sc1\n"
    "v_mul_f32 v208, v160, v160\n"
    "v_mul_f32 v209, v161, v161\n"
    "v_mul_f32 v210, v162, v162\n"
    "v_mul_f32 v211, v163, v163\n"
    "v_mul_f32 v212, v164, v164\n"
    "v_mul_f32 v213, v165, v165\n"
    "v_mul_f32 v214, v166, v166\n"
    "v_mul_f32 v215, v167, v167\n"
    "v_fma_f32 v208, v208, v253, v252\n"
    "v_fma_f32 v209, v209, v253, v252\n"
    "v_fma_f32 v210, v210, v253, v252\n"
    "v_fma_f32 v211, v211, v253, v252\n"
    "v_fma_f32 v212, v212, v253, v252\n"
    "v_fma_f32 v213, v213, v253, v252\n"
    "v_fma_f32 v214, v214, v253, v252\n"
    "v_fma_f32 v215, v215, v253, v252\n"
    "v_mul_f32 v208, v208, v160\n"
    "v_mul_f32 v209, v209, v161\n"
    "v_mul_f32 v210, v210, v162\n"
    "v_mul_f32 v211, v211, v163\n"
    "v_mul_f32 v212, v212, v164\n"
    "v_mul_f32 v213, v213, v165\n"
    "v_mul_f32 v214, v214, v166\n"
    "v_mul_f32 v215, v215, v167\n"
    "v_exp_f32 v208, v208\n"
    "v_mul_f32 v168, v160, v168\n"
    "v_exp_f32 v209, v209\n"
    "v_mul_f32 v169, v161, v169\n"
    "v_exp_f32 v210, v210\n"
    "v_mul_f32 v170, v162, v170\n"
    "v_exp_f32 v211, v211\n"
    "v_mul_f32 v171, v163, v171\n"
    "v_exp_f32 v212, v212\n"
    "v_mul_f32 v172, v164, v172\n"
    "v_exp_f32 v213, v213\n"
    "v_mul_f32 v173, v165, v173\n"
    "v_exp_f32 v214, v214\n"
    "v_mul_f32 v174, v166, v174\n"
    "v_exp_f32 v215, v215\n"
    "v_mul_f32 v175, v167, v175\n"
    "v_fma_f32 v208, v208, v254, v254\n"
    "v_fma_f32 v209, v209, v254, v254\n"
    "v_fma_f32 v210, v210, v254, v254\n"
    "v_fma_f32 v211, v211, v254, v254\n"
    "v_fma_f32 v212, v212, v254, v254\n"
    "v_fma_f32 v213, v213, v254, v254\n"
    "v_fma_f32 v214, v214, v254, v254\n"
    "v_fma_f32 v215, v215, v254, v254\n"
    "v_rcp_f32 v208, v208\n"
    "v_rcp_f32 v209, v209\n"
    "v_rcp_f32 v210, v210\n"
    "v_rcp_f32 v211, v211\n"
    "v_rcp_f32 v212, v212\n"
    "v_rcp_f32 v213, v213\n"
    "v_rcp_f32 v214, v214\n"
    "v_rcp_f32 v215, v215\n"
    "v_mul_f32 v168, v168, v208\n"
    "v_mul_f32 v169, v169, v209\n"
    "v_mul_f32 v170, v170, v210\n"
    "v_mul_f32 v171, v171, v211\n"
    "v_mul_f32 v172, v172, v212\n"
    "v_mul_f32 v173, v173, v213\n"
    "v_mul_f32 v174, v174, v214\n"
    "v_mul_f32 v175, v175, v215\n"
    "v_cvt_pk_bf16_f32 v220, v168, v169\n"
    "v_cvt_pk_bf16_f32 v221, v170, v171\n"
    "v_cvt_pk_bf16_f32 v222, v172, v173\n"
    "v_cvt_pk_bf16_f32 v223, v174, v175\n"
    "ds_write_b128 v27, v[220:223] offset:1024\n"
    "v_mov_b32 v24, 122412\n"
    "v_mov_b32 v25, 1\n"
    "s_mov_b64 exec, 1\n"
    "ds_add_u32 v24, v25\n"
    "s_mov_b64 exec, -1\n"
    "v_mov_b32 v32, 0\n"
    "v_mov_b32 v33, 0\n"
    "v_mov_b32 v34, 0\n"
    "v_mov_b32 v35, 0\n"
    "v_mov_b32 v36, 0\n"
    "v_mov_b32 v37, 0\n"
    "v_mov_b32 v38, 0\n"
    "v_mov_b32 v39, 0\n"
    "v_mov_b32 v40, 0\n"
    "v_mov_b32 v41, 0\n"
    "v_mov_b32 v42, 0\n"
    "v_mov_b32 v43, 0\n"
    "v_mov_b32 v44, 0\n"
    "v_mov_b32 v45, 0\n"
    "v_mov_b32 v46, 0\n"
    "v_mov_b32 v47, 0\n"
    "v_mov_b32 v48, 0\n"
    "v_mov_b32 v49, 0\n"
    "v_mov_b32 v50, 0\n"
    "v_mov_b32 v51, 0\n"
    "v_mov_b32 v52, 0\n"
    "v_mov_b32 v53, 0\n"
    "v_mov_b32 v54, 0\n"
    "v_mov_b32 v55, 0\n"
    "v_mov_b32 v56, 0\n"
    "v_mov_b32 v57, 0\n"
    "v_mov_b32 v58, 0\n"
    "v_mov_b32 v59, 0\n"
    "v_mov_b32 v60, 0\n"
    "v_mov_b32 v61, 0\n"
    "v_mov_b32 v62, 0\n"
    "v_mov_b32 v63, 0\n"
    "v_add_u32 v0, s66, v15\n"
    "v_xor_b32 v1, 4096, v0\n"
    "v_xor_b32 v2, 8192, v0\n"
    "v_xor_b32 v3, 12288, v0\n"
    "v_mov_b32 v4, 122400\n"
    "v_mov_b32 v5, 1\n"
    "s_mov_b32 s56, 4\n"
    "s_movk_i32 s59, 257\n"
    "s_setprio 1\n"
    "s_cmp_eq_u32 s20, 0\n"
    "s_cselect_b32 s80, 0, -1\n"
    "s_mov_b32 s81, 0\n"
    "s_mov_b32 s82, 0\n"
    "s_cselect_b32 s83, -1, 0\n"
    "s_mov_b32 s84, s24\n"
    "s_mov_b32 s85, s25\n"
    "s_mov_b32 s86, s26\n"
    "s_mov_b32 s87, s27\n"
    "v_mov_b32 v160, 0\n"
    "v_mov_b32 v161, 0\n"
    "v_mov_b32 v162, 0\n"
    "v_mov_b32 v163, 0\n"
    "v_mov_b32 v164, 0\n"
    "v_mov_b32 v165, 0\n"
    "v_mov_b32 v166, 0\n"
    "v_mov_b32 v167, 0\n"
    "v_mov_b32 v232, 0\n"
    "v_mov_b32 v233, 0\n"
    "v_mov_b32 v234, 0\n"
    "v_mov_b32 v235, 0\n"
    "v_mov_b32 v236, 0\n"
    "v_mov_b32 v237, 0\n"
    "v_mov_b32 v238, 0\n"
    "v_mov_b32 v239, 0\n"
    "v_mov_b32 v158, 0\n"
    "v_mov_b32 v159, 0\n"
    "s_waitcnt vmcnt(21)\n"
    "v_cndmask_b32 v64, v64, 0, s[80:81]\n"
    "v_cndmask_b32 v65, v65, 0, s[80:81]\n"
    "s_waitcnt vmcnt(20)\n"
    "v_cndmask_b32 v70, v70, 0, s[82:83]\n"
    "v_cndmask_b32 v71, v71, 0, s[82:83]\n"
    "s_waitcnt vmcnt(19)\n"
    "v_mfma_f32_32x32x16_bf16 v[208:223], v[96:99], v[64:67], 0\n"
    "s_waitcnt vmcnt(18)\n"
    "v_mfma_f32_32x32x16_bf16 v[208:223], v[100:103], v[68:71], v[208:223]\n"
    "s_waitcnt vmcnt(17)\n"
    "v_cndmask_b32 v72, v72, 0, s[80:81]\n"
    "v_cndmask_b32 v73, v73, 0, s[80:81]\n"
    "s_waitcnt vmcnt(16)\n"
    "v_cndmask_b32 v78, v78, 0, s[82:83]\n"
    "v_cndmask_b32 v79, v79, 0, s[82:83]\n"
    "s_waitcnt vmcnt(15)\n"
    "v_cndmask_b32 v80, v80, 0, s[80:81]\n"
    "v_cndmask_b32 v81, v81, 0, s[80:81]\n"
    "s_waitcnt vmcnt(14)\n"
    "v_cndmask_b32 v86, v86, 0, s[82:83]\n"
    "v_cndmask_b32 v87, v87, 0, s[82:83]\n"
    "s_waitcnt vmcnt(13)\n"
    "v_cndmask_b32 v88, v88, 0, s[80:81]\n"
    "v_cndmask_b32 v89, v89, 0, s[80:81]\n"
    "s_waitcnt vmcnt(12)\n"
    "v_cndmask_b32 v94, v94, 0, s[82:83]\n"
    "v_cndmask_b32 v95, v95, 0, s[82:83]\n"
    "s_waitcnt vmcnt(10)\n"
    "s_mov_b32 s79, 0\n"
    "v_mov_b32 v12, 122388\n"
    "attn_%=:\n"
    "v_mfma_f32_32x32x16_bf16 v[48:63], v[232:235], v[160:163], v[48:63]\n"
    "v_exp_f32 v208, v208\n"
    "v_cvt_pk_bf16_f32 v167, v158, v159\n"
    "v_exp_f32 v209, v209\n"
    "v_exp_f32 v210, v210\n"
    "v_mfma_f32_32x32x16_bf16 v[48:63], v[236:239], v[164:167], v[48:63]\n"
    "v_exp_f32 v211, v211\n"
    "v_cvt_pk_bf16_f32 v160, v208, v209\n"
    "v_exp_f32 v212, v212\n"
    "v_cvt_pk_bf16_f32 v161, v210, v211\n"
    "v_mfma_f32_32x32x16_bf16 v[144:159], v[96:99], v[72:75], 0\n"
    "v_exp_f32 v213, v213\n"
    "v_exp_f32 v214, v214\n"
    "v_exp_f32 v215, v215\n"
    "v_mfma_f32_32x32x16_bf16 v[144:159], v[100:103], v[76:79], v[144:159]\n"
    "buffer_load_dwordx4 v[232:235], v9, s[24:27], s73 offen sc1\n"
    "buffer_load_dwordx4 v[236:239], v9, s[24:27], s73 offen offset:1024 sc1\n"
    "v_exp_f32 v216, v216\n"
    "v_cvt_pk_bf16_f32 v162, v212, v213\n"
    "v_exp_f32 v217, v217\n"
    "v_cvt_pk_bf16_f32 v163, v214, v215\n"
    "v_exp_f32 v218, v218\n"
    "v_exp_f32 v219, v219\n"
    "v_cvt_pk_bf16_f32 v164, v216, v217\n"
    "v_exp_f32 v220, v220\n"
    "v_exp_f32 v221, v221\n"
    "v_cvt_pk_bf16_f32 v165, v218, v219\n"
    "v_exp_f32 v222, v222\n"
    "v_exp_f32 v223, v223\n"
    "v_cvt_pk_bf16_f32 v166, v220, v221\n"
    "v_mfma_f32_32x32x16_bf16 v[176:191], v[128:131], v[160:163], v[176:191]\n"
    "s_add_u32 s73, s73, 131072\n"
    "v_exp_f32 v144, v144\n"
    "v_cvt_pk_bf16_f32 v167, v222, v223\n"
    "v_exp_f32 v145, v145\n"
    "v_exp_f32 v146, v146\n"
    "v_mfma_f32_32x32x16_bf16 v[176:191], v[132:135], v[164:167], v[176:191]\n"
    "v_exp_f32 v147, v147\n"
    "v_cvt_pk_bf16_f32 v160, v144, v145\n"
    "v_exp_f32 v148, v148\n"
    "v_cvt_pk_bf16_f32 v161, v146, v147\n"
    "v_mfma_f32_32x32x16_bf16 v[208:223], v[96:99], v[80:83], 0\n"
    "v_exp_f32 v149, v149\n"
    "v_exp_f32 v150, v150\n"
    "v_exp_f32 v151, v151\n"
    "v_mfma_f32_32x32x16_bf16 v[208:223], v[100:103], v[84:87], v[208:223]\n"
    "v_exp_f32 v152, v152\n"
    "v_cvt_pk_bf16_f32 v162, v148, v149\n"
    "v_exp_f32 v153, v153\n"
    "v_cvt_pk_bf16_f32 v163, v150, v151\n"
    "v_exp_f32 v154, v154\n"
    "v_exp_f32 v155, v155\n"
    "v_cvt_pk_bf16_f32 v164, v152, v153\n"
    "v_exp_f32 v156, v156\n"
    "v_exp_f32 v157, v157\n"
    "v_cvt_pk_bf16_f32 v165, v154, v155\n"
    "v_exp_f32 v158, v158\n"
    "v_exp_f32 v159, v159\n"
    "v_cvt_pk_bf16_f32 v166, v156, v157\n"
    "v_mfma_f32_32x32x16_bf16 v[192:207], v[128:131], v[160:163], v[192:207]\n"
    "v_exp_f32 v208, v208\n"
    "v_cvt_pk_bf16_f32 v167, v158, v159\n"
    "v_exp_f32 v209, v209\n"
    "v_exp_f32 v210, v210\n"
    "v_mfma_f32_32x32x16_bf16 v[192:207], v[132:135], v[164:167], v[192:207]\n"
    "v_exp_f32 v211, v211\n"
    "v_cvt_pk_bf16_f32 v160, v208, v209\n"
    "v_exp_f32 v212, v212\n"
    "v_cvt_pk_bf16_f32 v161, v210, v211\n"
    "v_mfma_f32_32x32x16_bf16 v[144:159], v[96:99], v[88:91], 0\n"
    "v_exp_f32 v213, v213\n"
    "v_exp_f32 v214, v214\n"
    "v_exp_f32 v215, v215\n"
    "v_mfma_f32_32x32x16_bf16 v[144:159], v[100:103], v[92:95], v[144:159]\n"
    "buffer_load_dwordx4 v[96:99], v8, s[84:87], s73 offen sc1\n"
    "buffer_load_dwordx4 v[100:103], v8, s[84:87], s73 offen offset:1024 sc1\n"
    "s_waitcnt vmcnt(10)\n"
    "v_exp_f32 v216, v216\n"
    "v_cvt_pk_bf16_f32 v162, v212, v213\n"
    "v_exp_f32 v217, v217\n"
    "v_cvt_pk_bf16_f32 v163, v214, v215\n"
    "v_exp_f32 v218, v218\n"
    "v_exp_f32 v219, v219\n"
    "v_cvt_pk_bf16_f32 v164, v216, v217\n"
    "v_exp_f32 v220, v220\n"
    "v_exp_f32 v221, v221\n"
    "v_cvt_pk_bf16_f32 v165, v218, v219\n"
    "v_exp_f32 v222, v222\n"
    "v_exp_f32 v223, v223\n"
    "v_cvt_pk_bf16_f32 v166, v220, v221\n"
    "v_mfma_f32_32x32x16_bf16 v[32:47], v[128:131], v[160:163], v[32:47]\n"
    "v_exp_f32 v144, v144\n"
    "v_cvt_pk_bf16_f32 v167, v222, v223\n"
    "v_exp_f32 v145, v145\n"
    "v_exp_f32 v146, v146\n"
    "v_mfma_f32_32x32x16_bf16 v[32:47], v[132:135], v[164:167], v[32:47]\n"
    "v_exp_f32 v147, v147\n"
    "v_cvt_pk_bf16_f32 v160, v144, v145\n"
    "v_exp_f32 v148, v148\n"
    "v_cvt_pk_bf16_f32 v161, v146, v147\n"
    "v_mfma_f32_32x32x16_bf16 v[208:223], v[104:107], v[64:67], 0\n"
    "v_exp_f32 v149, v149\n"
    "v_exp_f32 v150, v150\n"
    "v_exp_f32 v151, v151\n"
    "v_mfma_f32_32x32x16_bf16 v[208:223], v[108:111], v[68:71], v[208:223]\n"
    "v_exp_f32 v152, v152\n"
    "v_cvt_pk_bf16_f32 v162, v148, v149\n"
    "v_exp_f32 v153, v153\n"
    "v_cvt_pk_bf16_f32 v163, v150, v151\n"
    "v_exp_f32 v154, v154\n"
    "v_exp_f32 v155, v155\n"
    "v_cvt_pk_bf16_f32 v164, v152, v153\n"
    "v_exp_f32 v156, v156\n"
    "v_exp_f32 v157, v157\n"
    "v_cvt_pk_bf16_f32 v165, v154, v155\n"
    "v_exp_f32 v158, v158\n"
    "v_exp_f32 v159, v159\n"
    "v_cvt_pk_bf16_f32 v166, v156, v157\n"
    "v_mfma_f32_32x32x16_bf16 v[48:63], v[128:131], v[160:163], v[48:63]\n"
    "v_exp_f32 v208, v208\n"
    "v_cvt_pk_bf16_f32 v167, v158, v159\n"
    "v_exp_f32 v209, v209\n"
    "v_exp_f32 v210, v210\n"
    "v_mfma_f32_32x32x16_bf16 v[48:63], v[132:135], v[164:167], v[48:63]\n"
    "v_exp_f32 v211, v211\n"
    "v_cvt_pk_bf16_f32 v160, v208, v209\n"
    "v_exp_f32 v212, v212\n"
    "v_cvt_pk_bf16_f32 v161, v210, v211\n"
    "v_mfma_f32_32x32x16_bf16 v[144:159], v[104:107], v[72:75], 0\n"
    "v_exp_f32 v213, v213\n"
    "v_exp_f32 v214, v214\n"
    "v_exp_f32 v215, v215\n"
    "v_mfma_f32_32x32x16_bf16 v[144:159], v[108:111], v[76:79], v[144:159]\n"
    "buffer_load_dwordx4 v[128:131], v9, s[84:87], s73 offen sc1\n"
    "buffer_load_dwordx4 v[132:135], v9, s[84:87], s73 offen offset:1024 sc1\n"
    "v_exp_f32 v216, v216\n"
    "v_cvt_pk_bf16_f32 v162, v212, v213\n"
    "v_exp_f32 v217, v217\n"
    "v_cvt_pk_bf16_f32 v163, v214, v215\n"
    "v_exp_f32 v218, v218\n"
    "v_exp_f32 v219, v219\n"
    "v_cvt_pk_bf16_f32 v164, v216, v217\n"
    "v_exp_f32 v220, v220\n"
    "v_exp_f32 v221, v221\n"
    "v_cvt_pk_bf16_f32 v165, v218, v219\n"
    "v_exp_f32 v222, v222\n"
    "v_exp_f32 v223, v223\n"
    "v_cvt_pk_bf16_f32 v166, v220, v221\n"
    "v_mfma_f32_32x32x16_bf16 v[176:191], v[136:139], v[160:163], v[176:191]\n"
    "s_add_u32 s73, s73, 131072\n"
    "v_exp_f32 v144, v144\n"
    "v_cvt_pk_bf16_f32 v167, v222, v223\n"
    "v_exp_f32 v145, v145\n"
    "v_exp_f32 v146, v146\n"
    "v_mfma_f32_32x32x16_bf16 v[176:191], v[140:143], v[164:167], v[176:191]\n"
    "v_exp_f32 v147, v147\n"
    "v_cvt_pk_bf16_f32 v160, v144, v145\n"
    "v_exp_f32 v148, v148\n"
    "v_cvt_pk_bf16_f32 v161, v146, v147\n"
    "v_mfma_f32_32x32x16_bf16 v[208:223], v[104:107], v[80:83], 0\n"
    "v_exp_f32 v149, v149\n"
    "v_exp_f32 v150, v150\n"
    "v_exp_f32 v151, v151\n"
    "v_mfma_f32_32x32x16_bf16 v[208:223], v[108:111], v[84:87], v[208:223]\n"
    "v_exp_f32 v152, v152\n"
    "v_cvt_pk_bf16_f32 v162, v148, v149\n"
    "v_exp_f32 v153, v153\n"
    "v_cvt_pk_bf16_f32 v163, v150, v151\n"
    "v_exp_f32 v154, v154\n"
    "v_exp_f32 v155, v155\n"
    "v_cvt_pk_bf16_f32 v164, v152, v153\n"
    "v_exp_f32 v156, v156\n"
    "v_exp_f32 v157, v157\n"
    "v_cvt_pk_bf16_f32 v165, v154, v155\n"
    "v_exp_f32 v158, v158\n"
    "v_exp_f32 v159, v159\n"
    "v_cvt_pk_bf16_f32 v166, v156, v157\n"
    "v_mfma_f32_32x32x16_bf16 v[192:207], v[136:139], v[160:163], v[192:207]\n"
    "v_exp_f32 v208, v208\n"
    "v_cvt_pk_bf16_f32 v167, v158, v159\n"
    "v_exp_f32 v209, v209\n"
    "v_exp_f32 v210, v210\n"
    "v_mfma_f32_32x32x16_bf16 v[192:207], v[140:143], v[164:167], v[192:207]\n"
    "v_exp_f32 v211, v211\n"
    "v_cvt_pk_bf16_f32 v160, v208, v209\n"
    "v_exp_f32 v212, v212\n"
    "v_cvt_pk_bf16_f32 v161, v210, v211\n"
    "v_mfma_f32_32x32x16_bf16 v[144:159], v[104:107], v[88:91], 0\n"
    "v_exp_f32 v213, v213\n"
    "v_exp_f32 v214, v214\n"
    "v_exp_f32 v215, v215\n"
    "v_mfma_f32_32x32x16_bf16 v[144:159], v[108:111], v[92:95], v[144:159]\n"
    "buffer_load_dwordx4 v[104:107], v8, s[84:87], s73 offen sc1\n"
    "buffer_load_dwordx4 v[108:111], v8, s[84:87], s73 offen offset:1024 sc1\n"
    "s_waitcnt vmcnt(10)\n"
    "v_exp_f32 v216, v216\n"
    "v_cvt_pk_bf16_f32 v162, v212, v213\n"
    "v_exp_f32 v217, v217\n"
    "v_cvt_pk_bf16_f32 v163, v214, v215\n"
    "v_exp_f32 v218, v218\n"
    "v_exp_f32 v219, v219\n"
    "v_cvt_pk_bf16_f32 v164, v216, v217\n"
    "v_exp_f32 v220, v220\n"
    "v_exp_f32 v221, v221\n"
    "v_cvt_pk_bf16_f32 v165, v218, v219\n"
    "v_exp_f32 v222, v222\n"
    "v_exp_f32 v223, v223\n"
    "v_cvt_pk_bf16_f32 v166, v220, v221\n"
    "v_mfma_f32_32x32x16_bf16 v[32:47], v[136:139], v[160:163], v[32:47]\n"
    "v_exp_f32 v144, v144\n"
    "v_cvt_pk_bf16_f32 v167, v222, v223\n"
    "v_exp_f32 v145, v145\n"
    "v_exp_f32 v146, v146\n"
    "v_mfma_f32_32x32x16_bf16 v[32:47], v[140:143], v[164:167], v[32:47]\n"
    "v_exp_f32 v147, v147\n"
    "v_cvt_pk_bf16_f32 v160, v144, v145\n"
    "v_exp_f32 v148, v148\n"
    "v_cvt_pk_bf16_f32 v161, v146, v147\n"
    "v_mfma_f32_32x32x16_bf16 v[208:223], v[112:115], v[64:67], 0\n"
    "v_exp_f32 v149, v149\n"
    "v_exp_f32 v150, v150\n"
    "v_exp_f32 v151, v151\n"
    "v_mfma_f32_32x32x16_bf16 v[208:223], v[116:119], v[68:71], v[208:223]\n"
    "v_exp_f32 v152, v152\n"
    "v_cvt_pk_bf16_f32 v162, v148, v149\n"
    "v_exp_f32 v153, v153\n"
    "v_cvt_pk_bf16_f32 v163, v150, v151\n"
    "v_exp_f32 v154, v154\n"
    "v_exp_f32 v155, v155\n"
    "v_cvt_pk_bf16_f32 v164, v152, v153\n"
    "v_exp_f32 v156, v156\n"
    "v_exp_f32 v157, v157\n"
    "v_cvt_pk_bf16_f32 v165, v154, v155\n"
    "v_exp_f32 v158, v158\n"
    "v_exp_f32 v159, v159\n"
    "v_cvt_pk_bf16_f32 v166, v156, v157\n"
    "v_mfma_f32_32x32x16_bf16 v[48:63], v[136:139], v[160:163], v[48:63]\n"
    "v_exp_f32 v208, v208\n"
    "v_cvt_pk_bf16_f32 v167, v158, v159\n"
    "v_exp_f32 v209, v209\n"
    "v_exp_f32 v210, v210\n"
    "v_mfma_f32_32x32x16_bf16 v[48:63], v[140:143], v[164:167], v[48:63]\n"
    "v_exp_f32 v211, v211\n"
    "v_cvt_pk_bf16_f32 v160, v208, v209\n"
    "v_exp_f32 v212, v212\n"
    "v_cvt_pk_bf16_f32 v161, v210, v211\n"
    "v_mfma_f32_32x32x16_bf16 v[144:159], v[112:115], v[72:75], 0\n"
    "v_exp_f32 v213, v213\n"
    "v_exp_f32 v214, v214\n"
    "v_exp_f32 v215, v215\n"
    "v_mfma_f32_32x32x16_bf16 v[144:159], v[116:119], v[76:79], v[144:159]\n"
    "buffer_load_dwordx4 v[136:139], v9, s[84:87], s73 offen sc1\n"
    "buffer_load_dwordx4 v[140:143], v9, s[84:87], s73 offen offset:1024 sc1\n"
    "v_exp_f32 v216, v216\n"
    "v_cvt_pk_bf16_f32 v162, v212, v213\n"
    "v_exp_f32 v217, v217\n"
    "v_cvt_pk_bf16_f32 v163, v214, v215\n"
    "v_exp_f32 v218, v218\n"
    "v_exp_f32 v219, v219\n"
    "v_cvt_pk_bf16_f32 v164, v216, v217\n"
    "v_exp_f32 v220, v220\n"
    "v_exp_f32 v221, v221\n"
    "v_cvt_pk_bf16_f32 v165, v218, v219\n"
    "v_exp_f32 v222, v222\n"
    "v_exp_f32 v223, v223\n"
    "v_cvt_pk_bf16_f32 v166, v220, v221\n"
    "v_mfma_f32_32x32x16_bf16 v[176:191], v[224:227], v[160:163], v[176:191]\n"
    "s_add_u32 s73, s73, 131072\n"
    "v_exp_f32 v144, v144\n"
    "v_cvt_pk_bf16_f32 v167, v222, v223\n"
    "v_exp_f32 v145, v145\n"
    "v_exp_f32 v146, v146\n"
    "v_mfma_f32_32x32x16_bf16 v[176:191], v[228:231], v[164:167], v[176:191]\n"
    "v_exp_f32 v147, v147\n"
    "v_cvt_pk_bf16_f32 v160, v144, v145\n"
    "v_exp_f32 v148, v148\n"
    "v_cvt_pk_bf16_f32 v161, v146, v147\n"
    "v_mfma_f32_32x32x16_bf16 v[208:223], v[112:115], v[80:83], 0\n"
    "v_exp_f32 v149, v149\n"
    "v_exp_f32 v150, v150\n"
    "v_exp_f32 v151, v151\n"
    "v_mfma_f32_32x32x16_bf16 v[208:223], v[116:119], v[84:87], v[208:223]\n"
    "v_exp_f32 v152, v152\n"
    "v_cvt_pk_bf16_f32 v162, v148, v149\n"
    "v_exp_f32 v153, v153\n"
    "v_cvt_pk_bf16_f32 v163, v150, v151\n"
    "v_exp_f32 v154, v154\n"
    "v_exp_f32 v155, v155\n"
    "v_cvt_pk_bf16_f32 v164, v152, v153\n"
    "v_exp_f32 v156, v156\n"
    "v_exp_f32 v157, v157\n"
    "v_cvt_pk_bf16_f32 v165, v154, v155\n"
    "v_exp_f32 v158, v158\n"
    "v_exp_f32 v159, v159\n"
    "v_cvt_pk_bf16_f32 v166, v156, v157\n"
    "v_mfma_f32_32x32x16_bf16 v[192:207], v[224:227], v[160:163], v[192:207]\n"
    "v_exp_f32 v208, v208\n"
    "v_cvt_pk_bf16_f32 v167, v158, v159\n"
    "v_exp_f32 v209, v209\n"
    "v_exp_f32 v210, v210\n"
    "v_mfma_f32_32x32x16_bf16 v[192:207], v[228:231], v[164:167], v[192:207]\n"
    "v_exp_f32 v211, v211\n"
    "v_cvt_pk_bf16_f32 v160, v208, v209\n"
    "v_exp_f32 v212, v212\n"
    "v_cvt_pk_bf16_f32 v161, v210, v211\n"
    "v_mfma_f32_32x32x16_bf16 v[144:159], v[112:115], v[88:91], 0\n"
    "v_exp_f32 v213, v213\n"
    "v_exp_f32 v214, v214\n"
    "v_exp_f32 v215, v215\n"
    "v_mfma_f32_32x32x16_bf16 v[144:159], v[116:119], v[92:95], v[144:159]\n"
    "buffer_load_dwordx4 v[112:115], v8, s[84:87], s73 offen sc1\n"
    "buffer_load_dwordx4 v[116:119], v8, s[84:87], s73 offen offset:1024 sc1\n"
    "s_waitcnt vmcnt(10)\n"
    "v_exp_f32 v216, v216\n"
    "v_cvt_pk_bf16_f32 v162, v212, v213\n"
    "v_exp_f32 v217, v217\n"
    "v_cvt_pk_bf16_f32 v163, v214, v215\n"
    "v_exp_f32 v218, v218\n"
    "v_exp_f32 v219, v219\n"
    "v_cvt_pk_bf16_f32 v164, v216, v217\n"
    "v_exp_f32 v220, v220\n"
    "v_exp_f32 v221, v221\n"
    "v_cvt_pk_bf16_f32 v165, v218, v219\n"
    "v_exp_f32 v222, v222\n"
    "v_exp_f32 v223, v223\n"
    "v_cvt_pk_bf16_f32 v166, v220, v221\n"
    "v_mfma_f32_32x32x16_bf16 v[32:47], v[224:227], v[160:163], v[32:47]\n"
    "v_exp_f32 v144, v144\n"
    "v_cvt_pk_bf16_f32 v167, v222, v223\n"
    "v_exp_f32 v145, v145\n"
    "v_exp_f32 v146, v146\n"
    "v_mfma_f32_32x32x16_bf16 v[32:47], v[228:231], v[164:167], v[32:47]\n"
    "v_exp_f32 v147, v147\n"
    "v_cvt_pk_bf16_f32 v160, v144, v145\n"
    "v_exp_f32 v148, v148\n"
    "v_cvt_pk_bf16_f32 v161, v146, v147\n"
    "v_mfma_f32_32x32x16_bf16 v[208:223], v[120:123], v[64:67], 0\n"
    "v_exp_f32 v149, v149\n"
    "v_exp_f32 v150, v150\n"
    "v_exp_f32 v151, v151\n"
    "v_mfma_f32_32x32x16_bf16 v[208:223], v[124:127], v[68:71], v[208:223]\n"
    "v_exp_f32 v152, v152\n"
    "v_cvt_pk_bf16_f32 v162, v148, v149\n"
    "v_exp_f32 v153, v153\n"
    "v_cvt_pk_bf16_f32 v163, v150, v151\n"
    "v_exp_f32 v154, v154\n"
    "v_exp_f32 v155, v155\n"
    "v_cvt_pk_bf16_f32 v164, v152, v153\n"
    "v_exp_f32 v156, v156\n"
    "v_exp_f32 v157, v157\n"
    "v_cvt_pk_bf16_f32 v165, v154, v155\n"
    "v_exp_f32 v158, v158\n"
    "v_exp_f32 v159, v159\n"
    "v_cvt_pk_bf16_f32 v166, v156, v157\n"
    "v_mfma_f32_32x32x16_bf16 v[48:63], v[224:227], v[160:163], v[48:63]\n"
    "v_exp_f32 v208, v208\n"
    "v_cvt_pk_bf16_f32 v167, v158, v159\n"
    "v_exp_f32 v209, v209\n"
    "v_exp_f32 v210, v210\n"
    "v_mfma_f32_32x32x16_bf16 v[48:63], v[228:231], v[164:167], v[48:63]\n"
    "v_exp_f32 v211, v211\n"
    "v_cvt_pk_bf16_f32 v160, v208, v209\n"
    "v_exp_f32 v212, v212\n"
    "v_cvt_pk_bf16_f32 v161, v210, v211\n"
    "v_mfma_f32_32x32x16_bf16 v[144:159], v[120:123], v[72:75], 0\n"
    "v_exp_f32 v213, v213\n"
    "v_exp_f32 v214, v214\n"
    "v_exp_f32 v215, v215\n"
    "v_mfma_f32_32x32x16_bf16 v[144:159], v[124:127], v[76:79], v[144:159]\n"
    "buffer_load_dwordx4 v[224:227], v9, s[84:87], s73 offen sc1\n"
    "buffer_load_dwordx4 v[228:231], v9, s[84:87], s73 offen offset:1024 sc1\n"
    "v_exp_f32 v216, v216\n"
    "v_cvt_pk_bf16_f32 v162, v212, v213\n"
    "v_exp_f32 v217, v217\n"
    "v_cvt_pk_bf16_f32 v163, v214, v215\n"
    "v_exp_f32 v218, v218\n"
    "v_exp_f32 v219, v219\n"
    "v_cvt_pk_bf16_f32 v164, v216, v217\n"
    "v_exp_f32 v220, v220\n"
    "v_exp_f32 v221, v221\n"
    "v_cvt_pk_bf16_f32 v165, v218, v219\n"
    "v_exp_f32 v222, v222\n"
    "v_exp_f32 v223, v223\n"
    "v_cvt_pk_bf16_f32 v166, v220, v221\n"
    "v_mfma_f32_32x32x16_bf16 v[176:191], v[232:235], v[160:163], v[176:191]\n"
    "s_add_u32 s73, s73, 131072\n"
    "ds_read_b32 v11, v12\n"
    "v_exp_f32 v144, v144\n"
    "v_cvt_pk_bf16_f32 v167, v222, v223\n"
    "v_exp_f32 v145, v145\n"
    "v_exp_f32 v146, v146\n"
    "v_mfma_f32_32x32x16_bf16 v[176:191], v[236:239], v[164:167], v[176:191]\n"
    "v_exp_f32 v147, v147\n"
    "v_cvt_pk_bf16_f32 v160, v144, v145\n"
    "v_exp_f32 v148, v148\n"
    "v_cvt_pk_bf16_f32 v161, v146, v147\n"
    "v_mfma_f32_32x32x16_bf16 v[208:223], v[120:123], v[80:83], 0\n"
    "v_exp_f32 v149, v149\n"
    "v_exp_f32 v150, v150\n"
    "v_exp_f32 v151, v151\n"
    "v_mfma_f32_32x32x16_bf16 v[208:223], v[124:127], v[84:87], v[208:223]\n"
    "v_exp_f32 v152, v152\n"
    "v_cvt_pk_bf16_f32 v162, v148, v149\n"
    "v_exp_f32 v153, v153\n"
    "v_cvt_pk_bf16_f32 v163, v150, v151\n"
    "v_exp_f32 v154, v154\n"
    "v_exp_f32 v155, v155\n"
    "v_cvt_pk_bf16_f32 v164, v152, v153\n"
    "v_exp_f32 v156, v156\n"
    "v_exp_f32 v157, v157\n"
    "v_cvt_pk_bf16_f32 v165, v154, v155\n"
    "v_exp_f32 v158, v158\n"
    "v_exp_f32 v159, v159\n"
    "v_cvt_pk_bf16_f32 v166, v156, v157\n"
    "v_mfma_f32_32x32x16_bf16 v[192:207], v[232:235], v[160:163], v[192:207]\n"
    "v_exp_f32 v208, v208\n"
    "v_cvt_pk_bf16_f32 v167, v158, v159\n"
    "v_exp_f32 v209, v209\n"
    "v_exp_f32 v210, v210\n"
    "v_mfma_f32_32x32x16_bf16 v[192:207], v[236:239], v[164:167], v[192:207]\n"
    "v_exp_f32 v211, v211\n"
    "v_cvt_pk_bf16_f32 v160, v208, v209\n"
    "v_exp_f32 v212, v212\n"
    "v_cvt_pk_bf16_f32 v161, v210, v211\n"
    "v_mfma_f32_32x32x16_bf16 v[144:159], v[120:123], v[88:91], 0\n"
    "v_exp_f32 v213, v213\n"
    "v_exp_f32 v214, v214\n"
    "v_exp_f32 v215, v215\n"
    "v_mfma_f32_32x32x16_bf16 v[144:159], v[124:127], v[92:95], v[144:159]\n"
    "buffer_load_dwordx4 v[120:123], v8, s[84:87], s73 offen sc1\n"
    "buffer_load_dwordx4 v[124:127], v8, s[84:87], s73 offen offset:1024 sc1\n"
    "s_waitcnt vmcnt(10)\n"
    "v_exp_f32 v216, v216\n"
    "v_cvt_pk_bf16_f32 v162, v212, v213\n"
    "v_exp_f32 v217, v217\n"
    "v_cvt_pk_bf16_f32 v163, v214, v215\n"
    "v_exp_f32 v218, v218\n"
    "v_exp_f32 v219, v219\n"
    "v_cvt_pk_bf16_f32 v164, v216, v217\n"
    "v_exp_f32 v220, v220\n"
    "v_exp_f32 v221, v221\n"
    "v_cvt_pk_bf16_f32 v165, v218, v219\n"
    "v_exp_f32 v222, v222\n"
    "v_exp_f32 v223, v223\n"
    "v_cvt_pk_bf16_f32 v166, v220, v221\n"
    "v_mfma_f32_32x32x16_bf16 v[32:47], v[232:235], v[160:163], v[32:47]\n"
    "v_exp_f32 v144, v144\n"
    "v_cvt_pk_bf16_f32 v167, v222, v223\n"
    "v_exp_f32 v145, v145\n"
    "v_exp_f32 v146, v146\n"
    "v_mfma_f32_32x32x16_bf16 v[32:47], v[236:239], v[164:167], v[32:47]\n"
    "v_exp_f32 v147, v147\n"
    "v_cvt_pk_bf16_f32 v160, v144, v145\n"
    "v_exp_f32 v148, v148\n"
    "v_cvt_pk_bf16_f32 v161, v146, v147\n"
    "v_mfma_f32_32x32x16_bf16 v[208:223], v[96:99], v[64:67], 0\n"
    "v_exp_f32 v149, v149\n"
    "v_exp_f32 v150, v150\n"
    "v_exp_f32 v151, v151\n"
    "v_mfma_f32_32x32x16_bf16 v[208:223], v[100:103], v[68:71], v[208:223]\n"
    "v_exp_f32 v152, v152\n"
    "v_cvt_pk_bf16_f32 v162, v148, v149\n"
    "v_exp_f32 v153, v153\n"
    "v_cvt_pk_bf16_f32 v163, v150, v151\n"
    "v_exp_f32 v154, v154\n"
    "v_exp_f32 v155, v155\n"
    "v_cvt_pk_bf16_f32 v164, v152, v153\n"
    "v_exp_f32 v156, v156\n"
    "v_exp_f32 v157, v157\n"
    "v_cvt_pk_bf16_f32 v165, v154, v155\n"
    "v_exp_f32 v158, v158\n"
    "v_exp_f32 v159, v159\n"
    "v_cvt_pk_bf16_f32 v166, v156, v157\n"
    "s_add_u32 s79, s79, 1\n"
    "s_cmp_eq_u32 s79, 3\n"
    "s_cselect_b32 s86, 0, s26\n"
    "s_cmp_lg_u32 s79, 4\n"
    "s_cbranch_scc1 attn_%=\n"
    "v_mfma_f32_32x32x16_bf16 v[48:63], v[232:235], v[160:163], v[48:63]\n"
    "v_cvt_pk_bf16_f32 v167, v158, v159\n"
    "s_nop 1\n"
    "v_mfma_f32_32x32x16_bf16 v[48:63], v[236:239], v[164:167], v[48:63]\n"
    "s_waitcnt lgkmcnt(0)\n"
    "v_readfirstlane_b32 s75, v11\n"
    "s_nop 0\n"
    "s_cmp_ge_u32 s75, 12\n"
    "s_cbranch_scc1 cok13_%=\n"
    "s_mov_b32 s79, 0x100000\n"
    "csp14_%=:\n"
    "ds_read_b32 v11, v12\n"
    "s_sub_u32 s79, s79, 1\n"
    "s_cbranch_scc1 cok13_%=\n"
    "s_sleep 1\n"
    "s_waitcnt lgkmcnt(0)\n"
    "v_readfirstlane_b32 s75, v11\n"
    "s_nop 0\n"
    "s_cmp_ge_u32 s75, 12\n"
    "s_cbranch_scc0 csp14_%=\n"
    "cok13_%=:\n"
    "red_%=:\n"
    "ds_write_b128 v1, v[192:195] offset:0\n"
    "ds_write_b128 v1, v[196:199] offset:1024\n"
    "ds_write_b128 v1, v[200:203] offset:2048\n"
    "ds_write_b128 v1, v[204:207] offset:3072\n"
    "ds_write_b128 v2, v[32:35] offset:16384\n"
    "ds_write_b128 v2, v[36:39] offset:17408\n"
    "ds_write_b128 v2, v[40:43] offset:18432\n"
    "ds_write_b128 v2, v[44:47] offset:19456\n"
    "ds_write_b128 v3, v[48:51] offset:32768\n"
    "ds_write_b128 v3, v[52:55] offset:33792\n"
    "ds_write_b128 v3, v[56:59] offset:34816\n"
    "ds_write_b128 v3, v[60:63] offset:35840\n"
    "s_waitcnt lgkmcnt(0)\n"
    "s_mov_b64 exec, 1\n"
    "ds_add_u32 v4, v5\n"
    "s_mov_b64 exec, -1\n"
    "s_mov_b32 s79, 0x100000\n"
    "sp15_%=:\n"
    "ds_read_b32 v6, v4\n"
    "s_sub_u32 s79, s79, 1\n"
    "s_cbranch_scc1 so16_%=\n"
    "s_waitcnt lgkmcnt(0)\n"
    "v_readfirstlane_b32 s75, v6\n"
    "s_nop 0\n"
    "s_cmp_ge_u32 s75, s56\n"
    "s_cbranch_scc0 sp15_%=\n"
    "so16_%=:\n"
    "s_waitcnt vmcnt(0)\n"
    "ds_read_b128 v[220:223], v0 offset:3072\n"
    "ds_read_b128 v[236:239], v0 offset:19456\n"
    "ds_read_b128 v[252:255], v0 offset:35840\n"
    "ds_read_b128 v[216:219], v0 offset:2048\n"
    "ds_read_b128 v[232:235], v0 offset:18432\n"
    "ds_read_b128 v[248:251], v0 offset:34816\n"
    "ds_read_b128 v[208:211], v0 offset:0\n"
    "ds_read_b128 v[224:227], v0 offset:16384\n"
    "ds_read_b128 v[240:243], v0 offset:32768\n"
    "ds_read_b128 v[212:215], v0 offset:1024\n"
    "ds_read_b128 v[228:231], v0 offset:17408\n"
    "ds_read_b128 v[244:247], v0 offset:33792\n"
    "s_waitcnt lgkmcnt(11)\n"
    "v_add_f32 v188, v188, v220\n"
    "v_add_f32 v189, v189, v221\n"
    "v_add_f32 v190, v190, v222\n"
    "v_add_f32 v191, v191, v223\n"
    "s_waitcnt lgkmcnt(9)\n"
    "v_add_f32 v236, v236, v252\n"
    "v_add_f32 v237, v237, v253\n"
    "v_add_f32 v238, v238, v254\n"
    "v_add_f32 v239, v239, v255\n"
    "v_add_f32 v188, v188, v236\n"
    "v_add_f32 v189, v189, v237\n"
    "v_add_f32 v190, v190, v238\n"
    "v_add_f32 v191, v191, v239\n"
    "v_mov_b32 v6, v188\n"
    "v_mov_b32 v7, v188\n"
    "s_waitcnt lgkmcnt(8)\n"
    "v_add_f32 v184, v184, v216\n"
    "v_add_f32 v185, v185, v217\n"
    "v_add_f32 v186, v186, v218\n"
    "v_add_f32 v187, v187, v219\n"
    "v_permlane32_swap_b32 v6, v7\n"
    "s_waitcnt lgkmcnt(6)\n"
    "v_add_f32 v232, v232, v248\n"
    "v_add_f32 v233, v233, v249\n"
    "v_add_f32 v234, v234, v250\n"
    "v_add_f32 v235, v235, v251\n"
    "v_add_f32 v184, v184, v232\n"
    "v_add_f32 v185, v185, v233\n"
    "v_add_f32 v186, v186, v234\n"
    "v_add_f32 v187, v187, v235\n"
    "v_rcp_f32 v6, v7\n"
    "v_subrev_u32 v7, 0x2b800000, v7\n"
    "v_cmp_lt_u32 vcc, 0x28000000, v7\n"
    "v_mul_f32 v188, v6, v188\n"
    "v_mul_f32 v189, v6, v189\n"
    "v_mul_f32 v190, v6, v190\n"
    "v_mul_f32 v191, v6, v191\n"
    "v_cvt_pk_bf16_f32 v166, v188, v189\n"
    "v_cvt_pk_bf16_f32 v167, v190, v191\n"
    "v_min_u32 v166, -2, v166\n"
    "v_min_u32 v167, -2, v167\n"
    "s_cmp_lg_u64 vcc, 0\n"
    "s_cselect_b32 s78, s59, 1\n"
    "s_cmp_eq_u32 s78, 1\n"
    "s_cselect_b32 s74, s71, 0x40000000\n"
    "s_cselect_b32 s76, s68, 0x40000000\n"
    "s_cselect_b32 s71, 0x40000000, s71\n"
    "s_cselect_b32 s68, 0x40000000, s68\n"
    "v_mul_f32 v184, v6, v184\n"
    "v_mul_f32 v185, v6, v185\n"
    "v_mul_f32 v186, v6, v186\n"
    "v_mul_f32 v187, v6, v187\n"
    "v_cvt_pk_bf16_f32 v164, v184, v185\n"
    "v_cvt_pk_bf16_f32 v165, v186, v187\n"
    "v_min_u32 v164, -2, v164\n"
    "v_min_u32 v165, -2, v165\n"
    "s_mov_b32 exec_hi, 0\n"
    "buffer_store_dwordx4 v[164:167], v15, s[24:27], s74 offen offset:1024 sc1\n"
    "s_not_b64 exec, exec\n"
    "buffer_store_dwordx2 v[164:165], v15, s[24:27], s76 offen offset:1024 sc1\n"
    "s_mov_b32 exec_lo, -1\n"
    "s_waitcnt lgkmcnt(5)\n"
    "v_add_f32 v176, v176, v208\n"
    "v_add_f32 v177, v177, v209\n"
    "v_add_f32 v178, v178, v210\n"
    "v_add_f32 v179, v179, v211\n"
    "s_waitcnt lgkmcnt(3)\n"
    "v_add_f32 v224, v224, v240\n"
    "v_add_f32 v225, v225, v241\n"
    "v_add_f32 v226, v226, v242\n"
    "v_add_f32 v227, v227, v243\n"
    "v_add_f32 v176, v176, v224\n"
    "v_add_f32 v177, v177, v225\n"
    "v_add_f32 v178, v178, v226\n"
    "v_add_f32 v179, v179, v227\n"
    "v_mul_f32 v176, v6, v176\n"
    "v_mul_f32 v177, v6, v177\n"
    "v_mul_f32 v178, v6, v178\n"
    "v_mul_f32 v179, v6, v179\n"
    "v_cvt_pk_bf16_f32 v160, v176, v177\n"
    "v_cvt_pk_bf16_f32 v161, v178, v179\n"
    "v_min_u32 v160, -2, v160\n"
    "v_min_u32 v161, -2, v161\n"
    "s_waitcnt lgkmcnt(2)\n"
    "v_add_f32 v180, v180, v212\n"
    "v_add_f32 v181, v181, v213\n"
    "v_add_f32 v182, v182, v214\n"
    "v_add_f32 v183, v183, v215\n"
    "s_waitcnt lgkmcnt(0)\n"
    "v_add_f32 v228, v228, v244\n"
    "v_add_f32 v229, v229, v245\n"
    "v_add_f32 v230, v230, v246\n"
    "v_add_f32 v231, v231, v247\n"
    "v_add_f32 v180, v180, v228\n"
    "v_add_f32 v181, v181, v229\n"
    "v_add_f32 v182, v182, v230\n"
    "v_add_f32 v183, v183, v231\n"
    "v_mul_f32 v180, v6, v180\n"
    "v_mul_f32 v181, v6, v181\n"
    "v_mul_f32 v182, v6, v182\n"
    "v_mul_f32 v183, v6, v183\n"
    "v_cvt_pk_bf16_f32 v162, v180, v181\n"
    "v_cvt_pk_bf16_f32 v163, v182, v183\n"
    "v_min_u32 v162, -2, v162\n"
    "v_min_u32 v163, -2, v163\n"
    "buffer_store_dwordx4 v[160:163], v15, s[24:27], s74 offen sc1\n"
    "v_mov_b32 v7, s78\n"
    "s_mov_b64 exec, 1\n"
    "ds_add_u32 v4, v7 offset:4\n"
    "s_mov_b64 exec, -1\n"
    "s_cmp_eq_u32 s59, 1\n"
    "s_cbranch_scc1 adone_%=\n"
    "v_add_u32 v7, 4, v4\n"
    "s_mov_b32 s79, 0x100000\n"
    "sp17_%=:\n"
    "ds_read_b32 v6, v7\n"
    "s_sub_u32 s79, s79, 1\n"
    "s_cbranch_scc1 so18_%=\n"
    "s_waitcnt lgkmcnt(0)\n"
    "v_readfirstlane_b32 s75, v6\n"
    "s_nop 0\n"
    "s_and_b32 s78, s75, 255\n"
    "s_cmp_ge_u32 s78, 4\n"
    "s_cbranch_scc0 sp17_%=\n"
    "so18_%=:\n"
    "s_cmp_ge_u32 s75, 256\n"
    "s_cbranch_scc1 slow_%=\n"
    "adone_%=:\n"
    "s_endpgm\n"
    ".p2align 6\n"
    "g_%=:\n"
    "s_bfe_u32 s7, s3, 0x20006\n"
    "s_lshl_b32 s4, s22, 7\n"
    "s_lshl4_add_u32 s5, s7, s4\n"
    "s_load_dwordx4 s[84:87], s[0:1], s5 offset:144\n"
    "s_load_dwordx4 s[88:91], s[0:1], s4 offset:128\n"
    "s_load_dwordx4 s[92:95], s[0:1], s4 offset:208\n"
    "s_lshl_b32 s4, s22, 6\n"
    "s_load_dwordx16 s[48:63], s[0:1], s4 offset:2176\n"
    "s_load_dwordx4 s[32:35], s[0:1], 32\n"
    "s_lshr_b32 s9, s2, 3\n"
    "s_cmp_lt_u32 s9, 28\n"
    "s_cselect_b32 s4, 48, 80\n"
    "s_cselect_b32 s20, 1, 0\n"
    "s_load_dwordx8 s[36:43], s[0:1], s4\n"
    "s_bfe_u32 s7, s3, 0x20006\n"
    "s_lshl2_add_u32 s8, s23, s7\n"
    "s_and_b32 s12, s7, 1\n"
    "s_lshr_b32 s13, s7, 1\n"
    "v_and_b32 v67, 44, v64\n"
    "v_lshlrev_b32 v67, 5, v67\n"
    "v_and_b32 v73, 8, v64\n"
    "v_lshl_or_b32 v67, v73, 3, v67\n"
    "v_and_b32 v74, 16, v64\n"
    "v_lshl_or_b32 v67, v74, 1, v67\n"
    "v_and_b32 v68, 3, v64\n"
    "v_mul_u32_u24 v73, 20, v68\n"
    "v_and_or_b32 v75, v73, 24, v67\n"
    "v_lshl_or_b32 v76, v74, 9, v75\n"
    "v_and_b32 v69, 0x1f0, v15\n"
    "v_lshrrev_b32 v70, 5, v64\n"
    "v_bfe_u32 v73, v64, 1, 3\n"
    "v_xor_b32 v73, v73, v70\n"
    "v_lshlrev_b32 v73, 4, v73\n"
    "v_lshl_or_b32 v73, v69, 3, v73\n"
    "s_lshl_b32 s4, s7, 12\n"
    "s_lshl_b32 s5, s13, 5\n"
    "s_or_b32 s4, s4, s5\n"
    "v_xor_b32 v0, s4, v73\n"
    "v_xor_b32 v1, 32, v0\n"
    "v_xor_b32 v2, 64, v0\n"
    "v_xor_b32 v3, 0x60, v0\n"
    "v_and_b32 v71, 1, v64\n"
    "v_lshlrev_b32 v71, 8, v71\n"
    "v_and_b32 v73, 48, v64\n"
    "v_lshl_or_b32 v71, v73, 2, v71\n"
    "v_and_b32 v73, 6, v64\n"
    "v_lshl_or_b32 v71, v73, 3, v71\n"
    "v_lshl_or_b32 v72, v70, 3, v69\n"
    "s_lshl_b32 s14, s13, 11\n"
    "s_lshl_b32 s4, s12, 5\n"
    "s_or_b32 s4, s4, s14\n"
    "s_or_b32 s4, s4, 64\n"
    "v_xor_b32 v16, s4, v76\n"
    "v_xor_b32 v17, 32, v16\n"
    "v_xor_b32 v18, 0x800, v16\n"
    "v_xor_b32 v19, 0x800, v17\n"
    "s_lshl_b32 s4, s12, 9\n"
    "s_add_u32 s4, s4, 118784\n"
    "s_lshl3_add_u32 s5, s13, s4\n"
    "v_add_u32 v21, s5, v71\n"
    "v_add_u32 v23, s4, v72\n"
    "s_lshl_b32 s15, s7, 9\n"
    "s_lshl_b32 s4, s8, 8\n"
    "s_add_u32 s4, s4, 16777216\n"
    "v_lshrrev_b32 v73, 1, v69\n"
    "v_add_u32 v25, s4, v73\n"
    "s_lshl_b32 s4, s12, 10\n"
    "v_add_u32 v27, s4, v15\n"
    "v_lshrrev_b32 v73, 4, v64\n"
    "v_xor_b32 v73, v73, v64\n"
    "v_and_b32 v73, 1, v73\n"
    "v_add_u32 v73, -1, v73\n"
    "v_and_b32 v244, 0x3f803f80, v73\n"
    "s_mov_b32 s21, 122384\n"
    "s_mul_i32 s68, s8, 114688\n"
    "s_lshl_b32 s4, s22, 12\n"
    "s_add_u32 s68, s68, s4\n"
    "s_add_u32 s68, s68, 8439808\n"
    "s_lshl_b32 s4, s23, 7\n"
    "s_lshl2_add_u32 s71, s22, s4\n"
    "s_lshl_b32 s71, s71, 5\n"
    "s_add_u32 s71, s71, 16924672\n"
    "v_lshrrev_b32 v66, 3, v64\n"
    "v_and_b32 v73, 0x70, v15\n"
    "v_lshlrev_b32 v65, 2, v64\n"
    "v_and_b32 v65, 64, v65\n"
    "v_xor_b32 v65, v65, v73\n"
    "s_mul_i32 s10, s9, 37\n"
    "s_lshr_b32 s10, s10, 8\n"
    "s_mul_i32 s4, s10, 7\n"
    "s_sub_u32 s9, s9, s4\n"
    "s_lshl_b32 s9, s9, 7\n"
    "s_and_b32 s4, s2, 7\n"
    "s_lshl2_add_u32 s10, s4, s10\n"
    "v_add_u32 v76, 0x6000, v76\n"
    "s_lshl_b32 s4, s7, 5\n"
    "s_or_b32 s4, s4, s14\n"
    "v_xor_b32 v4, s4, v76\n"
    "v_xor_b32 v5, 32, v4\n"
    "v_xor_b32 v6, 64, v4\n"
    "v_xor_b32 v7, 0x60, v4\n"
    "v_xor_b32 v8, 0x800, v4\n"
    "v_xor_b32 v9, 0x800, v5\n"
    "v_xor_b32 v10, 0x800, v6\n"
    "v_xor_b32 v11, 0x800, v7\n"
    "v_mul_u32_u24 v12, 896, v66\n"
    "v_add3_u32 v12, v12, s9, v65\n"
    "s_lshl_b32 s11, s13, 6\n"
    "v_xor_b32 v13, s11, v75\n"
    "v_xor_b32 v14, 64, v13\n"
    "s_add_u32 s4, s15, 114688\n"
    "v_add_u32 v20, s4, v71\n"
    "v_add_u32 v22, s4, v72\n"
    "s_lshl_b32 s4, s7, 10\n"
    "v_add_u32 v26, s4, v15\n"
    "s_mul_i32 s66, s7, 10240\n"
    "s_add_u32 s66, s66, 122880\n"
    "s_lshl_b32 s67, s7, 14\n"
    "s_add_u32 s67, s67, 49152\n"
    "v_lshrrev_b32 v73, 2, v64\n"
    "v_lshlrev_b32 v73, 9, v73\n"
    "v_lshl_add_u32 v73, v68, 4, v73\n"
    "v_cmp_eq_u32 vcc, 0, v68\n"
    "v_mov_b32 v74, 0x3f803f80\n"
    "s_nop 0\n"
    "v_cndmask_b32 v80, 0, v74, vcc\n"
    "v_cndmask_b32 v81, 0, v74, vcc\n"
    "v_cndmask_b32 v82, 0, v74, vcc\n"
    "v_cndmask_b32 v83, 0, v74, vcc\n"
    "s_lshl_b32 s4, s8, 17\n"
    "s_waitcnt lgkmcnt(0)\n"
    "s_add_u32 s4, s4, s48\n"
    "s_mov_b32 exec_hi, 0xffff\n"
    "buffer_store_dwordx4 v[80:83], v73, s[24:27], s4 offen sc1\n"
    "s_mov_b32 exec_hi, -1\n"
    "s_mov_b32 s4, 17055744\n"
    "buffer_load_dword v48, off, s[24:27], s4 sc1\n"
    "s_mul_i32 s4, s8, s63\n"
    "s_add_u32 s56, s56, s4\n"
    "s_mul_i32 s4, s23, s62\n"
    "s_add_u32 s70, s61, s4\n"
    "s_lshr_b32 s4, s10, 1\n"
    "s_cmp_eq_u32 s20, 1\n"
    "s_cselect_b32 s4, s4, s23\n"
    "s_cselect_b32 s6, 8388608, 0x40000000\n"
    "s_lshl_b32 s4, s4, 7\n"
    "s_lshl4_add_u32 s5, s7, s4\n"
    "s_lshl_b32 s5, s5, 5\n"
    "s_add_u32 s5, s5, 16924672\n"
    "v_lshl_add_u32 v28, v68, 7, s5\n"
    "s_mul_i32 s5, s7, 12\n"
    "s_add_u32 s5, s5, s4\n"
    "s_lshl_b32 s5, s5, 5\n"
    "s_add_u32 s5, s5, 16859136\n"
    "v_min_u32 v73, 2, v68\n"
    "v_lshl_add_u32 v30, v73, 7, s5\n"
    "s_lshl1_add_u32 s10, s10, s12\n"
    "s_mul_i32 s4, s10, 114688\n"
    "s_add_u32 s4, s4, s6\n"
    "s_mul_i32 s5, s7, 12288\n"
    "s_add_u32 s16, s4, s5\n"
    "s_lshl_b32 s5, s7, 14\n"
    "s_add_u32 s17, s4, s5\n"
    "s_add_u32 s17, s17, 49152\n"
    "s_mul_i32 s4, s12, 229376\n"
    "s_sub_u32 s64, 114688, s4\n"
    "s_mul_i32 s18, s7, 172032\n"
    "s_mul_i32 s61, s7, 229376\n"
    "s_add_u32 s61, s61, 688128\n"
    "s_mul_i32 s19, s7, 100352\n"
    "s_mul_i32 s65, s10, 28672\n"
    "s_add_u32 s65, s65, s9\n"
    "s_add_u32 s65, s65, s11\n"
    "s_lshl_b32 s63, s10, 17\n"
    "s_mul_i32 s4, s13, 14336\n"
    "s_add_u32 s63, s63, s4\n"
    "s_add_u32 s63, s63, 90112\n"
    "v_mul_u32_u24 v73, 56, v69\n"
    "v_lshl_add_u32 v31, v70, 4, v73\n"
    "s_mov_b32 s77, 0xffff0000\n"
    "s_mov_b32 s80, 0xcccccccc\n"
    "s_mov_b32 s81, 0xcccccccc\n"
    "s_mov_b32 s82, 0xf0f0f0f0\n"
    "s_mov_b32 s83, 0xf0f0f0f0\n"
    "v_mov_b32 v245, v244\n"
    "v_mov_b32 v246, v244\n"
    "v_mov_b32 v247, v244\n"
    "v_mov_b32 v250, 0\n"
    "v_mov_b32 v251, 0\n"
    "s_lshr_b32 s96, s2, 5\n"
    "s_mul_i32 s96, s96, 56\n"
    "s_cmp_lt_u32 s7, 2\n"
    "s_cselect_b32 s97, s92, 3584\n"
    "s_lshl_b32 s98, s22, 7\n"
    "s_add_u32 s98, s98, 1536\n"
    "s_cmp_eq_u32 s7, 0\n"
    "s_cselect_b32 s98, s94, s98\n"
    "s_cmp_eq_u32 s7, 1\n"
    "s_cselect_b32 s98, s95, s98\n"
    "s_mul_i32 s99, s7, 14\n"
    "s_add_u32 s99, s99, s96\n"
    "v_min_u32 v77, 55, v64\n"
    "v_add_u32 v77, s96, v77\n"
    "v_mul_u32_u24 v77, s97, v77\n"
    "v_min_u32 v78, 13, v64\n"
    "v_add_u32 v78, s99, v78\n"
    "v_mul_u32_u24 v78, 2688, v78\n"
    "s_nop 0\n"
    "buffer_load_dword v29, v78, s[88:91], s93 offen\n"
    "s_nop 0\n"
    "buffer_load_dword v29, v77, s[84:87], s98 offen\n"
    "s_lshl2_add_u32 s96, s2, s7\n"
    "s_mul_i32 s97, s96, 1792\n"
    "s_lshl_b32 s96, s96, 9\n"
    "v_min_u32 v77, 13, v64\n"
    "v_lshl_add_u32 v77, v77, 7, s97\n"
    "v_lshl_add_u32 v78, v68, 7, s96\n"
    "s_nop 0\n"
    "buffer_load_dword v29, v77, s[36:39], 0 offen\n"
    "s_nop 0\n"
    "buffer_load_dword v29, v78, s[40:43], 0 offen\n"
    "s_setprio 0\n"
    "s_barrier\n"
    "ds_read_b128 v[96:99], v0 offset:40960\n"
    "ds_read_b64_tr_b16 v[208:209], v4 offset:40960\n"
    "ds_read_b64_tr_b16 v[210:211], v4 offset:41472\n"
    "ds_read_b64_tr_b16 v[212:213], v5 offset:40960\n"
    "ds_read_b64_tr_b16 v[214:215], v5 offset:41472\n"
    "ds_read_b64_tr_b16 v[216:217], v6 offset:40960\n"
    "ds_read_b64_tr_b16 v[218:219], v6 offset:41472\n"
    "ds_read_b64_tr_b16 v[220:221], v7 offset:40960\n"
    "ds_read_b64_tr_b16 v[222:223], v7 offset:41472\n"
    "s_waitcnt lgkmcnt(6)\n"
    "v_mfma_f32_32x32x16_bf16 v[144:159], v[208:211], v[96:99], 0\n"
    "ds_read_b128 v[100:103], v1 offset:40960\n"
    "ds_read_b64_tr_b16 v[224:225], v8 offset:40960\n"
    "ds_read_b64_tr_b16 v[226:227], v8 offset:41472\n"
    "ds_read_b64_tr_b16 v[228:229], v9 offset:40960\n"
    "ds_read_b64_tr_b16 v[230:231], v9 offset:41472\n"
    "v_mfma_f32_16x16x32_bf16 v[240:243], v[208:211], v[244:247], 0\n"
    "s_waitcnt lgkmcnt(9)\n"
    "v_mfma_f32_32x32x16_bf16 v[160:175], v[212:215], v[96:99], 0\n"
    "ds_read_b64_tr_b16 v[232:233], v10 offset:40960\n"
    "ds_read_b64_tr_b16 v[234:235], v10 offset:41472\n"
    "ds_read_b64_tr_b16 v[236:237], v11 offset:40960\n"
    "ds_read_b64_tr_b16 v[238:239], v11 offset:41472\n"
    "s_waitcnt lgkmcnt(11)\n"
    "v_mfma_f32_32x32x16_bf16 v[176:191], v[216:219], v[96:99], 0\n"
    "ds_read_b128 v[104:107], v2 offset:40960\n"
    "ds_read_b64_tr_b16 v[208:209], v4 offset:45056\n"
    "ds_read_b64_tr_b16 v[210:211], v4 offset:45568\n"
    "ds_read_b64_tr_b16 v[212:213], v5 offset:45056\n"
    "ds_read_b64_tr_b16 v[214:215], v5 offset:45568\n"
    "s_waitcnt lgkmcnt(14)\n"
    "v_mfma_f32_32x32x16_bf16 v[192:207], v[220:223], v[96:99], 0\n"
    "ds_read_b64_tr_b16 v[216:217], v6 offset:45056\n"
    "ds_read_b64_tr_b16 v[218:219], v6 offset:45568\n"
    "s_waitcnt lgkmcnt(13)\n"
    "v_mfma_f32_32x32x16_bf16 v[144:159], v[224:227], v[100:103], v[144:159]\n"
    "ds_read_b64_tr_b16 v[220:221], v7 offset:45056\n"
    "ds_read_b64_tr_b16 v[222:223], v7 offset:45568\n"
    "v_mfma_f32_16x16x32_bf16 v[240:243], v[224:227], v[244:247], v[240:243]\n"
    "s_waitcnt lgkmcnt(13)\n"
    "v_mfma_f32_32x32x16_bf16 v[160:175], v[228:231], v[100:103], v[160:175]\n"
    "ds_read_b128 v[108:111], v3 offset:40960\n"
    "ds_read_b64_tr_b16 v[224:225], v8 offset:45056\n"
    "ds_read_b64_tr_b16 v[226:227], v8 offset:45568\n"
    "s_waitcnt lgkmcnt(14)\n"
    "v_mfma_f32_32x32x16_bf16 v[176:191], v[232:235], v[100:103], v[176:191]\n"
    "ds_read_b64_tr_b16 v[228:229], v9 offset:45056\n"
    "ds_read_b64_tr_b16 v[230:231], v9 offset:45568\n"
    "s_waitcnt lgkmcnt(14)\n"
    "v_mfma_f32_32x32x16_bf16 v[192:207], v[236:239], v[100:103], v[192:207]\n"
    "ds_read_b64_tr_b16 v[232:233], v10 offset:45056\n"
    "ds_read_b64_tr_b16 v[234:235], v10 offset:45568\n"
    "s_waitcnt lgkmcnt(13)\n"
    "v_mfma_f32_32x32x16_bf16 v[144:159], v[208:211], v[104:107], v[144:159]\n"
    "ds_read_b64_tr_b16 v[236:237], v11 offset:45056\n"
    "ds_read_b64_tr_b16 v[238:239], v11 offset:45568\n"
    "v_mfma_f32_16x16x32_bf16 v[240:243], v[208:211], v[244:247], v[240:243]\n"
    "s_waitcnt lgkmcnt(13)\n"
    "v_mfma_f32_32x32x16_bf16 v[160:175], v[212:215], v[104:107], v[160:175]\n"
    "v_add_u32 v0, 0x14000, v0\n"
    "v_add_u32 v1, 0x14000, v1\n"
    "v_add_u32 v2, 0x14000, v2\n"
    "v_add_u32 v3, 0x14000, v3\n"
    "v_add_u32 v4, 0x14000, v4\n"
    "s_waitcnt lgkmcnt(11)\n"
    "v_mfma_f32_32x32x16_bf16 v[176:191], v[216:219], v[104:107], v[176:191]\n"
    "v_add_u32 v5, 0x14000, v5\n"
    "v_add_u32 v6, 0x14000, v6\n"
    "v_add_u32 v7, 0x14000, v7\n"
    "v_add_u32 v8, 0x14000, v8\n"
    "v_add_u32 v9, 0x14000, v9\n"
    "s_waitcnt lgkmcnt(9)\n"
    "v_mfma_f32_32x32x16_bf16 v[192:207], v[220:223], v[104:107], v[192:207]\n"
    "s_waitcnt lgkmcnt(0)\n"
    "s_barrier\n"
    "ds_read_b128 v[112:115], v0 offset:0\n"
    "ds_read_b64_tr_b16 v[208:209], v4 offset:0\n"
    "ds_read_b64_tr_b16 v[210:211], v4 offset:512\n"
    "ds_read_b64_tr_b16 v[212:213], v5 offset:0\n"
    "ds_read_b64_tr_b16 v[214:215], v5 offset:512\n"
    "v_mfma_f32_32x32x16_bf16 v[144:159], v[224:227], v[108:111], v[144:159]\n"
    "ds_read_b64_tr_b16 v[216:217], v6 offset:0\n"
    "ds_read_b64_tr_b16 v[218:219], v6 offset:512\n"
    "ds_read_b64_tr_b16 v[220:221], v7 offset:0\n"
    "ds_read_b64_tr_b16 v[222:223], v7 offset:512\n"
    "v_mfma_f32_16x16x32_bf16 v[240:243], v[224:227], v[244:247], v[240:243]\n"
    "v_mfma_f32_32x32x16_bf16 v[160:175], v[228:231], v[108:111], v[160:175]\n"
    "ds_read_b128 v[116:119], v1 offset:0\n"
    "ds_read_b64_tr_b16 v[224:225], v8 offset:0\n"
    "ds_read_b64_tr_b16 v[226:227], v8 offset:512\n"
    "v_mfma_f32_32x32x16_bf16 v[176:191], v[232:235], v[108:111], v[176:191]\n"
    "ds_read_b64_tr_b16 v[228:229], v9 offset:0\n"
    "ds_read_b64_tr_b16 v[230:231], v9 offset:512\n"
    "v_mfma_f32_32x32x16_bf16 v[192:207], v[236:239], v[108:111], v[192:207]\n"
    "v_add_u32 v10, 0x14000, v10\n"
    "ds_read_b64_tr_b16 v[232:233], v10 offset:0\n"
    "ds_read_b64_tr_b16 v[234:235], v10 offset:512\n"
    "s_waitcnt lgkmcnt(13)\n"
    "v_mfma_f32_32x32x16_bf16 v[144:159], v[208:211], v[112:115], v[144:159]\n"
    "v_add_u32 v11, 0x14000, v11\n"
    "ds_read_b64_tr_b16 v[236:237], v11 offset:0\n"
    "ds_read_b64_tr_b16 v[238:239], v11 offset:512\n"
    "v_mfma_f32_16x16x32_bf16 v[240:243], v[208:211], v[244:247], v[240:243]\n"
    "s_waitcnt lgkmcnt(13)\n"
    "v_mfma_f32_32x32x16_bf16 v[160:175], v[212:215], v[112:115], v[160:175]\n"
    "ds_read_b128 v[120:123], v2 offset:0\n"
    "ds_read_b64_tr_b16 v[208:209], v4 offset:4096\n"
    "ds_read_b64_tr_b16 v[210:211], v4 offset:4608\n"
    "s_waitcnt lgkmcnt(14)\n"
    "v_mfma_f32_32x32x16_bf16 v[176:191], v[216:219], v[112:115], v[176:191]\n"
    "ds_read_b64_tr_b16 v[212:213], v5 offset:4096\n"
    "ds_read_b64_tr_b16 v[214:215], v5 offset:4608\n"
    "s_waitcnt lgkmcnt(14)\n"
    "v_mfma_f32_32x32x16_bf16 v[192:207], v[220:223], v[112:115], v[192:207]\n"
    "ds_read_b64_tr_b16 v[216:217], v6 offset:4096\n"
    "ds_read_b64_tr_b16 v[218:219], v6 offset:4608\n"
    "s_waitcnt lgkmcnt(13)\n"
    "v_mfma_f32_32x32x16_bf16 v[144:159], v[224:227], v[116:119], v[144:159]\n"
    "ds_read_b64_tr_b16 v[220:221], v7 offset:4096\n"
    "ds_read_b64_tr_b16 v[222:223], v7 offset:4608\n"
    "v_mfma_f32_16x16x32_bf16 v[240:243], v[224:227], v[244:247], v[240:243]\n"
    "s_waitcnt lgkmcnt(13)\n"
    "v_mfma_f32_32x32x16_bf16 v[160:175], v[228:231], v[116:119], v[160:175]\n"
    "ds_read_b128 v[124:127], v3 offset:0\n"
    "ds_read_b64_tr_b16 v[224:225], v8 offset:4096\n"
    "ds_read_b64_tr_b16 v[226:227], v8 offset:4608\n"
    "s_waitcnt lgkmcnt(14)\n"
    "v_mfma_f32_32x32x16_bf16 v[176:191], v[232:235], v[116:119], v[176:191]\n"
    "ds_read_b64_tr_b16 v[228:229], v9 offset:4096\n"
    "ds_read_b64_tr_b16 v[230:231], v9 offset:4608\n"
    "s_waitcnt lgkmcnt(14)\n"
    "v_mfma_f32_32x32x16_bf16 v[192:207], v[236:239], v[116:119], v[192:207]\n"
    "ds_read_b64_tr_b16 v[232:233], v10 offset:4096\n"
    "ds_read_b64_tr_b16 v[234:235], v10 offset:4608\n"
    "s_waitcnt lgkmcnt(13)\n"
    "v_mfma_f32_32x32x16_bf16 v[144:159], v[208:211], v[120:123], v[144:159]\n"
    "ds_read_b64_tr_b16 v[236:237], v11 offset:4096\n"
    "ds_read_b64_tr_b16 v[238:239], v11 offset:4608\n"
    "v_mfma_f32_16x16x32_bf16 v[240:243], v[208:211], v[244:247], v[240:243]\n"
    "s_waitcnt lgkmcnt(13)\n"
    "v_mfma_f32_32x32x16_bf16 v[160:175], v[212:215], v[120:123], v[160:175]\n"
    "s_waitcnt lgkmcnt(11)\n"
    "v_mfma_f32_32x32x16_bf16 v[176:191], v[216:219], v[120:123], v[176:191]\n"
    "s_waitcnt lgkmcnt(9)\n"
    "v_mfma_f32_32x32x16_bf16 v[192:207], v[220:223], v[120:123], v[192:207]\n"
    "s_waitcnt lgkmcnt(0)\n"
    "s_barrier\n"
    "ds_read_b128 v[128:131], v0 offset:40960\n"
    "ds_read_b64_tr_b16 v[208:209], v4 offset:40960\n"
    "ds_read_b64_tr_b16 v[210:211], v4 offset:41472\n"
    "ds_read_b64_tr_b16 v[212:213], v5 offset:40960\n"
    "ds_read_b64_tr_b16 v[214:215], v5 offset:41472\n"
    "v_mfma_f32_32x32x16_bf16 v[144:159], v[224:227], v[124:127], v[144:159]\n"
    "ds_read_b64_tr_b16 v[216:217], v6 offset:40960\n"
    "ds_read_b64_tr_b16 v[218:219], v6 offset:41472\n"
    "ds_read_b64_tr_b16 v[220:221], v7 offset:40960\n"
    "ds_read_b64_tr_b16 v[222:223], v7 offset:41472\n"
    "v_mfma_f32_16x16x32_bf16 v[240:243], v[224:227], v[244:247], v[240:243]\n"
    "v_mfma_f32_32x32x16_bf16 v[160:175], v[228:231], v[124:127], v[160:175]\n"
    "ds_read_b128 v[132:135], v1 offset:40960\n"
    "ds_read_b64_tr_b16 v[224:225], v8 offset:40960\n"
    "ds_read_b64_tr_b16 v[226:227], v8 offset:41472\n"
    "v_mfma_f32_32x32x16_bf16 v[176:191], v[232:235], v[124:127], v[176:191]\n"
    "ds_read_b64_tr_b16 v[228:229], v9 offset:40960\n"
    "ds_read_b64_tr_b16 v[230:231], v9 offset:41472\n"
    "v_mfma_f32_32x32x16_bf16 v[192:207], v[236:239], v[124:127], v[192:207]\n"
    "ds_read_b64_tr_b16 v[232:233], v10 offset:40960\n"
    "ds_read_b64_tr_b16 v[234:235], v10 offset:41472\n"
    "s_waitcnt lgkmcnt(13)\n"
    "v_mfma_f32_32x32x16_bf16 v[144:159], v[208:211], v[128:131], v[144:159]\n"
    "ds_read_b64_tr_b16 v[236:237], v11 offset:40960\n"
    "ds_read_b64_tr_b16 v[238:239], v11 offset:41472\n"
    "v_mfma_f32_16x16x32_bf16 v[240:243], v[208:211], v[244:247], v[240:243]\n"
    "s_waitcnt lgkmcnt(13)\n"
    "v_mfma_f32_32x32x16_bf16 v[160:175], v[212:215], v[128:131], v[160:175]\n"
    "ds_read_b128 v[136:139], v2 offset:40960\n"
    "ds_read_b64_tr_b16 v[208:209], v4 offset:45056\n"
    "ds_read_b64_tr_b16 v[210:211], v4 offset:45568\n"
    "s_waitcnt lgkmcnt(14)\n"
    "v_mfma_f32_32x32x16_bf16 v[176:191], v[216:219], v[128:131], v[176:191]\n"
    "ds_read_b64_tr_b16 v[212:213], v5 offset:45056\n"
    "ds_read_b64_tr_b16 v[214:215], v5 offset:45568\n"
    "s_waitcnt vmcnt(4)\n"
    "v_readfirstlane_b32 s69, v48\n"
    "s_waitcnt lgkmcnt(14)\n"
    "v_mfma_f32_32x32x16_bf16 v[192:207], v[220:223], v[128:131], v[192:207]\n"
    "ds_read_b64_tr_b16 v[216:217], v6 offset:45056\n"
    "ds_read_b64_tr_b16 v[218:219], v6 offset:45568\n"
    "s_waitcnt lgkmcnt(13)\n"
    "v_mfma_f32_32x32x16_bf16 v[144:159], v[224:227], v[132:135], v[144:159]\n"
    "ds_read_b64_tr_b16 v[220:221], v7 offset:45056\n"
    "ds_read_b64_tr_b16 v[222:223], v7 offset:45568\n"
    "v_mfma_f32_16x16x32_bf16 v[240:243], v[224:227], v[244:247], v[240:243]\n"
    "s_waitcnt lgkmcnt(13)\n"
    "v_mfma_f32_32x32x16_bf16 v[160:175], v[228:231], v[132:135], v[160:175]\n"
    "ds_read_b128 v[140:143], v3 offset:40960\n"
    "ds_read_b64_tr_b16 v[224:225], v8 offset:45056\n"
    "ds_read_b64_tr_b16 v[226:227], v8 offset:45568\n"
    "s_waitcnt lgkmcnt(14)\n"
    "v_mfma_f32_32x32x16_bf16 v[176:191], v[232:235], v[132:135], v[176:191]\n"
    "ds_read_b64_tr_b16 v[228:229], v9 offset:45056\n"
    "ds_read_b64_tr_b16 v[230:231], v9 offset:45568\n"
    "s_waitcnt lgkmcnt(14)\n"
    "v_mfma_f32_32x32x16_bf16 v[192:207], v[236:239], v[132:135], v[192:207]\n"
    "ds_read_b64_tr_b16 v[232:233], v10 offset:45056\n"
    "ds_read_b64_tr_b16 v[234:235], v10 offset:45568\n"
    "s_waitcnt lgkmcnt(13)\n"
    "v_mfma_f32_32x32x16_bf16 v[144:159], v[208:211], v[136:139], v[144:159]\n"
    "ds_read_b64_tr_b16 v[236:237], v11 offset:45056\n"
    "ds_read_b64_tr_b16 v[238:239], v11 offset:45568\n"
    "v_mfma_f32_16x16x32_bf16 v[240:243], v[208:211], v[244:247], v[240:243]\n"
    "s_waitcnt lgkmcnt(13)\n"
    "v_mfma_f32_32x32x16_bf16 v[160:175], v[212:215], v[136:139], v[160:175]\n"
    "v_add_u32 v0, 0xfffec000, v0\n"
    "v_add_u32 v1, 0xfffec000, v1\n"
    "v_add_u32 v2, 0xfffec000, v2\n"
    "v_add_u32 v3, 0xfffec000, v3\n"
    "v_add_u32 v4, 0xfffec000, v4\n"
    "s_add_u32 s69, s69, 1\n"
    "s_waitcnt lgkmcnt(11)\n"
    "v_mfma_f32_32x32x16_bf16 v[176:191], v[216:219], v[136:139], v[176:191]\n"
    "v_add_u32 v5, 0xfffec000, v5\n"
    "v_add_u32 v6, 0xfffec000, v6\n"
    "v_add_u32 v7, 0xfffec000, v7\n"
    "v_add_u32 v8, 0xfffec000, v8\n"
    "v_add_u32 v9, 0xfffec000, v9\n"
    "s_waitcnt lgkmcnt(9)\n"
    "v_mfma_f32_32x32x16_bf16 v[192:207], v[220:223], v[136:139], v[192:207]\n"
    "s_waitcnt lgkmcnt(0)\n"
    "s_barrier\n"
    "ds_read_b128 v[64:67], v0 offset:0\n"
    "ds_read_b64_tr_b16 v[208:209], v4 offset:0\n"
    "ds_read_b64_tr_b16 v[210:211], v4 offset:512\n"
    "ds_read_b64_tr_b16 v[212:213], v5 offset:0\n"
    "ds_read_b64_tr_b16 v[214:215], v5 offset:512\n"
    "v_mfma_f32_32x32x16_bf16 v[144:159], v[224:227], v[140:143], v[144:159]\n"
    "ds_read_b64_tr_b16 v[216:217], v6 offset:0\n"
    "ds_read_b64_tr_b16 v[218:219], v6 offset:512\n"
    "ds_read_b64_tr_b16 v[220:221], v7 offset:0\n"
    "ds_read_b64_tr_b16 v[222:223], v7 offset:512\n"
    "v_mfma_f32_16x16x32_bf16 v[240:243], v[224:227], v[244:247], v[240:243]\n"
    "v_mfma_f32_32x32x16_bf16 v[160:175], v[228:231], v[140:143], v[160:175]\n"
    "ds_read_b128 v[68:71], v1 offset:0\n"
    "ds_read_b64_tr_b16 v[224:225], v8 offset:0\n"
    "ds_read_b64_tr_b16 v[226:227], v8 offset:512\n"
    "v_mfma_f32_32x32x16_bf16 v[176:191], v[232:235], v[140:143], v[176:191]\n"
    "ds_read_b64_tr_b16 v[228:229], v9 offset:0\n"
    "ds_read_b64_tr_b16 v[230:231], v9 offset:512\n"
    "v_mfma_f32_32x32x16_bf16 v[192:207], v[236:239], v[140:143], v[192:207]\n"
    "v_add_u32 v10, 0xfffec000, v10\n"
    "ds_read_b64_tr_b16 v[232:233], v10 offset:0\n"
    "ds_read_b64_tr_b16 v[234:235], v10 offset:512\n"
    "s_waitcnt lgkmcnt(13)\n"
    "v_mfma_f32_32x32x16_bf16 v[144:159], v[208:211], v[64:67], v[144:159]\n"
    "v_add_u32 v11, 0xfffec000, v11\n"
    "ds_read_b64_tr_b16 v[236:237], v11 offset:0\n"
    "ds_read_b64_tr_b16 v[238:239], v11 offset:512\n"
    "v_mfma_f32_16x16x32_bf16 v[240:243], v[208:211], v[244:247], v[240:243]\n"
    "s_waitcnt lgkmcnt(13)\n"
    "v_mfma_f32_32x32x16_bf16 v[160:175], v[212:215], v[64:67], v[160:175]\n"
    "ds_read_b128 v[72:75], v2 offset:0\n"
    "ds_read_b64_tr_b16 v[208:209], v4 offset:4096\n"
    "ds_read_b64_tr_b16 v[210:211], v4 offset:4608\n"
    "s_waitcnt lgkmcnt(14)\n"
    "v_mfma_f32_32x32x16_bf16 v[176:191], v[216:219], v[64:67], v[176:191]\n"
    "ds_read_b64_tr_b16 v[212:213], v5 offset:4096\n"
    "ds_read_b64_tr_b16 v[214:215], v5 offset:4608\n"
    "s_waitcnt lgkmcnt(14)\n"
    "v_mfma_f32_32x32x16_bf16 v[192:207], v[220:223], v[64:67], v[192:207]\n"
    "ds_read_b64_tr_b16 v[216:217], v6 offset:4096\n"
    "ds_read_b64_tr_b16 v[218:219], v6 offset:4608\n"
    "s_waitcnt lgkmcnt(13)\n"
    "v_mfma_f32_32x32x16_bf16 v[144:159], v[224:227], v[68:71], v[144:159]\n"
    "ds_read_b64_tr_b16 v[220:221], v7 offset:4096\n"
    "ds_read_b64_tr_b16 v[222:223], v7 offset:4608\n"
    "v_mfma_f32_16x16x32_bf16 v[240:243], v[224:227], v[244:247], v[240:243]\n"
    "s_waitcnt lgkmcnt(13)\n"
    "v_mfma_f32_32x32x16_bf16 v[160:175], v[228:231], v[68:71], v[160:175]\n"
    "ds_read_b128 v[76:79], v3 offset:0\n"
    "ds_read_b64_tr_b16 v[224:225], v8 offset:4096\n"
    "ds_read_b64_tr_b16 v[226:227], v8 offset:4608\n"
    "s_waitcnt lgkmcnt(14)\n"
    "v_mfma_f32_32x32x16_bf16 v[176:191], v[232:235], v[68:71], v[176:191]\n"
    "ds_read_b64_tr_b16 v[228:229], v9 offset:4096\n"
    "ds_read_b64_tr_b16 v[230:231], v9 offset:4608\n"
    "s_waitcnt lgkmcnt(14)\n"
    "v_mfma_f32_32x32x16_bf16 v[192:207], v[236:239], v[68:71], v[192:207]\n"
    "ds_read_b64_tr_b16 v[232:233], v10 offset:4096\n"
    "ds_read_b64_tr_b16 v[234:235], v10 offset:4608\n"
    "s_waitcnt lgkmcnt(13)\n"
    "v_mfma_f32_32x32x16_bf16 v[144:159], v[208:211], v[72:75], v[144:159]\n"
    "ds_read_b64_tr_b16 v[236:237], v11 offset:4096\n"
    "ds_read_b64_tr_b16 v[238:239], v11 offset:4608\n"
    "v_mfma_f32_16x16x32_bf16 v[240:243], v[208:211], v[244:247], v[240:243]\n"
    "s_waitcnt lgkmcnt(13)\n"
    "v_mfma_f32_32x32x16_bf16 v[160:175], v[212:215], v[72:75], v[160:175]\n"
    "s_waitcnt lgkmcnt(11)\n"
    "v_mfma_f32_32x32x16_bf16 v[176:191], v[216:219], v[72:75], v[176:191]\n"
    "s_waitcnt lgkmcnt(9)\n"
    "v_mfma_f32_32x32x16_bf16 v[192:207], v[220:223], v[72:75], v[192:207]\n"
    "s_waitcnt lgkmcnt(0)\n"
    "s_barrier\n"
    "ds_read_b128 v[80:83], v0 offset:40960\n"
    "ds_read_b64_tr_b16 v[208:209], v4 offset:40960\n"
    "ds_read_b64_tr_b16 v[210:211], v4 offset:41472\n"
    "ds_read_b64_tr_b16 v[212:213], v5 offset:40960\n"
    "ds_read_b64_tr_b16 v[214:215], v5 offset:41472\n"
    "v_mfma_f32_32x32x16_bf16 v[144:159], v[224:227], v[76:79], v[144:159]\n"
    "ds_read_b64_tr_b16 v[216:217], v6 offset:40960\n"
    "ds_read_b64_tr_b16 v[218:219], v6 offset:41472\n"
    "ds_read_b64_tr_b16 v[220:221], v7 offset:40960\n"
    "ds_read_b64_tr_b16 v[222:223], v7 offset:41472\n"
    "v_mfma_f32_16x16x32_bf16 v[240:243], v[224:227], v[244:247], v[240:243]\n"
    "v_mfma_f32_32x32x16_bf16 v[160:175], v[228:231], v[76:79], v[160:175]\n"
    "ds_read_b128 v[40:43], v1 offset:40960\n"
    "ds_read_b64_tr_b16 v[224:225], v8 offset:40960\n"
    "ds_read_b64_tr_b16 v[226:227], v8 offset:41472\n"
    "v_mfma_f32_32x32x16_bf16 v[176:191], v[232:235], v[76:79], v[176:191]\n"
    "ds_read_b64_tr_b16 v[228:229], v9 offset:40960\n"
    "ds_read_b64_tr_b16 v[230:231], v9 offset:41472\n"
    "v_mfma_f32_32x32x16_bf16 v[192:207], v[236:239], v[76:79], v[192:207]\n"
    "ds_read_b64_tr_b16 v[232:233], v10 offset:40960\n"
    "ds_read_b64_tr_b16 v[234:235], v10 offset:41472\n"
    "s_waitcnt lgkmcnt(13)\n"
    "v_mfma_f32_32x32x16_bf16 v[144:159], v[208:211], v[80:83], v[144:159]\n"
    "ds_read_b64_tr_b16 v[236:237], v11 offset:40960\n"
    "ds_read_b64_tr_b16 v[238:239], v11 offset:41472\n"
    "v_mfma_f32_16x16x32_bf16 v[240:243], v[208:211], v[244:247], v[240:243]\n"
    "s_waitcnt lgkmcnt(13)\n"
    "v_mfma_f32_32x32x16_bf16 v[160:175], v[212:215], v[80:83], v[160:175]\n"
    "ds_read_b128 v[84:87], v2 offset:40960\n"
    "ds_read_b64_tr_b16 v[208:209], v4 offset:45056\n"
    "ds_read_b64_tr_b16 v[210:211], v4 offset:45568\n"
    "s_waitcnt lgkmcnt(14)\n"
    "v_mfma_f32_32x32x16_bf16 v[176:191], v[216:219], v[80:83], v[176:191]\n"
    "ds_read_b64_tr_b16 v[212:213], v5 offset:45056\n"
    "ds_read_b64_tr_b16 v[214:215], v5 offset:45568\n"
    "s_waitcnt lgkmcnt(14)\n"
    "v_mfma_f32_32x32x16_bf16 v[192:207], v[220:223], v[80:83], v[192:207]\n"
    "ds_read_b64_tr_b16 v[216:217], v6 offset:45056\n"
    "ds_read_b64_tr_b16 v[218:219], v6 offset:45568\n"
    "s_waitcnt lgkmcnt(13)\n"
    "v_mfma_f32_32x32x16_bf16 v[144:159], v[224:227], v[40:43], v[144:159]\n"
    "ds_read_b64_tr_b16 v[220:221], v7 offset:45056\n"
    "ds_read_b64_tr_b16 v[222:223], v7 offset:45568\n"
    "v_mfma_f32_16x16x32_bf16 v[240:243], v[224:227], v[244:247], v[240:243]\n"
    "s_waitcnt lgkmcnt(13)\n"
    "v_mfma_f32_32x32x16_bf16 v[160:175], v[228:231], v[40:43], v[160:175]\n"
    "ds_read_b128 v[44:47], v3 offset:40960\n"
    "ds_read_b64_tr_b16 v[224:225], v8 offset:45056\n"
    "ds_read_b64_tr_b16 v[226:227], v8 offset:45568\n"
    "s_waitcnt lgkmcnt(14)\n"
    "v_mfma_f32_32x32x16_bf16 v[176:191], v[232:235], v[40:43], v[176:191]\n"
    "ds_read_b64_tr_b16 v[228:229], v9 offset:45056\n"
    "ds_read_b64_tr_b16 v[230:231], v9 offset:45568\n"
    "s_waitcnt lgkmcnt(14)\n"
    "v_mfma_f32_32x32x16_bf16 v[192:207], v[236:239], v[40:43], v[192:207]\n"
    "ds_read_b64_tr_b16 v[232:233], v10 offset:45056\n"
    "ds_read_b64_tr_b16 v[234:235], v10 offset:45568\n"
    "s_waitcnt lgkmcnt(13)\n"
    "v_mfma_f32_32x32x16_bf16 v[144:159], v[208:211], v[84:87], v[144:159]\n"
    "ds_read_b64_tr_b16 v[236:237], v11 offset:45056\n"
    "ds_read_b64_tr_b16 v[238:239], v11 offset:45568\n"
    "v_mfma_f32_16x16x32_bf16 v[240:243], v[208:211], v[244:247], v[240:243]\n"
    "s_waitcnt lgkmcnt(13)\n"
    "v_mfma_f32_32x32x16_bf16 v[160:175], v[212:215], v[84:87], v[160:175]\n"
    "v_add_u32 v0, 0x14000, v0\n"
    "v_add_u32 v1, 0x14000, v1\n"
    "v_add_u32 v2, 0x14000, v2\n"
    "v_add_u32 v3, 0x14000, v3\n"
    "v_add_u32 v4, 0x14000, v4\n"
    "buffer_load_dwordx2 v[252:253], v25, s[24:27], 0 offen sc1\n"
    "s_waitcnt lgkmcnt(11)\n"
    "v_mfma_f32_32x32x16_bf16 v[176:191], v[216:219], v[84:87], v[176:191]\n"
    "v_add_u32 v5, 0x14000, v5\n"
    "v_add_u32 v6, 0x14000, v6\n"
    "v_add_u32 v7, 0x14000, v7\n"
    "v_add_u32 v8, 0x14000, v8\n"
    "v_add_u32 v9, 0x14000, v9\n"
    "s_waitcnt lgkmcnt(9)\n"
    "v_mfma_f32_32x32x16_bf16 v[192:207], v[220:223], v[84:87], v[192:207]\n"
    "s_waitcnt lgkmcnt(0)\n"
    "s_barrier\n"
    "ds_read_b128 v[88:91], v0 offset:0\n"
    "ds_read_b64_tr_b16 v[208:209], v4 offset:0\n"
    "ds_read_b64_tr_b16 v[210:211], v4 offset:512\n"
    "ds_read_b64_tr_b16 v[212:213], v5 offset:0\n"
    "ds_read_b64_tr_b16 v[214:215], v5 offset:512\n"
    "v_mfma_f32_32x32x16_bf16 v[144:159], v[224:227], v[44:47], v[144:159]\n"
    "ds_read_b64_tr_b16 v[216:217], v6 offset:0\n"
    "ds_read_b64_tr_b16 v[218:219], v6 offset:512\n"
    "ds_read_b64_tr_b16 v[220:221], v7 offset:0\n"
    "ds_read_b64_tr_b16 v[222:223], v7 offset:512\n"
    "v_mfma_f32_16x16x32_bf16 v[240:243], v[224:227], v[244:247], v[240:243]\n"
    "v_mfma_f32_32x32x16_bf16 v[160:175], v[228:231], v[44:47], v[160:175]\n"
    "ds_read_b128 v[48:51], v1 offset:0\n"
    "ds_read_b64_tr_b16 v[224:225], v8 offset:0\n"
    "ds_read_b64_tr_b16 v[226:227], v8 offset:512\n"
    "v_mfma_f32_32x32x16_bf16 v[176:191], v[232:235], v[44:47], v[176:191]\n"
    "ds_read_b64_tr_b16 v[228:229], v9 offset:0\n"
    "ds_read_b64_tr_b16 v[230:231], v9 offset:512\n"
    "v_mfma_f32_32x32x16_bf16 v[192:207], v[236:239], v[44:47], v[192:207]\n"
    "v_add_u32 v10, 0x14000, v10\n"
    "ds_read_b64_tr_b16 v[232:233], v10 offset:0\n"
    "ds_read_b64_tr_b16 v[234:235], v10 offset:512\n"
    "s_waitcnt lgkmcnt(13)\n"
    "v_mfma_f32_32x32x16_bf16 v[144:159], v[208:211], v[88:91], v[144:159]\n"
    "v_add_u32 v11, 0x14000, v11\n"
    "ds_read_b64_tr_b16 v[236:237], v11 offset:0\n"
    "ds_read_b64_tr_b16 v[238:239], v11 offset:512\n"
    "v_mfma_f32_16x16x32_bf16 v[240:243], v[208:211], v[244:247], v[240:243]\n"
    "s_waitcnt lgkmcnt(13)\n"
    "v_mfma_f32_32x32x16_bf16 v[160:175], v[212:215], v[88:91], v[160:175]\n"
    "ds_read_b128 v[92:95], v2 offset:0\n"
    "ds_read_b64_tr_b16 v[208:209], v4 offset:4096\n"
    "ds_read_b64_tr_b16 v[210:211], v4 offset:4608\n"
    "s_waitcnt lgkmcnt(14)\n"
    "v_mfma_f32_32x32x16_bf16 v[176:191], v[216:219], v[88:91], v[176:191]\n"
    "ds_read_b64_tr_b16 v[212:213], v5 offset:4096\n"
    "ds_read_b64_tr_b16 v[214:215], v5 offset:4608\n"
    "s_waitcnt lgkmcnt(14)\n"
    "v_mfma_f32_32x32x16_bf16 v[192:207], v[220:223], v[88:91], v[192:207]\n"
    "ds_read_b64_tr_b16 v[216:217], v6 offset:4096\n"
    "ds_read_b64_tr_b16 v[218:219], v6 offset:4608\n"
    "s_waitcnt vmcnt(0)\n"
    "v_cmp_ne_u32 vcc, s69, v253\n"
    "s_mov_b32 s79, 0x40000\n"
    "s_cmp_eq_u64 vcc, 0\n"
    "s_cbranch_scc1 pok19_%=\n"
    "psp20_%=:\n"
    "buffer_load_dwordx2 v[252:253], v25, s[24:27], 0 offen sc1\n"
    "s_sub_u32 s79, s79, 1\n"
    "s_cbranch_scc1 pok19_%=\n"
    "s_waitcnt vmcnt(0)\n"
    "v_cmp_ne_u32 vcc, s69, v253\n"
    "s_sleep 1\n"
    "s_cmp_eq_u64 vcc, 0\n"
    "s_cbranch_scc0 psp20_%=\n"
    "pok19_%=:\n"
    "v_and_b32 v254, s77, v252\n"
    "s_waitcnt lgkmcnt(13)\n"
    "v_mfma_f32_32x32x16_bf16 v[144:159], v[224:227], v[48:51], v[144:159]\n"
    "ds_read_b64_tr_b16 v[220:221], v7 offset:4096\n"
    "ds_read_b64_tr_b16 v[222:223], v7 offset:4608\n"
    "v_mfma_f32_16x16x32_bf16 v[240:243], v[224:227], v[244:247], v[240:243]\n"
    "v_and_b32 v248, 0xffff, v252\n"
    "s_waitcnt lgkmcnt(13)\n"
    "v_mfma_f32_32x32x16_bf16 v[160:175], v[228:231], v[48:51], v[160:175]\n"
    "ds_read_b128 v[52:55], v3 offset:0\n"
    "ds_read_b64_tr_b16 v[224:225], v8 offset:4096\n"
    "ds_read_b64_tr_b16 v[226:227], v8 offset:4608\n"
    "v_rcp_f32 v255, v254\n"
    "v_lshl_or_b32 v248, v252, 16, v248\n"
    "s_waitcnt lgkmcnt(14)\n"
    "v_mfma_f32_32x32x16_bf16 v[176:191], v[232:235], v[48:51], v[176:191]\n"
    "ds_read_b64_tr_b16 v[228:229], v9 offset:4096\n"
    "ds_read_b64_tr_b16 v[230:231], v9 offset:4608\n"
    "v_mul_f32 v254, v254, v254\n"
    "s_waitcnt lgkmcnt(14)\n"
    "v_mfma_f32_32x32x16_bf16 v[192:207], v[236:239], v[48:51], v[192:207]\n"
    "ds_read_b64_tr_b16 v[232:233], v10 offset:4096\n"
    "ds_read_b64_tr_b16 v[234:235], v10 offset:4608\n"
    "v_mov_b32 v249, v248\n"
    "v_mul_f32 v252, 0xc0135761, v255\n"
    "s_waitcnt lgkmcnt(13)\n"
    "v_mfma_f32_32x32x16_bf16 v[144:159], v[208:211], v[92:95], v[144:159]\n"
    "ds_read_b64_tr_b16 v[236:237], v11 offset:4096\n"
    "ds_read_b64_tr_b16 v[238:239], v11 offset:4608\n"
    "v_mfma_f32_16x16x32_bf16 v[240:243], v[208:211], v[244:247], v[240:243]\n"
    "v_mul_f32 v253, v255, v255\n"
    "s_waitcnt lgkmcnt(13)\n"
    "v_mfma_f32_32x32x16_bf16 v[160:175], v[212:215], v[92:95], v[160:175]\n"
    "v_mul_f32 v253, v253, v252\n"
    "v_mul_f32 v253, 0x3d372713, v253\n"
    "s_waitcnt lgkmcnt(11)\n"
    "v_mfma_f32_32x32x16_bf16 v[176:191], v[216:219], v[92:95], v[176:191]\n"
    "s_waitcnt lgkmcnt(9)\n"
    "v_mfma_f32_32x32x16_bf16 v[192:207], v[220:223], v[92:95], v[192:207]\n"
    "s_waitcnt lgkmcnt(0)\n"
    "s_barrier\n"
    "ds_read_b128 v[32:35], v0 offset:40960\n"
    "ds_read_b64_tr_b16 v[208:209], v4 offset:40960\n"
    "ds_read_b64_tr_b16 v[210:211], v4 offset:41472\n"
    "ds_read_b128 v[56:59], v1 offset:40960\n"
    "ds_read_b64_tr_b16 v[212:213], v8 offset:40960\n"
    "ds_read_b64_tr_b16 v[214:215], v8 offset:41472\n"
    "v_mfma_f32_32x32x16_bf16 v[144:159], v[224:227], v[52:55], v[144:159]\n"
    "ds_read_b128 v[36:39], v2 offset:40960\n"
    "ds_read_b64_tr_b16 v[216:217], v4 offset:45056\n"
    "ds_read_b64_tr_b16 v[218:219], v4 offset:45568\n"
    "ds_read_b128 v[60:63], v3 offset:40960\n"
    "ds_read_b64_tr_b16 v[220:221], v8 offset:45056\n"
    "ds_read_b64_tr_b16 v[222:223], v8 offset:45568\n"
    "v_mfma_f32_16x16x32_bf16 v[240:243], v[224:227], v[244:247], v[240:243]\n"
    "v_lshrrev_b32 v24, 2, v15\n"
    "v_mfma_f32_32x32x16_bf16 v[160:175], v[228:231], v[52:55], v[160:175]\n"
    "ds_read_b64_tr_b16 v[224:225], v5 offset:40960\n"
    "ds_read_b64_tr_b16 v[226:227], v5 offset:41472\n"
    "v_mfma_f32_32x32x16_bf16 v[176:191], v[232:235], v[52:55], v[176:191]\n"
    "ds_read_b64_tr_b16 v[228:229], v6 offset:40960\n"
    "ds_read_b64_tr_b16 v[230:231], v6 offset:41472\n"
    "v_add_u32 v24, 122368, v24\n"
    "v_mfma_f32_32x32x16_bf16 v[192:207], v[236:239], v[52:55], v[192:207]\n"
    "ds_read_b64_tr_b16 v[232:233], v7 offset:40960\n"
    "ds_read_b64_tr_b16 v[234:235], v7 offset:41472\n"
    "s_waitcnt lgkmcnt(15)\n"
    "v_mfma_f32_32x32x16_bf16 v[144:159], v[208:211], v[32:35], v[144:159]\n"
    "ds_read_b64_tr_b16 v[236:237], v9 offset:40960\n"
    "ds_read_b64_tr_b16 v[238:239], v9 offset:41472\n"
    "v_mfma_f32_16x16x32_bf16 v[240:243], v[208:211], v[244:247], v[240:243]\n"
    "ds_write_b32 v24, v250\n"
    "s_waitcnt lgkmcnt(15)\n"
    "v_mfma_f32_32x32x16_bf16 v[144:159], v[212:215], v[56:59], v[144:159]\n"
    "ds_read_b64_tr_b16 v[208:209], v10 offset:40960\n"
    "ds_read_b64_tr_b16 v[210:211], v10 offset:41472\n"
    "v_mfma_f32_16x16x32_bf16 v[240:243], v[212:215], v[244:247], v[240:243]\n"
    "s_waitcnt lgkmcnt(14)\n"
    "v_mfma_f32_32x32x16_bf16 v[144:159], v[216:219], v[36:39], v[144:159]\n"
    "ds_read_b64_tr_b16 v[212:213], v11 offset:40960\n"
    "ds_read_b64_tr_b16 v[214:215], v11 offset:41472\n"
    "v_mfma_f32_16x16x32_bf16 v[240:243], v[216:219], v[244:247], v[240:243]\n"
    "s_waitcnt lgkmcnt(13)\n"
    "v_mfma_f32_32x32x16_bf16 v[144:159], v[220:223], v[60:63], v[144:159]\n"
    "ds_read_b64_tr_b16 v[216:217], v5 offset:45056\n"
    "ds_read_b64_tr_b16 v[218:219], v5 offset:45568\n"
    "v_mfma_f32_16x16x32_bf16 v[240:243], v[220:223], v[244:247], v[240:243]\n"
    "s_waitcnt lgkmcnt(13)\n"
    "v_mfma_f32_32x32x16_bf16 v[160:175], v[224:227], v[32:35], v[160:175]\n"
    "ds_read_b64_tr_b16 v[220:221], v6 offset:45056\n"
    "ds_read_b64_tr_b16 v[222:223], v6 offset:45568\n"
    "s_waitcnt lgkmcnt(13)\n"
    "v_mfma_f32_32x32x16_bf16 v[176:191], v[228:231], v[32:35], v[176:191]\n"
    "ds_read_b64_tr_b16 v[224:225], v7 offset:45056\n"
    "ds_read_b64_tr_b16 v[226:227], v7 offset:45568\n"
    "v_cndmask_b32 v240, v240, v241, s[80:81]\n"
    "v_cndmask_b32 v242, v242, v243, s[80:81]\n"
    "v_cndmask_b32 v240, v240, v242, s[82:83]\n"
    "v_and_b32 v241, s77, v240\n"
    "v_sub_f32 v241, v240, v241\n"
    "s_waitcnt lgkmcnt(13)\n"
    "v_mfma_f32_32x32x16_bf16 v[192:207], v[232:235], v[32:35], v[192:207]\n"
    "ds_read_b64_tr_b16 v[228:229], v9 offset:45056\n"
    "ds_read_b64_tr_b16 v[230:231], v9 offset:45568\n"
    "v_and_b32 v242, s77, v241\n"
    "v_sub_f32 v243, v241, v242\n"
    "v_lshrrev_b32 v240, 16, v240\n"
    "v_or_b32 v242, v240, v242\n"
    "v_lshrrev_b32 v243, 16, v243\n"
    "ds_write_b64 v20, v[242:243]\n"
    "ds_write_b64 v20, v[250:251] offset:8\n"
    "s_waitcnt lgkmcnt(15)\n"
    "v_mfma_f32_32x32x16_bf16 v[160:175], v[236:239], v[56:59], v[160:175]\n"
    "ds_read_b64_tr_b16 v[232:233], v10 offset:45056\n"
    "ds_read_b64_tr_b16 v[234:235], v10 offset:45568\n"
    "s_waitcnt lgkmcnt(14)\n"
    "v_mfma_f32_32x32x16_bf16 v[176:191], v[208:211], v[56:59], v[176:191]\n"
    "ds_read_b64_tr_b16 v[236:237], v11 offset:45056\n"
    "ds_read_b64_tr_b16 v[238:239], v11 offset:45568\n"
    "s_waitcnt lgkmcnt(14)\n"
    "v_mfma_f32_32x32x16_bf16 v[192:207], v[212:215], v[56:59], v[192:207]\n"
    "s_waitcnt lgkmcnt(12)\n"
    "v_mfma_f32_32x32x16_bf16 v[160:175], v[216:219], v[36:39], v[160:175]\n"
    "s_waitcnt lgkmcnt(10)\n"
    "v_mfma_f32_32x32x16_bf16 v[176:191], v[220:223], v[36:39], v[176:191]\n"
    "s_waitcnt lgkmcnt(8)\n"
    "v_mfma_f32_32x32x16_bf16 v[192:207], v[224:227], v[36:39], v[192:207]\n"
    "s_waitcnt lgkmcnt(0)\n"
    "s_barrier\n"
    "ds_read_b64 v[208:209], v22\n"
    "v_xor_b32 v1, 512, v22\n"
    "ds_read_b64 v[212:213], v1\n"
    "v_xor_b32 v2, 1024, v22\n"
    "ds_read_b64 v[216:217], v2\n"
    "v_xor_b32 v3, 1536, v22\n"
    "ds_read_b64 v[220:221], v3\n"
    "v_mfma_f32_32x32x16_bf16 v[160:175], v[228:231], v[60:63], v[160:175]\n"
    "v_mfma_f32_32x32x16_bf16 v[176:191], v[232:235], v[60:63], v[176:191]\n"
    "v_mfma_f32_32x32x16_bf16 v[192:207], v[236:239], v[60:63], v[192:207]\n"
    "s_waitcnt lgkmcnt(3)\n"
    "v_mfma_f32_32x32x16_bf16 v[144:159], v[208:211], v[248:251], v[144:159]\n"
    "s_waitcnt lgkmcnt(2)\n"
    "v_mfma_f32_32x32x16_bf16 v[160:175], v[212:215], v[248:251], v[160:175]\n"
    "s_waitcnt lgkmcnt(1)\n"
    "v_mfma_f32_32x32x16_bf16 v[176:191], v[216:219], v[248:251], v[176:191]\n"
    "s_waitcnt lgkmcnt(0)\n"
    "v_mfma_f32_32x32x16_bf16 v[192:207], v[220:223], v[248:251], v[192:207]\n"
    "s_cmp_eq_u32 s57, 4\n"
    "s_cbranch_scc1 gq_%=\n"
    "s_nop 3\n"
    "v_mul_f32 v224, v144, v144\n"
    "v_mul_f32 v225, v145, v145\n"
    "v_mul_f32 v226, v146, v146\n"
    "v_mul_f32 v227, v147, v147\n"
    "v_mul_f32 v228, v148, v148\n"
    "v_mul_f32 v229, v149, v149\n"
    "v_mul_f32 v230, v150, v150\n"
    "v_mul_f32 v231, v151, v151\n"
    "v_fma_f32 v224, v224, v253, v252\n"
    "v_fma_f32 v225, v225, v253, v252\n"
    "v_fma_f32 v226, v226, v253, v252\n"
    "v_fma_f32 v227, v227, v253, v252\n"
    "v_fma_f32 v228, v228, v253, v252\n"
    "v_fma_f32 v229, v229, v253, v252\n"
    "v_fma_f32 v230, v230, v253, v252\n"
    "v_fma_f32 v231, v231, v253, v252\n"
    "v_mul_f32 v224, v224, v144\n"
    "v_mul_f32 v225, v225, v145\n"
    "v_mul_f32 v226, v226, v146\n"
    "v_mul_f32 v227, v227, v147\n"
    "v_mul_f32 v228, v228, v148\n"
    "v_mul_f32 v229, v229, v149\n"
    "v_mul_f32 v230, v230, v150\n"
    "v_mul_f32 v231, v231, v151\n"
    "v_exp_f32 v224, v224\n"
    "v_mul_f32 v152, v144, v152\n"
    "v_exp_f32 v225, v225\n"
    "v_mul_f32 v153, v145, v153\n"
    "v_exp_f32 v226, v226\n"
    "v_mul_f32 v154, v146, v154\n"
    "v_exp_f32 v227, v227\n"
    "v_mul_f32 v155, v147, v155\n"
    "v_exp_f32 v228, v228\n"
    "v_mul_f32 v156, v148, v156\n"
    "v_exp_f32 v229, v229\n"
    "v_mul_f32 v157, v149, v157\n"
    "v_exp_f32 v230, v230\n"
    "v_mul_f32 v158, v150, v158\n"
    "v_exp_f32 v231, v231\n"
    "v_mul_f32 v159, v151, v159\n"
    "v_fma_f32 v224, v224, v254, v254\n"
    "v_fma_f32 v225, v225, v254, v254\n"
    "v_fma_f32 v226, v226, v254, v254\n"
    "v_fma_f32 v227, v227, v254, v254\n"
    "v_fma_f32 v228, v228, v254, v254\n"
    "v_fma_f32 v229, v229, v254, v254\n"
    "v_fma_f32 v230, v230, v254, v254\n"
    "v_fma_f32 v231, v231, v254, v254\n"
    "v_rcp_f32 v224, v224\n"
    "v_rcp_f32 v225, v225\n"
    "v_rcp_f32 v226, v226\n"
    "v_rcp_f32 v227, v227\n"
    "v_rcp_f32 v228, v228\n"
    "v_rcp_f32 v229, v229\n"
    "v_rcp_f32 v230, v230\n"
    "v_rcp_f32 v231, v231\n"
    "v_mul_f32 v152, v152, v224\n"
    "v_mul_f32 v153, v153, v225\n"
    "v_mul_f32 v154, v154, v226\n"
    "v_mul_f32 v155, v155, v227\n"
    "v_mul_f32 v156, v156, v228\n"
    "v_mul_f32 v157, v157, v229\n"
    "v_mul_f32 v158, v158, v230\n"
    "v_mul_f32 v159, v159, v231\n"
    "v_cvt_pk_bf16_f32 v0, v152, v153\n"
    "v_cvt_pk_bf16_f32 v1, v154, v155\n"
    "v_cvt_pk_bf16_f32 v2, v156, v157\n"
    "v_cvt_pk_bf16_f32 v3, v158, v159\n"
    "v_mul_f32 v224, v160, v160\n"
    "v_mul_f32 v225, v161, v161\n"
    "v_mul_f32 v226, v162, v162\n"
    "v_mul_f32 v227, v163, v163\n"
    "v_mul_f32 v228, v164, v164\n"
    "v_mul_f32 v229, v165, v165\n"
    "v_mul_f32 v230, v166, v166\n"
    "v_mul_f32 v231, v167, v167\n"
    "v_fma_f32 v224, v224, v253, v252\n"
    "v_fma_f32 v225, v225, v253, v252\n"
    "v_fma_f32 v226, v226, v253, v252\n"
    "v_fma_f32 v227, v227, v253, v252\n"
    "v_fma_f32 v228, v228, v253, v252\n"
    "v_fma_f32 v229, v229, v253, v252\n"
    "v_fma_f32 v230, v230, v253, v252\n"
    "v_fma_f32 v231, v231, v253, v252\n"
    "v_mul_f32 v224, v224, v160\n"
    "v_mul_f32 v225, v225, v161\n"
    "v_mul_f32 v226, v226, v162\n"
    "v_mul_f32 v227, v227, v163\n"
    "v_mul_f32 v228, v228, v164\n"
    "v_mul_f32 v229, v229, v165\n"
    "v_mul_f32 v230, v230, v166\n"
    "v_mul_f32 v231, v231, v167\n"
    "v_exp_f32 v224, v224\n"
    "v_mul_f32 v168, v160, v168\n"
    "v_exp_f32 v225, v225\n"
    "v_mul_f32 v169, v161, v169\n"
    "v_exp_f32 v226, v226\n"
    "v_mul_f32 v170, v162, v170\n"
    "v_exp_f32 v227, v227\n"
    "v_mul_f32 v171, v163, v171\n"
    "v_exp_f32 v228, v228\n"
    "v_mul_f32 v172, v164, v172\n"
    "v_exp_f32 v229, v229\n"
    "v_mul_f32 v173, v165, v173\n"
    "v_exp_f32 v230, v230\n"
    "v_mul_f32 v174, v166, v174\n"
    "v_exp_f32 v231, v231\n"
    "v_mul_f32 v175, v167, v175\n"
    "v_fma_f32 v224, v224, v254, v254\n"
    "v_fma_f32 v225, v225, v254, v254\n"
    "v_fma_f32 v226, v226, v254, v254\n"
    "v_fma_f32 v227, v227, v254, v254\n"
    "v_fma_f32 v228, v228, v254, v254\n"
    "v_fma_f32 v229, v229, v254, v254\n"
    "v_fma_f32 v230, v230, v254, v254\n"
    "v_fma_f32 v231, v231, v254, v254\n"
    "v_rcp_f32 v224, v224\n"
    "v_rcp_f32 v225, v225\n"
    "v_rcp_f32 v226, v226\n"
    "v_rcp_f32 v227, v227\n"
    "v_rcp_f32 v228, v228\n"
    "v_rcp_f32 v229, v229\n"
    "v_rcp_f32 v230, v230\n"
    "v_rcp_f32 v231, v231\n"
    "v_mul_f32 v168, v168, v224\n"
    "v_mul_f32 v169, v169, v225\n"
    "v_mul_f32 v170, v170, v226\n"
    "v_mul_f32 v171, v171, v227\n"
    "v_mul_f32 v172, v172, v228\n"
    "v_mul_f32 v173, v173, v229\n"
    "v_mul_f32 v174, v174, v230\n"
    "v_mul_f32 v175, v175, v231\n"
    "v_cvt_pk_bf16_f32 v4, v168, v169\n"
    "v_cvt_pk_bf16_f32 v5, v170, v171\n"
    "v_cvt_pk_bf16_f32 v6, v172, v173\n"
    "v_cvt_pk_bf16_f32 v7, v174, v175\n"
    "v_mul_f32 v224, v176, v176\n"
    "v_mul_f32 v225, v177, v177\n"
    "v_mul_f32 v226, v178, v178\n"
    "v_mul_f32 v227, v179, v179\n"
    "v_mul_f32 v228, v180, v180\n"
    "v_mul_f32 v229, v181, v181\n"
    "v_mul_f32 v230, v182, v182\n"
    "v_mul_f32 v231, v183, v183\n"
    "v_fma_f32 v224, v224, v253, v252\n"
    "v_fma_f32 v225, v225, v253, v252\n"
    "v_fma_f32 v226, v226, v253, v252\n"
    "v_fma_f32 v227, v227, v253, v252\n"
    "v_fma_f32 v228, v228, v253, v252\n"
    "v_fma_f32 v229, v229, v253, v252\n"
    "v_fma_f32 v230, v230, v253, v252\n"
    "v_fma_f32 v231, v231, v253, v252\n"
    "v_mul_f32 v224, v224, v176\n"
    "v_mul_f32 v225, v225, v177\n"
    "v_mul_f32 v226, v226, v178\n"
    "v_mul_f32 v227, v227, v179\n"
    "v_mul_f32 v228, v228, v180\n"
    "v_mul_f32 v229, v229, v181\n"
    "v_mul_f32 v230, v230, v182\n"
    "v_mul_f32 v231, v231, v183\n"
    "v_exp_f32 v224, v224\n"
    "v_mul_f32 v184, v176, v184\n"
    "v_exp_f32 v225, v225\n"
    "v_mul_f32 v185, v177, v185\n"
    "v_exp_f32 v226, v226\n"
    "v_mul_f32 v186, v178, v186\n"
    "v_exp_f32 v227, v227\n"
    "v_mul_f32 v187, v179, v187\n"
    "v_exp_f32 v228, v228\n"
    "v_mul_f32 v188, v180, v188\n"
    "v_exp_f32 v229, v229\n"
    "v_mul_f32 v189, v181, v189\n"
    "v_exp_f32 v230, v230\n"
    "v_mul_f32 v190, v182, v190\n"
    "v_exp_f32 v231, v231\n"
    "v_mul_f32 v191, v183, v191\n"
    "v_fma_f32 v224, v224, v254, v254\n"
    "v_fma_f32 v225, v225, v254, v254\n"
    "v_fma_f32 v226, v226, v254, v254\n"
    "v_fma_f32 v227, v227, v254, v254\n"
    "v_fma_f32 v228, v228, v254, v254\n"
    "v_fma_f32 v229, v229, v254, v254\n"
    "v_fma_f32 v230, v230, v254, v254\n"
    "v_fma_f32 v231, v231, v254, v254\n"
    "v_rcp_f32 v224, v224\n"
    "v_rcp_f32 v225, v225\n"
    "v_rcp_f32 v226, v226\n"
    "v_rcp_f32 v227, v227\n"
    "v_rcp_f32 v228, v228\n"
    "v_rcp_f32 v229, v229\n"
    "v_rcp_f32 v230, v230\n"
    "v_rcp_f32 v231, v231\n"
    "v_mul_f32 v184, v184, v224\n"
    "v_mul_f32 v185, v185, v225\n"
    "v_mul_f32 v186, v186, v226\n"
    "v_mul_f32 v187, v187, v227\n"
    "v_mul_f32 v188, v188, v228\n"
    "v_mul_f32 v189, v189, v229\n"
    "v_mul_f32 v190, v190, v230\n"
    "v_mul_f32 v191, v191, v231\n"
    "v_cvt_pk_bf16_f32 v176, v184, v185\n"
    "v_cvt_pk_bf16_f32 v177, v186, v187\n"
    "v_cvt_pk_bf16_f32 v178, v188, v189\n"
    "v_cvt_pk_bf16_f32 v179, v190, v191\n"
    "v_mul_f32 v224, v192, v192\n"
    "v_mul_f32 v225, v193, v193\n"
    "v_mul_f32 v226, v194, v194\n"
    "v_mul_f32 v227, v195, v195\n"
    "v_mul_f32 v228, v196, v196\n"
    "v_mul_f32 v229, v197, v197\n"
    "v_mul_f32 v230, v198, v198\n"
    "v_mul_f32 v231, v199, v199\n"
    "v_fma_f32 v224, v224, v253, v252\n"
    "v_fma_f32 v225, v225, v253, v252\n"
    "v_fma_f32 v226, v226, v253, v252\n"
    "v_fma_f32 v227, v227, v253, v252\n"
    "v_fma_f32 v228, v228, v253, v252\n"
    "v_fma_f32 v229, v229, v253, v252\n"
    "v_fma_f32 v230, v230, v253, v252\n"
    "v_fma_f32 v231, v231, v253, v252\n"
    "v_mul_f32 v224, v224, v192\n"
    "v_mul_f32 v225, v225, v193\n"
    "v_mul_f32 v226, v226, v194\n"
    "v_mul_f32 v227, v227, v195\n"
    "v_mul_f32 v228, v228, v196\n"
    "v_mul_f32 v229, v229, v197\n"
    "v_mul_f32 v230, v230, v198\n"
    "v_mul_f32 v231, v231, v199\n"
    "v_exp_f32 v224, v224\n"
    "v_mul_f32 v200, v192, v200\n"
    "v_exp_f32 v225, v225\n"
    "v_mul_f32 v201, v193, v201\n"
    "v_exp_f32 v226, v226\n"
    "v_mul_f32 v202, v194, v202\n"
    "v_exp_f32 v227, v227\n"
    "v_mul_f32 v203, v195, v203\n"
    "v_exp_f32 v228, v228\n"
    "v_mul_f32 v204, v196, v204\n"
    "v_exp_f32 v229, v229\n"
    "v_mul_f32 v205, v197, v205\n"
    "v_exp_f32 v230, v230\n"
    "v_mul_f32 v206, v198, v206\n"
    "v_exp_f32 v231, v231\n"
    "v_mul_f32 v207, v199, v207\n"
    "v_fma_f32 v224, v224, v254, v254\n"
    "v_fma_f32 v225, v225, v254, v254\n"
    "v_fma_f32 v226, v226, v254, v254\n"
    "v_fma_f32 v227, v227, v254, v254\n"
    "v_fma_f32 v228, v228, v254, v254\n"
    "v_fma_f32 v229, v229, v254, v254\n"
    "v_fma_f32 v230, v230, v254, v254\n"
    "v_fma_f32 v231, v231, v254, v254\n"
    "v_rcp_f32 v224, v224\n"
    "v_rcp_f32 v225, v225\n"
    "v_rcp_f32 v226, v226\n"
    "v_rcp_f32 v227, v227\n"
    "v_rcp_f32 v228, v228\n"
    "v_rcp_f32 v229, v229\n"
    "v_rcp_f32 v230, v230\n"
    "v_rcp_f32 v231, v231\n"
    "v_mul_f32 v200, v200, v224\n"
    "v_mul_f32 v201, v201, v225\n"
    "v_mul_f32 v202, v202, v226\n"
    "v_mul_f32 v203, v203, v227\n"
    "v_mul_f32 v204, v204, v228\n"
    "v_mul_f32 v205, v205, v229\n"
    "v_mul_f32 v206, v206, v230\n"
    "v_mul_f32 v207, v207, v231\n"
    "v_cvt_pk_bf16_f32 v192, v200, v201\n"
    "v_cvt_pk_bf16_f32 v193, v202, v203\n"
    "v_cvt_pk_bf16_f32 v194, v204, v205\n"
    "v_cvt_pk_bf16_f32 v195, v206, v207\n"
    "s_branch g2_%=\n"
    "gq_%=:\n"
    "s_setprio 2\n"
    "v_mul_f32 v8, s58, v255\n"
    "s_add_u32 s75, s56, 4096\n"
    "s_nop 0\n"
    "v_mul_f32 v144, v8, v144\n"
    "v_mul_f32 v145, v8, v145\n"
    "v_mul_f32 v146, v8, v146\n"
    "v_mul_f32 v147, v8, v147\n"
    "v_mul_f32 v148, v8, v148\n"
    "v_mul_f32 v149, v8, v149\n"
    "v_mul_f32 v150, v8, v150\n"
    "v_mul_f32 v151, v8, v151\n"
    "v_cvt_pk_bf16_f32 v224, v144, v145\n"
    "v_cvt_pk_bf16_f32 v225, v146, v147\n"
    "v_cvt_pk_bf16_f32 v226, v148, v149\n"
    "v_cvt_pk_bf16_f32 v227, v150, v151\n"
    "buffer_store_dwordx4 v[224:227], v26, s[24:27], s56 offen sc1\n"
    "v_mul_f32 v152, v255, v152\n"
    "v_mul_f32 v153, v255, v153\n"
    "v_mul_f32 v154, v255, v154\n"
    "v_mul_f32 v155, v255, v155\n"
    "v_mul_f32 v156, v255, v156\n"
    "v_mul_f32 v157, v255, v157\n"
    "v_mul_f32 v158, v255, v158\n"
    "v_mul_f32 v159, v255, v159\n"
    "v_cvt_pk_bf16_f32 v228, v152, v153\n"
    "v_cvt_pk_bf16_f32 v229, v154, v155\n"
    "v_cvt_pk_bf16_f32 v230, v156, v157\n"
    "v_cvt_pk_bf16_f32 v231, v158, v159\n"
    "buffer_store_dwordx4 v[228:231], v26, s[24:27], s75 offen sc1\n"
    "v_xor_b32 v9, 1024, v26\n"
    "v_mul_f32 v160, v8, v160\n"
    "v_mul_f32 v161, v8, v161\n"
    "v_mul_f32 v162, v8, v162\n"
    "v_mul_f32 v163, v8, v163\n"
    "v_mul_f32 v164, v8, v164\n"
    "v_mul_f32 v165, v8, v165\n"
    "v_mul_f32 v166, v8, v166\n"
    "v_mul_f32 v167, v8, v167\n"
    "v_cvt_pk_bf16_f32 v232, v160, v161\n"
    "v_cvt_pk_bf16_f32 v233, v162, v163\n"
    "v_cvt_pk_bf16_f32 v234, v164, v165\n"
    "v_cvt_pk_bf16_f32 v235, v166, v167\n"
    "buffer_store_dwordx4 v[232:235], v9, s[24:27], s56 offen sc1\n"
    "v_mul_f32 v168, v255, v168\n"
    "v_mul_f32 v169, v255, v169\n"
    "v_mul_f32 v170, v255, v170\n"
    "v_mul_f32 v171, v255, v171\n"
    "v_mul_f32 v172, v255, v172\n"
    "v_mul_f32 v173, v255, v173\n"
    "v_mul_f32 v174, v255, v174\n"
    "v_mul_f32 v175, v255, v175\n"
    "v_cvt_pk_bf16_f32 v236, v168, v169\n"
    "v_cvt_pk_bf16_f32 v237, v170, v171\n"
    "v_cvt_pk_bf16_f32 v238, v172, v173\n"
    "v_cvt_pk_bf16_f32 v239, v174, v175\n"
    "buffer_store_dwordx4 v[236:239], v9, s[24:27], s75 offen sc1\n"
    "v_xor_b32 v9, 2048, v26\n"
    "v_mul_f32 v176, v8, v176\n"
    "v_mul_f32 v177, v8, v177\n"
    "v_mul_f32 v178, v8, v178\n"
    "v_mul_f32 v179, v8, v179\n"
    "v_mul_f32 v180, v8, v180\n"
    "v_mul_f32 v181, v8, v181\n"
    "v_mul_f32 v182, v8, v182\n"
    "v_mul_f32 v183, v8, v183\n"
    "v_cvt_pk_bf16_f32 v224, v176, v177\n"
    "v_cvt_pk_bf16_f32 v225, v178, v179\n"
    "v_cvt_pk_bf16_f32 v226, v180, v181\n"
    "v_cvt_pk_bf16_f32 v227, v182, v183\n"
    "buffer_store_dwordx4 v[224:227], v9, s[24:27], s56 offen sc1\n"
    "v_mul_f32 v184, v255, v184\n"
    "v_mul_f32 v185, v255, v185\n"
    "v_mul_f32 v186, v255, v186\n"
    "v_mul_f32 v187, v255, v187\n"
    "v_mul_f32 v188, v255, v188\n"
    "v_mul_f32 v189, v255, v189\n"
    "v_mul_f32 v190, v255, v190\n"
    "v_mul_f32 v191, v255, v191\n"
    "v_cvt_pk_bf16_f32 v228, v184, v185\n"
    "v_cvt_pk_bf16_f32 v229, v186, v187\n"
    "v_cvt_pk_bf16_f32 v230, v188, v189\n"
    "v_cvt_pk_bf16_f32 v231, v190, v191\n"
    "buffer_store_dwordx4 v[228:231], v9, s[24:27], s75 offen sc1\n"
    "v_xor_b32 v9, 3072, v26\n"
    "v_mul_f32 v192, v8, v192\n"
    "v_mul_f32 v193, v8, v193\n"
    "v_mul_f32 v194, v8, v194\n"
    "v_mul_f32 v195, v8, v195\n"
    "v_mul_f32 v196, v8, v196\n"
    "v_mul_f32 v197, v8, v197\n"
    "v_mul_f32 v198, v8, v198\n"
    "v_mul_f32 v199, v8, v199\n"
    "v_cvt_pk_bf16_f32 v232, v192, v193\n"
    "v_cvt_pk_bf16_f32 v233, v194, v195\n"
    "v_cvt_pk_bf16_f32 v234, v196, v197\n"
    "v_cvt_pk_bf16_f32 v235, v198, v199\n"
    "buffer_store_dwordx4 v[232:235], v9, s[24:27], s56 offen sc1\n"
    "v_mul_f32 v200, v255, v200\n"
    "v_mul_f32 v201, v255, v201\n"
    "v_mul_f32 v202, v255, v202\n"
    "v_mul_f32 v203, v255, v203\n"
    "v_mul_f32 v204, v255, v204\n"
    "v_mul_f32 v205, v255, v205\n"
    "v_mul_f32 v206, v255, v206\n"
    "v_mul_f32 v207, v255, v207\n"
    "v_cvt_pk_bf16_f32 v236, v200, v201\n"
    "v_cvt_pk_bf16_f32 v237, v202, v203\n"
    "v_cvt_pk_bf16_f32 v238, v204, v205\n"
    "v_cvt_pk_bf16_f32 v239, v206, v207\n"
    "buffer_store_dwordx4 v[236:239], v9, s[24:27], s75 offen sc1\n"
    "s_waitcnt vmcnt(0)\n"
    "v_mov_b32 v24, s60\n"
    "v_mov_b32 v25, 1\n"
    "s_mov_b64 exec, 1\n"
    "ds_add_rtn_u32 v10, v24, v25\n"
    "s_mov_b64 exec, -1\n"
    "s_waitcnt lgkmcnt(0)\n"
    "v_readfirstlane_b32 s75, v10\n"
    "v_mov_b32 v25, s69\n"
    "v_mov_b32 v24, 0\n"
    "s_cmp_eq_u32 s75, s59\n"
    "s_cselect_b32 s75, s70, 0x40000000\n"
    "s_mov_b64 exec, 1\n"
    "buffer_store_dword v25, v24, s[24:27], s75 offen sc1\n"
    "s_mov_b64 exec, -1\n"
    "s_mov_b32 s56, 0x40000000\n"
    "s_mov_b32 s70, 0x40000000\n"
    "s_mov_b32 s60, 122424\n"
    "g2_%=:\n"
    "s_setprio 2\n"
    "ds_read_b64_tr_b16 v[208:209], v16 offset:0\n"
    "ds_read_b64_tr_b16 v[210:211], v16 offset:512\n"
    "ds_read_b64_tr_b16 v[212:213], v17 offset:0\n"
    "ds_read_b64_tr_b16 v[214:215], v17 offset:512\n"
    "ds_read_b64_tr_b16 v[216:217], v16 offset:4096\n"
    "ds_read_b64_tr_b16 v[218:219], v16 offset:4608\n"
    "ds_read_b64_tr_b16 v[220:221], v17 offset:4096\n"
    "ds_read_b64_tr_b16 v[222:223], v17 offset:4608\n"
    "ds_read_b64_tr_b16 v[224:225], v18 offset:0\n"
    "ds_read_b64_tr_b16 v[226:227], v18 offset:512\n"
    "ds_read_b64_tr_b16 v[228:229], v19 offset:0\n"
    "ds_read_b64_tr_b16 v[230:231], v19 offset:512\n"
    "s_waitcnt lgkmcnt(10)\n"
    "v_mfma_f32_32x32x16_bf16 v[144:159], v[208:211], v[96:99], 0\n"
    "ds_read_b64_tr_b16 v[232:233], v18 offset:4096\n"
    "ds_read_b64_tr_b16 v[234:235], v18 offset:4608\n"
    "s_add_u32 m0, s66, 0\n"
    "v_mfma_f32_16x16x32_bf16 v[240:243], v[208:211], v[244:247], 0\n"
    "s_waitcnt lgkmcnt(10)\n"
    "v_mfma_f32_32x32x16_bf16 v[160:175], v[212:215], v[96:99], 0\n"
    "ds_read_b64_tr_b16 v[236:237], v19 offset:4096\n"
    "ds_read_b64_tr_b16 v[238:239], v19 offset:4608\n"
    "s_add_u32 s74, s18, 0\n"
    "buffer_load_dwordx4 v12, s[36:39], s74 offen lds\n"
    "s_waitcnt lgkmcnt(10)\n"
    "v_mfma_f32_32x32x16_bf16 v[144:159], v[216:219], v[104:107], v[144:159]\n"
    "ds_read_b64_tr_b16 v[208:209], v16 offset:16384\n"
    "ds_read_b64_tr_b16 v[210:211], v16 offset:16896\n"
    "v_mfma_f32_16x16x32_bf16 v[240:243], v[216:219], v[244:247], v[240:243]\n"
    "s_waitcnt lgkmcnt(10)\n"
    "v_mfma_f32_32x32x16_bf16 v[160:175], v[220:223], v[104:107], v[160:175]\n"
    "ds_read_b64_tr_b16 v[212:213], v17 offset:16384\n"
    "ds_read_b64_tr_b16 v[214:215], v17 offset:16896\n"
    "s_add_u32 s74, s18, 6144\n"
    "buffer_load_dwordx4 v12, s[36:39], s74 offen offset:1024 lds\n"
    "s_waitcnt lgkmcnt(10)\n"
    "v_mfma_f32_32x32x16_bf16 v[144:159], v[224:227], v[100:103], v[144:159]\n"
    "ds_read_b64_tr_b16 v[216:217], v16 offset:20480\n"
    "ds_read_b64_tr_b16 v[218:219], v16 offset:20992\n"
    "s_add_u32 m0, s66, 2048\n"
    "s_waitcnt lgkmcnt(10)\n"
    "v_mfma_f32_32x32x16_bf16 v[160:175], v[228:231], v[100:103], v[160:175]\n"
    "ds_read_b64_tr_b16 v[220:221], v17 offset:20480\n"
    "ds_read_b64_tr_b16 v[222:223], v17 offset:20992\n"
    "s_add_u32 s74, s18, 14336\n"
    "buffer_load_dwordx4 v12, s[36:39], s74 offen lds\n"
    "s_waitcnt lgkmcnt(10)\n"
    "v_mfma_f32_32x32x16_bf16 v[144:159], v[232:235], v[108:111], v[144:159]\n"
    "ds_read_b64_tr_b16 v[224:225], v18 offset:16384\n"
    "ds_read_b64_tr_b16 v[226:227], v18 offset:16896\n"
    "s_add_u32 s74, s18, 20480\n"
    "s_waitcnt lgkmcnt(10)\n"
    "v_mfma_f32_32x32x16_bf16 v[160:175], v[236:239], v[108:111], v[160:175]\n"
    "ds_read_b64_tr_b16 v[228:229], v19 offset:16384\n"
    "ds_read_b64_tr_b16 v[230:231], v19 offset:16896\n"
    "buffer_load_dwordx4 v12, s[36:39], s74 offen offset:1024 lds\n"
    "s_waitcnt lgkmcnt(10)\n"
    "v_mfma_f32_32x32x16_bf16 v[144:159], v[208:211], v[112:115], v[144:159]\n"
    "ds_read_b64_tr_b16 v[232:233], v18 offset:20480\n"
    "ds_read_b64_tr_b16 v[234:235], v18 offset:20992\n"
    "s_add_u32 m0, s66, 4096\n"
    "v_mfma_f32_16x16x32_bf16 v[240:243], v[208:211], v[244:247], v[240:243]\n"
    "s_waitcnt lgkmcnt(10)\n"
    "v_mfma_f32_32x32x16_bf16 v[160:175], v[212:215], v[112:115], v[160:175]\n"
    "ds_read_b64_tr_b16 v[236:237], v19 offset:20480\n"
    "ds_read_b64_tr_b16 v[238:239], v19 offset:20992\n"
    "s_add_u32 s74, s18, 28672\n"
    "buffer_load_dwordx4 v12, s[36:39], s74 offen lds\n"
    "v_mov_b32 v24, 122432\n"
    "v_mov_b32 v25, 1\n"
    "s_mov_b64 exec, 1\n"
    "ds_add_u32 v24, v25\n"
    "s_mov_b64 exec, -1\n"
    "s_waitcnt lgkmcnt(11)\n"
    "v_mfma_f32_32x32x16_bf16 v[144:159], v[216:219], v[120:123], v[144:159]\n"
    "ds_read_b64_tr_b16 v[208:209], v16 offset:32768\n"
    "ds_read_b64_tr_b16 v[210:211], v16 offset:33280\n"
    "v_mfma_f32_16x16x32_bf16 v[240:243], v[216:219], v[244:247], v[240:243]\n"
    "s_waitcnt lgkmcnt(11)\n"
    "v_mfma_f32_32x32x16_bf16 v[160:175], v[220:223], v[120:123], v[160:175]\n"
    "ds_read_b64_tr_b16 v[212:213], v17 offset:32768\n"
    "ds_read_b64_tr_b16 v[214:215], v17 offset:33280\n"
    "s_add_u32 s74, s18, 34816\n"
    "buffer_load_dwordx4 v12, s[36:39], s74 offen offset:1024 lds\n"
    "s_waitcnt lgkmcnt(11)\n"
    "v_mfma_f32_32x32x16_bf16 v[144:159], v[224:227], v[116:119], v[144:159]\n"
    "ds_read_b64_tr_b16 v[216:217], v16 offset:36864\n"
    "ds_read_b64_tr_b16 v[218:219], v16 offset:37376\n"
    "s_add_u32 m0, s66, 6144\n"
    "s_waitcnt lgkmcnt(11)\n"
    "v_mfma_f32_32x32x16_bf16 v[160:175], v[228:231], v[116:119], v[160:175]\n"
    "ds_read_b64_tr_b16 v[220:221], v17 offset:36864\n"
    "ds_read_b64_tr_b16 v[222:223], v17 offset:37376\n"
    "s_add_u32 s74, s18, 43008\n"
    "buffer_load_dwordx4 v12, s[36:39], s74 offen lds\n"
    "s_waitcnt lgkmcnt(11)\n"
    "v_mfma_f32_32x32x16_bf16 v[144:159], v[232:235], v[124:127], v[144:159]\n"
    "ds_read_b64_tr_b16 v[224:225], v18 offset:32768\n"
    "ds_read_b64_tr_b16 v[226:227], v18 offset:33280\n"
    "s_add_u32 s74, s18, 49152\n"
    "s_waitcnt lgkmcnt(11)\n"
    "v_mfma_f32_32x32x16_bf16 v[160:175], v[236:239], v[124:127], v[160:175]\n"
    "ds_read_b64_tr_b16 v[228:229], v19 offset:32768\n"
    "ds_read_b64_tr_b16 v[230:231], v19 offset:33280\n"
    "buffer_load_dwordx4 v12, s[36:39], s74 offen offset:1024 lds\n"
    "s_waitcnt lgkmcnt(10)\n"
    "v_mfma_f32_32x32x16_bf16 v[144:159], v[208:211], v[128:131], v[144:159]\n"
    "ds_read_b64_tr_b16 v[232:233], v18 offset:36864\n"
    "ds_read_b64_tr_b16 v[234:235], v18 offset:37376\n"
    "v_mfma_f32_16x16x32_bf16 v[240:243], v[208:211], v[244:247], v[240:243]\n"
    "s_waitcnt lgkmcnt(10)\n"
    "v_mfma_f32_32x32x16_bf16 v[160:175], v[212:215], v[128:131], v[160:175]\n"
    "ds_read_b64_tr_b16 v[236:237], v19 offset:36864\n"
    "ds_read_b64_tr_b16 v[238:239], v19 offset:37376\n"
    "s_waitcnt lgkmcnt(10)\n"
    "v_mfma_f32_32x32x16_bf16 v[144:159], v[216:219], v[136:139], v[144:159]\n"
    "ds_read_b64_tr_b16 v[208:209], v16 offset:49152\n"
    "ds_read_b64_tr_b16 v[210:211], v16 offset:49664\n"
    "v_mfma_f32_16x16x32_bf16 v[240:243], v[216:219], v[244:247], v[240:243]\n"
    "s_waitcnt lgkmcnt(10)\n"
    "v_mfma_f32_32x32x16_bf16 v[160:175], v[220:223], v[136:139], v[160:175]\n"
    "ds_read_b64_tr_b16 v[212:213], v17 offset:49152\n"
    "ds_read_b64_tr_b16 v[214:215], v17 offset:49664\n"
    "s_waitcnt lgkmcnt(10)\n"
    "v_mfma_f32_32x32x16_bf16 v[144:159], v[224:227], v[132:135], v[144:159]\n"
    "ds_read_b64_tr_b16 v[216:217], v16 offset:53248\n"
    "ds_read_b64_tr_b16 v[218:219], v16 offset:53760\n"
    "s_waitcnt lgkmcnt(10)\n"
    "v_mfma_f32_32x32x16_bf16 v[160:175], v[228:231], v[132:135], v[160:175]\n"
    "ds_read_b64_tr_b16 v[220:221], v17 offset:53248\n"
    "ds_read_b64_tr_b16 v[222:223], v17 offset:53760\n"
    "s_waitcnt lgkmcnt(10)\n"
    "v_mfma_f32_32x32x16_bf16 v[144:159], v[232:235], v[140:143], v[144:159]\n"
    "ds_read_b64_tr_b16 v[224:225], v18 offset:49152\n"
    "ds_read_b64_tr_b16 v[226:227], v18 offset:49664\n"
    "s_waitcnt lgkmcnt(10)\n"
    "v_mfma_f32_32x32x16_bf16 v[160:175], v[236:239], v[140:143], v[160:175]\n"
    "ds_read_b64_tr_b16 v[228:229], v19 offset:49152\n"
    "ds_read_b64_tr_b16 v[230:231], v19 offset:49664\n"
    "s_waitcnt lgkmcnt(10)\n"
    "v_mfma_f32_32x32x16_bf16 v[144:159], v[208:211], v[64:67], v[144:159]\n"
    "ds_read_b64_tr_b16 v[232:233], v18 offset:53248\n"
    "ds_read_b64_tr_b16 v[234:235], v18 offset:53760\n"
    "v_mfma_f32_16x16x32_bf16 v[240:243], v[208:211], v[244:247], v[240:243]\n"
    "s_waitcnt lgkmcnt(10)\n"
    "v_mfma_f32_32x32x16_bf16 v[160:175], v[212:215], v[64:67], v[160:175]\n"
    "ds_read_b64_tr_b16 v[236:237], v19 offset:53248\n"
    "ds_read_b64_tr_b16 v[238:239], v19 offset:53760\n"
    "s_waitcnt lgkmcnt(10)\n"
    "v_mfma_f32_32x32x16_bf16 v[144:159], v[216:219], v[72:75], v[144:159]\n"
    "v_add_u32 v16, 0x10000, v16\n"
    "v_add_u32 v17, 0x10000, v17\n"
    "ds_read_b64_tr_b16 v[208:209], v16 offset:0\n"
    "ds_read_b64_tr_b16 v[210:211], v16 offset:512\n"
    "v_mfma_f32_16x16x32_bf16 v[240:243], v[216:219], v[244:247], v[240:243]\n"
    "s_waitcnt lgkmcnt(10)\n"
    "v_mfma_f32_32x32x16_bf16 v[160:175], v[220:223], v[72:75], v[160:175]\n"
    "ds_read_b64_tr_b16 v[212:213], v17 offset:0\n"
    "ds_read_b64_tr_b16 v[214:215], v17 offset:512\n"
    "s_waitcnt lgkmcnt(10)\n"
    "v_mfma_f32_32x32x16_bf16 v[144:159], v[224:227], v[68:71], v[144:159]\n"
    "ds_read_b64_tr_b16 v[216:217], v16 offset:4096\n"
    "ds_read_b64_tr_b16 v[218:219], v16 offset:4608\n"
    "s_waitcnt lgkmcnt(10)\n"
    "v_mfma_f32_32x32x16_bf16 v[160:175], v[228:231], v[68:71], v[160:175]\n"
    "ds_read_b64_tr_b16 v[220:221], v17 offset:4096\n"
    "ds_read_b64_tr_b16 v[222:223], v17 offset:4608\n"
    "v_mov_b32 v9, 122376\n"
    "ds_read_b32 v8, v9\n"
    "s_waitcnt lgkmcnt(11)\n"
    "v_mfma_f32_32x32x16_bf16 v[144:159], v[232:235], v[76:79], v[144:159]\n"
    "v_add_u32 v18, 0x10000, v18\n"
    "v_add_u32 v19, 0x10000, v19\n"
    "ds_read_b64_tr_b16 v[224:225], v18 offset:0\n"
    "ds_read_b64_tr_b16 v[226:227], v18 offset:512\n"
    "s_waitcnt lgkmcnt(11)\n"
    "v_mfma_f32_32x32x16_bf16 v[160:175], v[236:239], v[76:79], v[160:175]\n"
    "ds_read_b64_tr_b16 v[228:229], v19 offset:0\n"
    "ds_read_b64_tr_b16 v[230:231], v19 offset:512\n"
    "s_waitcnt lgkmcnt(11)\n"
    "v_mfma_f32_32x32x16_bf16 v[144:159], v[208:211], v[80:83], v[144:159]\n"
    "ds_read_b64_tr_b16 v[232:233], v18 offset:4096\n"
    "ds_read_b64_tr_b16 v[234:235], v18 offset:4608\n"
    "v_mfma_f32_16x16x32_bf16 v[240:243], v[208:211], v[244:247], v[240:243]\n"
    "s_waitcnt lgkmcnt(11)\n"
    "v_mfma_f32_32x32x16_bf16 v[160:175], v[212:215], v[80:83], v[160:175]\n"
    "ds_read_b64_tr_b16 v[236:237], v19 offset:4096\n"
    "ds_read_b64_tr_b16 v[238:239], v19 offset:4608\n"
    "s_waitcnt lgkmcnt(8)\n"
    "v_readfirstlane_b32 s75, v8\n"
    "s_nop 0\n"
    "s_cmp_ge_u32 s75, 4\n"
    "s_cbranch_scc1 cok21_%=\n"
    "s_mov_b32 s79, 0x100000\n"
    "csp22_%=:\n"
    "ds_read_b32 v8, v9\n"
    "s_sub_u32 s79, s79, 1\n"
    "s_cbranch_scc1 cok21_%=\n"
    "s_sleep 1\n"
    "s_waitcnt lgkmcnt(0)\n"
    "v_readfirstlane_b32 s75, v8\n"
    "s_nop 0\n"
    "s_cmp_ge_u32 s75, 4\n"
    "s_cbranch_scc0 csp22_%=\n"
    "cok21_%=:\n"
    "v_mfma_f32_32x32x16_bf16 v[144:159], v[216:219], v[84:87], v[144:159]\n"
    "ds_read_b64_tr_b16 v[208:209], v16 offset:16384\n"
    "ds_read_b64_tr_b16 v[210:211], v16 offset:16896\n"
    "v_mfma_f32_16x16x32_bf16 v[240:243], v[216:219], v[244:247], v[240:243]\n"
    "v_mfma_f32_32x32x16_bf16 v[160:175], v[220:223], v[84:87], v[160:175]\n"
    "ds_read_b64_tr_b16 v[212:213], v17 offset:16384\n"
    "ds_read_b64_tr_b16 v[214:215], v17 offset:16896\n"
    "s_waitcnt lgkmcnt(10)\n"
    "v_mfma_f32_32x32x16_bf16 v[144:159], v[224:227], v[40:43], v[144:159]\n"
    "ds_read_b64_tr_b16 v[216:217], v16 offset:20480\n"
    "ds_read_b64_tr_b16 v[218:219], v16 offset:20992\n"
    "s_waitcnt lgkmcnt(10)\n"
    "v_mfma_f32_32x32x16_bf16 v[160:175], v[228:231], v[40:43], v[160:175]\n"
    "ds_read_b64_tr_b16 v[220:221], v17 offset:20480\n"
    "ds_read_b64_tr_b16 v[222:223], v17 offset:20992\n"
    "s_waitcnt lgkmcnt(10)\n"
    "v_mfma_f32_32x32x16_bf16 v[144:159], v[232:235], v[44:47], v[144:159]\n"
    "ds_read_b64_tr_b16 v[224:225], v16 offset:32768\n"
    "ds_read_b64_tr_b16 v[226:227], v16 offset:33280\n"
    "s_waitcnt lgkmcnt(10)\n"
    "v_mfma_f32_32x32x16_bf16 v[160:175], v[236:239], v[44:47], v[160:175]\n"
    "ds_read_b64_tr_b16 v[228:229], v17 offset:32768\n"
    "ds_read_b64_tr_b16 v[230:231], v17 offset:33280\n"
    "s_waitcnt lgkmcnt(10)\n"
    "v_mfma_f32_32x32x16_bf16 v[144:159], v[208:211], v[88:91], v[144:159]\n"
    "ds_read_b64_tr_b16 v[232:233], v16 offset:36864\n"
    "ds_read_b64_tr_b16 v[234:235], v16 offset:37376\n"
    "v_mfma_f32_16x16x32_bf16 v[240:243], v[208:211], v[244:247], v[240:243]\n"
    "s_waitcnt lgkmcnt(10)\n"
    "v_mfma_f32_32x32x16_bf16 v[160:175], v[212:215], v[88:91], v[160:175]\n"
    "ds_read_b64_tr_b16 v[236:237], v17 offset:36864\n"
    "ds_read_b64_tr_b16 v[238:239], v17 offset:37376\n"
    "s_waitcnt lgkmcnt(10)\n"
    "v_mfma_f32_32x32x16_bf16 v[144:159], v[216:219], v[92:95], v[144:159]\n"
    "ds_read_b64_tr_b16 v[208:209], v18 offset:16384\n"
    "ds_read_b64_tr_b16 v[210:211], v18 offset:16896\n"
    "v_mfma_f32_16x16x32_bf16 v[240:243], v[216:219], v[244:247], v[240:243]\n"
    "s_waitcnt lgkmcnt(10)\n"
    "v_mfma_f32_32x32x16_bf16 v[160:175], v[220:223], v[92:95], v[160:175]\n"
    "ds_read_b64_tr_b16 v[212:213], v19 offset:16384\n"
    "ds_read_b64_tr_b16 v[214:215], v19 offset:16896\n"
    "s_waitcnt lgkmcnt(10)\n"
    "v_mfma_f32_32x32x16_bf16 v[144:159], v[224:227], v[32:35], v[144:159]\n"
    "ds_read_b64_tr_b16 v[216:217], v18 offset:20480\n"
    "ds_read_b64_tr_b16 v[218:219], v18 offset:20992\n"
    "v_mfma_f32_16x16x32_bf16 v[240:243], v[224:227], v[244:247], v[240:243]\n"
    "s_waitcnt lgkmcnt(10)\n"
    "v_mfma_f32_32x32x16_bf16 v[160:175], v[228:231], v[32:35], v[160:175]\n"
    "ds_read_b64_tr_b16 v[220:221], v19 offset:20480\n"
    "ds_read_b64_tr_b16 v[222:223], v19 offset:20992\n"
    "s_waitcnt lgkmcnt(10)\n"
    "v_mfma_f32_32x32x16_bf16 v[144:159], v[232:235], v[36:39], v[144:159]\n"
    "ds_read_b64_tr_b16 v[224:225], v18 offset:32768\n"
    "ds_read_b64_tr_b16 v[226:227], v18 offset:33280\n"
    "v_mfma_f32_16x16x32_bf16 v[240:243], v[232:235], v[244:247], v[240:243]\n"
    "s_waitcnt lgkmcnt(10)\n"
    "v_mfma_f32_32x32x16_bf16 v[160:175], v[236:239], v[36:39], v[160:175]\n"
    "ds_read_b64_tr_b16 v[228:229], v19 offset:32768\n"
    "ds_read_b64_tr_b16 v[230:231], v19 offset:33280\n"
    "s_waitcnt lgkmcnt(10)\n"
    "v_mfma_f32_32x32x16_bf16 v[144:159], v[208:211], v[48:51], v[144:159]\n"
    "ds_read_b64_tr_b16 v[232:233], v18 offset:36864\n"
    "ds_read_b64_tr_b16 v[234:235], v18 offset:37376\n"
    "s_waitcnt lgkmcnt(10)\n"
    "v_mfma_f32_32x32x16_bf16 v[160:175], v[212:215], v[48:51], v[160:175]\n"
    "ds_read_b64_tr_b16 v[236:237], v19 offset:36864\n"
    "ds_read_b64_tr_b16 v[238:239], v19 offset:37376\n"
    "v_cndmask_b32 v240, v240, v241, s[80:81]\n"
    "v_cndmask_b32 v242, v242, v243, s[80:81]\n"
    "v_cndmask_b32 v240, v240, v242, s[82:83]\n"
    "v_and_b32 v241, s77, v240\n"
    "v_sub_f32 v241, v240, v241\n"
    "v_and_b32 v242, s77, v241\n"
    "v_sub_f32 v243, v241, v242\n"
    "v_lshrrev_b32 v240, 16, v240\n"
    "v_or_b32 v242, v240, v242\n"
    "v_lshrrev_b32 v243, 16, v243\n"
    "ds_write_b64 v21, v[242:243]\n"
    "v_mov_b32 v24, s21\n"
    "v_mov_b32 v25, 1\n"
    "s_mov_b64 exec, 1\n"
    "ds_add_u32 v24, v25\n"
    "s_mov_b64 exec, -1\n"
    "v_mov_b32 v242, 0\n"
    "v_mov_b32 v243, 0\n"
    "v_xor_b32 v22, 512, v23\n"
    "v_mov_b32 v24, 122388\n"
    "v_mov_b32 v25, 1\n"
    "s_mov_b64 exec, 1\n"
    "ds_add_u32 v24, v25\n"
    "s_mov_b64 exec, -1\n"
    "s_waitcnt lgkmcnt(13)\n"
    "v_mfma_f32_32x32x16_bf16 v[144:159], v[216:219], v[52:55], v[144:159]\n"
    "s_waitcnt lgkmcnt(11)\n"
    "v_mfma_f32_32x32x16_bf16 v[160:175], v[220:223], v[52:55], v[160:175]\n"
    "s_waitcnt lgkmcnt(9)\n"
    "v_mfma_f32_32x32x16_bf16 v[144:159], v[224:227], v[56:59], v[144:159]\n"
    "s_waitcnt lgkmcnt(7)\n"
    "v_mfma_f32_32x32x16_bf16 v[160:175], v[228:231], v[56:59], v[160:175]\n"
    "v_mov_b32 v9, s21\n"
    "ds_read_b32 v8, v9\n"
    "ds_read_b64 v[240:241], v23\n"
    "ds_read_b64 v[244:245], v22\n"
    "s_waitcnt lgkmcnt(8)\n"
    "v_mfma_f32_32x32x16_bf16 v[144:159], v[232:235], v[60:63], v[144:159]\n"
    "s_waitcnt lgkmcnt(6)\n"
    "v_mfma_f32_32x32x16_bf16 v[160:175], v[236:239], v[60:63], v[160:175]\n"
    "s_waitcnt lgkmcnt(2)\n"
    "v_readfirstlane_b32 s75, v8\n"
    "s_nop 0\n"
    "s_cmp_ge_u32 s75, 4\n"
    "s_cbranch_scc1 cok23_%=\n"
    "s_mov_b32 s79, 0x100000\n"
    "csp24_%=:\n"
    "ds_read_b32 v8, v9\n"
    "s_sub_u32 s79, s79, 1\n"
    "s_cbranch_scc1 cok23_%=\n"
    "s_sleep 1\n"
    "s_waitcnt lgkmcnt(0)\n"
    "v_readfirstlane_b32 s75, v8\n"
    "s_nop 0\n"
    "s_cmp_ge_u32 s75, 4\n"
    "s_cbranch_scc0 csp24_%=\n"
    "ds_read_b64 v[240:241], v23\n"
    "ds_read_b64 v[244:245], v22\n"
    "s_waitcnt lgkmcnt(0)\n"
    "cok23_%=:\n"
    "s_waitcnt lgkmcnt(1)\n"
    "v_mfma_f32_32x32x16_bf16 v[144:159], v[240:243], v[248:251], v[144:159]\n"
    "s_waitcnt lgkmcnt(0)\n"
    "v_mfma_f32_32x32x16_bf16 v[160:175], v[244:247], v[248:251], v[160:175]\n"
    "s_setprio 2\n"
    "buffer_store_dwordx4 v[0:3], v26, s[24:27], s56 offen sc1\n"
    "s_nop 7\n"
    "v_mul_f32 v208, v144, v144\n"
    "v_mul_f32 v209, v145, v145\n"
    "v_mul_f32 v210, v146, v146\n"
    "v_mul_f32 v211, v147, v147\n"
    "v_mul_f32 v212, v148, v148\n"
    "v_mul_f32 v213, v149, v149\n"
    "v_mul_f32 v214, v150, v150\n"
    "v_mul_f32 v215, v151, v151\n"
    "v_xor_b32 v26, 1024, v26\n"
    "buffer_store_dwordx4 v[4:7], v26, s[24:27], s56 offen sc1\n"
    "v_fma_f32 v208, v208, v253, v252\n"
    "v_fma_f32 v209, v209, v253, v252\n"
    "v_fma_f32 v210, v210, v253, v252\n"
    "v_fma_f32 v211, v211, v253, v252\n"
    "v_fma_f32 v212, v212, v253, v252\n"
    "v_fma_f32 v213, v213, v253, v252\n"
    "v_fma_f32 v214, v214, v253, v252\n"
    "v_fma_f32 v215, v215, v253, v252\n"
    "v_xor_b32 v26, 3072, v26\n"
    "buffer_store_dwordx4 v[176:179], v26, s[24:27], s56 offen sc1\n"
    "v_mul_f32 v208, v208, v144\n"
    "v_mul_f32 v209, v209, v145\n"
    "v_mul_f32 v210, v210, v146\n"
    "v_mul_f32 v211, v211, v147\n"
    "v_mul_f32 v212, v212, v148\n"
    "v_mul_f32 v213, v213, v149\n"
    "v_mul_f32 v214, v214, v150\n"
    "v_mul_f32 v215, v215, v151\n"
    "v_xor_b32 v26, 1024, v26\n"
    "buffer_store_dwordx4 v[192:195], v26, s[24:27], s56 offen sc1\n"
    "v_exp_f32 v208, v208\n"
    "v_mul_f32 v152, v144, v152\n"
    "v_exp_f32 v209, v209\n"
    "v_mul_f32 v153, v145, v153\n"
    "v_exp_f32 v210, v210\n"
    "v_mul_f32 v154, v146, v154\n"
    "v_exp_f32 v211, v211\n"
    "v_mul_f32 v155, v147, v155\n"
    "v_exp_f32 v212, v212\n"
    "v_mul_f32 v156, v148, v156\n"
    "v_exp_f32 v213, v213\n"
    "v_mul_f32 v157, v149, v157\n"
    "v_exp_f32 v214, v214\n"
    "v_mul_f32 v158, v150, v158\n"
    "v_exp_f32 v215, v215\n"
    "v_mul_f32 v159, v151, v159\n"
    "v_fma_f32 v208, v208, v254, v254\n"
    "v_fma_f32 v209, v209, v254, v254\n"
    "v_fma_f32 v210, v210, v254, v254\n"
    "v_fma_f32 v211, v211, v254, v254\n"
    "v_fma_f32 v212, v212, v254, v254\n"
    "v_fma_f32 v213, v213, v254, v254\n"
    "v_fma_f32 v214, v214, v254, v254\n"
    "v_fma_f32 v215, v215, v254, v254\n"
    "v_rcp_f32 v208, v208\n"
    "v_rcp_f32 v209, v209\n"
    "v_rcp_f32 v210, v210\n"
    "v_rcp_f32 v211, v211\n"
    "v_rcp_f32 v212, v212\n"
    "v_rcp_f32 v213, v213\n"
    "v_rcp_f32 v214, v214\n"
    "v_rcp_f32 v215, v215\n"
    "v_mul_f32 v152, v152, v208\n"
    "v_mul_f32 v153, v153, v209\n"
    "v_mul_f32 v154, v154, v210\n"
    "v_mul_f32 v155, v155, v211\n"
    "v_mul_f32 v156, v156, v212\n"
    "v_mul_f32 v157, v157, v213\n"
    "v_mul_f32 v158, v158, v214\n"
    "v_mul_f32 v159, v159, v215\n"
    "v_cvt_pk_bf16_f32 v0, v152, v153\n"
    "v_cvt_pk_bf16_f32 v1, v154, v155\n"
    "v_cvt_pk_bf16_f32 v2, v156, v157\n"
    "v_cvt_pk_bf16_f32 v3, v158, v159\n"
    "v_mul_f32 v208, v160, v160\n"
    "v_mul_f32 v209, v161, v161\n"
    "v_mul_f32 v210, v162, v162\n"
    "v_mul_f32 v211, v163, v163\n"
    "v_mul_f32 v212, v164, v164\n"
    "v_mul_f32 v213, v165, v165\n"
    "v_mul_f32 v214, v166, v166\n"
    "v_mul_f32 v215, v167, v167\n"
    "v_fma_f32 v208, v208, v253, v252\n"
    "v_fma_f32 v209, v209, v253, v252\n"
    "v_fma_f32 v210, v210, v253, v252\n"
    "v_fma_f32 v211, v211, v253, v252\n"
    "v_fma_f32 v212, v212, v253, v252\n"
    "v_fma_f32 v213, v213, v253, v252\n"
    "v_fma_f32 v214, v214, v253, v252\n"
    "v_fma_f32 v215, v215, v253, v252\n"
    "v_mul_f32 v208, v208, v160\n"
    "v_mul_f32 v209, v209, v161\n"
    "v_mul_f32 v210, v210, v162\n"
    "v_mul_f32 v211, v211, v163\n"
    "v_mul_f32 v212, v212, v164\n"
    "v_mul_f32 v213, v213, v165\n"
    "v_mul_f32 v214, v214, v166\n"
    "v_mul_f32 v215, v215, v167\n"
    "v_exp_f32 v208, v208\n"
    "v_mul_f32 v168, v160, v168\n"
    "v_exp_f32 v209, v209\n"
    "v_mul_f32 v169, v161, v169\n"
    "v_exp_f32 v210, v210\n"
    "v_mul_f32 v170, v162, v170\n"
    "v_exp_f32 v211, v211\n"
    "v_mul_f32 v171, v163, v171\n"
    "v_exp_f32 v212, v212\n"
    "v_mul_f32 v172, v164, v172\n"
    "v_exp_f32 v213, v213\n"
    "v_mul_f32 v173, v165, v173\n"
    "v_exp_f32 v214, v214\n"
    "v_mul_f32 v174, v166, v174\n"
    "v_exp_f32 v215, v215\n"
    "v_mul_f32 v175, v167, v175\n"
    "v_fma_f32 v208, v208, v254, v254\n"
    "v_fma_f32 v209, v209, v254, v254\n"
    "v_fma_f32 v210, v210, v254, v254\n"
    "v_fma_f32 v211, v211, v254, v254\n"
    "v_fma_f32 v212, v212, v254, v254\n"
    "v_fma_f32 v213, v213, v254, v254\n"
    "v_fma_f32 v214, v214, v254, v254\n"
    "v_fma_f32 v215, v215, v254, v254\n"
    "v_rcp_f32 v208, v208\n"
    "v_rcp_f32 v209, v209\n"
    "v_rcp_f32 v210, v210\n"
    "v_rcp_f32 v211, v211\n"
    "v_rcp_f32 v212, v212\n"
    "v_rcp_f32 v213, v213\n"
    "v_rcp_f32 v214, v214\n"
    "v_rcp_f32 v215, v215\n"
    "v_mul_f32 v168, v168, v208\n"
    "v_mul_f32 v169, v169, v209\n"
    "v_mul_f32 v170, v170, v210\n"
    "v_mul_f32 v171, v171, v211\n"
    "s_waitcnt vmcnt(0)\n"
    "v_mov_b32 v24, s60\n"
    "v_mov_b32 v25, 1\n"
    "s_mov_b64 exec, 1\n"
    "ds_add_rtn_u32 v10, v24, v25\n"
    "s_mov_b64 exec, -1\n"
    "v_mul_f32 v172, v172, v212\n"
    "v_mul_f32 v173, v173, v213\n"
    "v_mul_f32 v174, v174, v214\n"
    "v_mul_f32 v175, v175, v215\n"
    "v_cvt_pk_bf16_f32 v4, v168, v169\n"
    "v_cvt_pk_bf16_f32 v5, v170, v171\n"
    "v_cvt_pk_bf16_f32 v6, v172, v173\n"
    "v_cvt_pk_bf16_f32 v7, v174, v175\n"
    "s_waitcnt lgkmcnt(0)\n"
    "v_readfirstlane_b32 s75, v10\n"
    "v_mov_b32 v25, s69\n"
    "v_mov_b32 v24, 0\n"
    "s_cmp_eq_u32 s75, s59\n"
    "s_cselect_b32 s75, s70, 0x40000000\n"
    "s_mov_b64 exec, 1\n"
    "buffer_store_dword v25, v24, s[24:27], s75 offen sc1\n"
    "s_mov_b64 exec, -1\n"
    "s_bfe_u32 s75, s3, 0x20006\n"
    "s_lshl_b32 s75, s75, 11\n"
    "v_add_u32 v252, s75, v15\n"
    "v_xor_b32 v253, 1024, v27\n"
    "s_sub_u32 s78, s68, 2048\n"
    "v_mov_b32 v255, 122412\n"
    "ds_read_b32 v254, v255\n"
    "s_waitcnt lgkmcnt(0)\n"
    "v_readfirstlane_b32 s75, v254\n"
    "s_nop 0\n"
    "s_cmp_ge_u32 s75, 4\n"
    "s_cbranch_scc1 cok25_%=\n"
    "s_mov_b32 s79, 0x100000\n"
    "csp26_%=:\n"
    "ds_read_b32 v254, v255\n"
    "s_sub_u32 s79, s79, 1\n"
    "s_cbranch_scc1 cok25_%=\n"
    "s_sleep 1\n"
    "s_waitcnt lgkmcnt(0)\n"
    "v_readfirstlane_b32 s75, v254\n"
    "s_nop 0\n"
    "s_cmp_ge_u32 s75, 4\n"
    "s_cbranch_scc0 csp26_%=\n"
    "cok25_%=:\n"
    "ds_read_b128 v[16:19], v252\n"
    "ds_read_b128 v[20:23], v252 offset:1024\n"
    "buffer_store_dwordx4 v[0:3], v27, s[24:27], s68 offen sc1\n"
    "buffer_store_dwordx4 v[4:7], v253, s[24:27], s68 offen sc1\n"
    "s_waitcnt lgkmcnt(1)\n"
    "buffer_store_dwordx4 v[16:19], v27, s[24:27], s78 offen sc1\n"
    "s_waitcnt lgkmcnt(0)\n"
    "buffer_store_dwordx4 v[20:23], v253, s[24:27], s78 offen sc1\n"
    "v_mov_b32 v11, 122388\n"
    "ds_read_b32 v10, v11\n"
    "buffer_load_dword v8, v30, s[24:27], 0 offen sc1\n"
    "s_waitcnt vmcnt(0)\n"
    "v_cmp_ne_u32 vcc, s69, v8\n"
    "s_mov_b32 s79, 0x40000\n"
    "s_cmp_eq_u64 vcc, 0\n"
    "s_cbranch_scc1 pok27_%=\n"
    "psp28_%=:\n"
    "buffer_load_dword v8, v30, s[24:27], 0 offen sc1\n"
    "s_sub_u32 s79, s79, 1\n"
    "s_cbranch_scc1 pok27_%=\n"
    "s_waitcnt vmcnt(0)\n"
    "v_cmp_ne_u32 vcc, s69, v8\n"
    "s_sleep 1\n"
    "s_cmp_eq_u64 vcc, 0\n"
    "s_cbranch_scc0 psp28_%=\n"
    "pok27_%=:\n"
    "s_waitcnt lgkmcnt(0)\n"
    "v_readfirstlane_b32 s75, v10\n"
    "s_nop 0\n"
    "s_cmp_ge_u32 s75, 8\n"
    "s_cbranch_scc1 cok29_%=\n"
    "s_mov_b32 s79, 0x100000\n"
    "csp30_%=:\n"
    "ds_read_b32 v10, v11\n"
    "s_sub_u32 s79, s79, 1\n"
    "s_cbranch_scc1 cok29_%=\n"
    "s_sleep 1\n"
    "s_waitcnt lgkmcnt(0)\n"
    "v_readfirstlane_b32 s75, v10\n"
    "s_nop 0\n"
    "s_cmp_ge_u32 s75, 8\n"
    "s_cbranch_scc0 csp30_%=\n"
    "cok29_%=:\n"
    "v_mov_b32 v24, 122388\n"
    "v_mov_b32 v25, 1\n"
    "s_mov_b64 exec, 1\n"
    "ds_add_u32 v24, v25\n"
    "s_mov_b64 exec, -1\n"
    "s_add_u32 s72, s16, 0\n"
    "s_add_u32 s73, s72, s64\n"
    "buffer_load_dwordx4 v[64:67], v15, s[24:27], s72 offen sc1\n"
    "buffer_load_dwordx4 v[68:71], v15, s[24:27], s73 offen sc1\n"
    "buffer_load_dwordx4 v[72:75], v15, s[24:27], s72 offen offset:1024 sc1\n"
    "buffer_load_dwordx4 v[76:79], v15, s[24:27], s73 offen offset:1024 sc1\n"
    "buffer_load_dwordx4 v[80:83], v15, s[24:27], s72 offen offset:2048 sc1\n"
    "buffer_load_dwordx4 v[84:87], v15, s[24:27], s73 offen offset:2048 sc1\n"
    "buffer_load_dwordx4 v[88:91], v15, s[24:27], s72 offen offset:3072 sc1\n"
    "buffer_load_dwordx4 v[92:95], v15, s[24:27], s73 offen offset:3072 sc1\n"
    "s_add_u32 m0, s66, 8192\n"
    "s_add_u32 s74, s18, 57344\n"
    "buffer_load_dwordx4 v12, s[36:39], s74 offen lds\n"
    "s_add_u32 s74, s18, 63488\n"
    "buffer_load_dwordx4 v12, s[36:39], s74 offen offset:1024 lds\n"
    "s_add_u32 m0, s67, 0\n"
    "s_add_u32 s74, s18, 71680\n"
    "buffer_load_dwordx4 v12, s[36:39], s74 offen lds\n"
    "s_add_u32 s74, s18, 77824\n"
    "buffer_load_dwordx4 v12, s[36:39], s74 offen offset:1024 lds\n"
    "s_cmp_eq_u32 s20, 0\n"
    "s_cbranch_scc1 idle_%=\n"
    "v_add_u32 v8, s66, v13\n"
    "v_add_u32 v9, s66, v14\n"
    "v_add_u32 v10, s67, v13\n"
    "v_add_u32 v11, s67, v14\n"
    "v_and_b32 v29, 512, v15\n"
    "v_mad_u32_u24 v26, v29, 7, v12\n"
    "v_mad_u32_u24 v29, v29, 42, v12\n"
    "s_setprio 0\n"
    "ds_read_b64_tr_b16 v[208:209], v8 offset:0\n"
    "ds_read_b64_tr_b16 v[210:211], v8 offset:512\n"
    "ds_read_b64_tr_b16 v[212:213], v9 offset:0\n"
    "ds_read_b64_tr_b16 v[214:215], v9 offset:512\n"
    "ds_read_b64_tr_b16 v[216:217], v8 offset:2048\n"
    "ds_read_b64_tr_b16 v[218:219], v8 offset:2560\n"
    "ds_read_b64_tr_b16 v[220:221], v9 offset:2048\n"
    "ds_read_b64_tr_b16 v[222:223], v9 offset:2560\n"
    "s_waitcnt vmcnt(11) lgkmcnt(6)\n"
    "v_mfma_f32_32x32x16_bf16 v[176:191], v[208:211], v[64:67], 0\n"
    "s_waitcnt vmcnt(10)\n"
    "v_mfma_f32_32x32x16_bf16 v[192:207], v[208:211], v[68:71], 0\n"
    "s_add_u32 s72, s16, 4096\n"
    "s_add_u32 s73, s72, s64\n"
    "buffer_load_dwordx4 v[96:99], v15, s[24:27], s72 offen sc1\n"
    "buffer_load_dwordx4 v[100:103], v15, s[24:27], s73 offen sc1\n"
    "ds_read_b64_tr_b16 v[224:225], v8 offset:4096\n"
    "ds_read_b64_tr_b16 v[226:227], v8 offset:4608\n"
    "ds_read_b64_tr_b16 v[228:229], v9 offset:4096\n"
    "ds_read_b64_tr_b16 v[230:231], v9 offset:4608\n"
    "s_sleep 1\n"
    "s_waitcnt lgkmcnt(8)\n"
    "v_mfma_f32_32x32x16_bf16 v[32:47], v[212:215], v[64:67], 0\n"
    "v_mfma_f32_32x32x16_bf16 v[48:63], v[212:215], v[68:71], 0\n"
    "s_add_u32 m0, s67, 2048\n"
    "s_add_u32 s74, s18, 86016\n"
    "buffer_load_dwordx4 v12, s[36:39], s74 offen lds\n"
    "s_add_u32 s74, s18, 92160\n"
    "buffer_load_dwordx4 v12, s[36:39], s74 offen offset:1024 lds\n"
    "s_sleep 1\n"
    "s_setprio 3\n"
    "v_mov_b32 v24, 122392\n"
    "v_mov_b32 v25, 1\n"
    "s_mov_b64 exec, 1\n"
    "ds_add_rtn_u32 v253, v24, v25\n"
    "s_mov_b64 exec, -1\n"
    "s_waitcnt lgkmcnt(0)\n"
    "v_readfirstlane_b32 s75, v253\n"
    "v_mov_b32 v25, s69\n"
    "v_mov_b32 v24, 0\n"
    "s_cmp_eq_u32 s75, 3\n"
    "s_cselect_b32 s75, s71, 0x40000000\n"
    "s_mov_b64 exec, 1\n"
    "buffer_store_dword v25, v24, s[24:27], s75 offen sc1\n"
    "s_mov_b64 exec, -1\n"
    "s_setprio 0\n"
    "s_waitcnt vmcnt(14)\n"
    "v_mfma_f32_32x32x16_bf16 v[176:191], v[216:219], v[72:75], v[176:191]\n"
    "s_waitcnt vmcnt(13)\n"
    "v_mfma_f32_32x32x16_bf16 v[192:207], v[216:219], v[76:79], v[192:207]\n"
    "buffer_load_dwordx4 v[104:107], v15, s[24:27], s72 offen offset:1024 sc1\n"
    "buffer_load_dwordx4 v[108:111], v15, s[24:27], s73 offen offset:1024 sc1\n"
    "ds_read_b64_tr_b16 v[232:233], v8 offset:6144\n"
    "ds_read_b64_tr_b16 v[234:235], v8 offset:6656\n"
    "ds_read_b64_tr_b16 v[236:237], v9 offset:6144\n"
    "ds_read_b64_tr_b16 v[238:239], v9 offset:6656\n"
    "s_sleep 1\n"
    "v_mfma_f32_32x32x16_bf16 v[32:47], v[220:223], v[72:75], v[32:47]\n"
    "v_mfma_f32_32x32x16_bf16 v[48:63], v[220:223], v[76:79], v[48:63]\n"
    "s_add_u32 m0, s67, 4096\n"
    "s_add_u32 s74, s18, 100352\n"
    "buffer_load_dwordx4 v12, s[36:39], s74 offen lds\n"
    "s_add_u32 s74, s18, 106496\n"
    "buffer_load_dwordx4 v12, s[36:39], s74 offen offset:1024 lds\n"
    "s_sleep 1\n"
    "s_waitcnt vmcnt(11)\n"
    "v_mfma_f32_32x32x16_bf16 v[176:191], v[224:227], v[80:83], v[176:191]\n"
    "v_mfma_f32_32x32x16_bf16 v[192:207], v[224:227], v[84:87], v[192:207]\n"
    "buffer_load_dwordx4 v[112:115], v15, s[24:27], s72 offen offset:2048 sc1\n"
    "buffer_load_dwordx4 v[116:119], v15, s[24:27], s73 offen offset:2048 sc1\n"
    "ds_read_b64_tr_b16 v[208:209], v8 offset:8192\n"
    "ds_read_b64_tr_b16 v[210:211], v8 offset:8704\n"
    "ds_read_b64_tr_b16 v[212:213], v9 offset:8192\n"
    "ds_read_b64_tr_b16 v[214:215], v9 offset:8704\n"
    "s_sleep 1\n"
    "v_mfma_f32_32x32x16_bf16 v[32:47], v[228:231], v[80:83], v[32:47]\n"
    "v_mfma_f32_32x32x16_bf16 v[48:63], v[228:231], v[84:87], v[48:63]\n"
    "s_add_u32 m0, s67, 6144\n"
    "s_add_u32 s74, s18, 114688\n"
    "buffer_load_dwordx4 v12, s[36:39], s74 offen lds\n"
    "s_add_u32 s74, s18, 120832\n"
    "buffer_load_dwordx4 v12, s[36:39], s74 offen offset:1024 lds\n"
    "s_sleep 1\n"
    "s_waitcnt vmcnt(13)\n"
    "s_waitcnt lgkmcnt(6)\n"
    "v_mfma_f32_32x32x16_bf16 v[176:191], v[232:235], v[88:91], v[176:191]\n"
    "v_mfma_f32_32x32x16_bf16 v[192:207], v[232:235], v[92:95], v[192:207]\n"
    "buffer_load_dwordx4 v[120:123], v15, s[24:27], s72 offen offset:3072 sc1\n"
    "buffer_load_dwordx4 v[124:127], v15, s[24:27], s73 offen offset:3072 sc1\n"
    "ds_read_b64_tr_b16 v[216:217], v10 offset:0\n"
    "ds_read_b64_tr_b16 v[218:219], v10 offset:512\n"
    "ds_read_b64_tr_b16 v[220:221], v11 offset:0\n"
    "ds_read_b64_tr_b16 v[222:223], v11 offset:512\n"
    "s_sleep 1\n"
    "s_waitcnt lgkmcnt(8)\n"
    "v_mfma_f32_32x32x16_bf16 v[32:47], v[236:239], v[88:91], v[32:47]\n"
    "v_mfma_f32_32x32x16_bf16 v[48:63], v[236:239], v[92:95], v[48:63]\n"
    "s_add_u32 m0, s67, 8192\n"
    "s_add_u32 s74, s18, 129024\n"
    "buffer_load_dwordx4 v12, s[36:39], s74 offen lds\n"
    "s_add_u32 s74, s18, 135168\n"
    "buffer_load_dwordx4 v12, s[36:39], s74 offen offset:1024 lds\n"
    "s_sleep 1\n"
    "s_waitcnt vmcnt(13)\n"
    "s_waitcnt lgkmcnt(6)\n"
    "v_mfma_f32_32x32x16_bf16 v[176:191], v[208:211], v[96:99], v[176:191]\n"
    "v_mfma_f32_32x32x16_bf16 v[192:207], v[208:211], v[100:103], v[192:207]\n"
    "s_add_u32 s72, s16, 8192\n"
    "s_add_u32 s73, s72, s64\n"
    "buffer_load_dwordx4 v[128:131], v15, s[24:27], s72 offen sc1\n"
    "buffer_load_dwordx4 v[132:135], v15, s[24:27], s73 offen sc1\n"
    "ds_read_b64_tr_b16 v[224:225], v10 offset:2048\n"
    "ds_read_b64_tr_b16 v[226:227], v10 offset:2560\n"
    "ds_read_b64_tr_b16 v[228:229], v11 offset:2048\n"
    "ds_read_b64_tr_b16 v[230:231], v11 offset:2560\n"
    "s_sleep 1\n"
    "s_waitcnt lgkmcnt(8)\n"
    "v_mfma_f32_32x32x16_bf16 v[32:47], v[212:215], v[96:99], v[32:47]\n"
    "v_mfma_f32_32x32x16_bf16 v[48:63], v[212:215], v[100:103], v[48:63]\n"
    "s_add_u32 m0, s67, 10240\n"
    "s_add_u32 s74, s18, 143360\n"
    "buffer_load_dwordx4 v12, s[36:39], s74 offen lds\n"
    "s_add_u32 s74, s18, 149504\n"
    "buffer_load_dwordx4 v12, s[36:39], s74 offen offset:1024 lds\n"
    "s_sleep 1\n"
    "s_waitcnt vmcnt(12)\n"
    "s_waitcnt lgkmcnt(6)\n"
    "v_mfma_f32_32x32x16_bf16 v[176:191], v[216:219], v[104:107], v[176:191]\n"
    "v_mfma_f32_32x32x16_bf16 v[192:207], v[216:219], v[108:111], v[192:207]\n"
    "buffer_load_dwordx4 v[136:139], v15, s[24:27], s72 offen offset:1024 sc1\n"
    "buffer_load_dwordx4 v[140:143], v15, s[24:27], s73 offen offset:1024 sc1\n"
    "ds_read_b64_tr_b16 v[232:233], v10 offset:4096\n"
    "ds_read_b64_tr_b16 v[234:235], v10 offset:4608\n"
    "ds_read_b64_tr_b16 v[236:237], v11 offset:4096\n"
    "ds_read_b64_tr_b16 v[238:239], v11 offset:4608\n"
    "s_sleep 1\n"
    "s_waitcnt lgkmcnt(8)\n"
    "v_mfma_f32_32x32x16_bf16 v[32:47], v[220:223], v[104:107], v[32:47]\n"
    "v_mfma_f32_32x32x16_bf16 v[48:63], v[220:223], v[108:111], v[48:63]\n"
    "s_add_u32 m0, s67, 12288\n"
    "s_add_u32 s74, s18, 157696\n"
    "buffer_load_dwordx4 v12, s[36:39], s74 offen lds\n"
    "s_add_u32 s74, s18, 163840\n"
    "buffer_load_dwordx4 v12, s[36:39], s74 offen offset:1024 lds\n"
    "s_sleep 1\n"
    "buffer_load_dword v252, v28, s[24:27], 0 offen sc1\n"
    "s_waitcnt vmcnt(13)\n"
    "s_waitcnt lgkmcnt(6)\n"
    "v_mfma_f32_32x32x16_bf16 v[176:191], v[224:227], v[112:115], v[176:191]\n"
    "v_mfma_f32_32x32x16_bf16 v[192:207], v[224:227], v[116:119], v[192:207]\n"
    "buffer_load_dwordx4 v[144:147], v15, s[24:27], s72 offen offset:2048 sc1\n"
    "buffer_load_dwordx4 v[148:151], v15, s[24:27], s73 offen offset:2048 sc1\n"
    "ds_read_b64_tr_b16 v[208:209], v10 offset:6144\n"
    "ds_read_b64_tr_b16 v[210:211], v10 offset:6656\n"
    "ds_read_b64_tr_b16 v[212:213], v11 offset:6144\n"
    "ds_read_b64_tr_b16 v[214:215], v11 offset:6656\n"
    "s_sleep 1\n"
    "s_waitcnt lgkmcnt(8)\n"
    "v_mfma_f32_32x32x16_bf16 v[32:47], v[228:231], v[112:115], v[32:47]\n"
    "v_mfma_f32_32x32x16_bf16 v[48:63], v[228:231], v[116:119], v[48:63]\n"
    "s_add_u32 m0, s67, 14336\n"
    "s_add_u32 s74, s61, 0\n"
    "buffer_load_dwordx4 v12, s[36:39], s74 offen lds\n"
    "s_add_u32 s74, s61, 6144\n"
    "buffer_load_dwordx4 v12, s[36:39], s74 offen offset:1024 lds\n"
    "s_sleep 1\n"
    "s_waitcnt vmcnt(13)\n"
    "s_waitcnt lgkmcnt(6)\n"
    "v_mfma_f32_32x32x16_bf16 v[176:191], v[232:235], v[120:123], v[176:191]\n"
    "v_mfma_f32_32x32x16_bf16 v[192:207], v[232:235], v[124:127], v[192:207]\n"
    "buffer_load_dwordx4 v[152:155], v15, s[24:27], s72 offen offset:3072 sc1\n"
    "buffer_load_dwordx4 v[156:159], v15, s[24:27], s73 offen offset:3072 sc1\n"
    "ds_read_b64_tr_b16 v[216:217], v10 offset:8192\n"
    "ds_read_b64_tr_b16 v[218:219], v10 offset:8704\n"
    "ds_read_b64_tr_b16 v[220:221], v11 offset:8192\n"
    "ds_read_b64_tr_b16 v[222:223], v11 offset:8704\n"
    "s_sleep 1\n"
    "s_waitcnt lgkmcnt(8)\n"
    "v_mfma_f32_32x32x16_bf16 v[32:47], v[236:239], v[120:123], v[32:47]\n"
    "v_mfma_f32_32x32x16_bf16 v[48:63], v[236:239], v[124:127], v[48:63]\n"
    "s_add_u32 m0, s66, 0\n"
    "s_add_u32 s74, s61, 14336\n"
    "buffer_load_dwordx4 v12, s[36:39], s74 offen lds\n"
    "s_add_u32 s74, s61, 20480\n"
    "buffer_load_dwordx4 v12, s[36:39], s74 offen offset:1024 lds\n"
    "s_sleep 1\n"
    "s_setprio 3\n"
    "s_waitcnt vmcnt(8)\n"
    "v_cmp_ne_u32 vcc, s69, v252\n"
    "s_mov_b32 s79, 0x40000\n"
    "s_cmp_eq_u64 vcc, 0\n"
    "s_cbranch_scc1 pok31_%=\n"
    "psp32_%=:\n"
    "buffer_load_dword v252, v28, s[24:27], 0 offen sc1\n"
    "s_sub_u32 s79, s79, 1\n"
    "s_cbranch_scc1 pok31_%=\n"
    "s_waitcnt vmcnt(0)\n"
    "v_cmp_ne_u32 vcc, s69, v252\n"
    "s_sleep 1\n"
    "s_cmp_eq_u64 vcc, 0\n"
    "s_cbranch_scc0 psp32_%=\n"
    "pok31_%=:\n"
    "s_setprio 0\n"
    "s_waitcnt lgkmcnt(6)\n"
    "v_mfma_f32_32x32x16_bf16 v[176:191], v[208:211], v[128:131], v[176:191]\n"
    "v_mfma_f32_32x32x16_bf16 v[192:207], v[208:211], v[132:135], v[192:207]\n"
    "s_add_u32 s72, s17, 0\n"
    "s_add_u32 s73, s72, s64\n"
    "buffer_load_dwordx4 v[160:163], v15, s[24:27], s72 offen sc1\n"
    "buffer_load_dwordx4 v[164:167], v15, s[24:27], s73 offen sc1\n"
    "ds_read_b64_tr_b16 v[224:225], v10 offset:10240\n"
    "ds_read_b64_tr_b16 v[226:227], v10 offset:10752\n"
    "ds_read_b64_tr_b16 v[228:229], v11 offset:10240\n"
    "ds_read_b64_tr_b16 v[230:231], v11 offset:10752\n"
    "s_sleep 1\n"
    "s_waitcnt lgkmcnt(8)\n"
    "v_mfma_f32_32x32x16_bf16 v[32:47], v[212:215], v[128:131], v[32:47]\n"
    "v_mfma_f32_32x32x16_bf16 v[48:63], v[212:215], v[132:135], v[48:63]\n"
    "s_add_u32 m0, s66, 2048\n"
    "s_add_u32 s74, s61, 28672\n"
    "buffer_load_dwordx4 v12, s[36:39], s74 offen lds\n"
    "s_add_u32 s74, s61, 34816\n"
    "buffer_load_dwordx4 v12, s[36:39], s74 offen offset:1024 lds\n"
    "s_sleep 1\n"
    "s_waitcnt lgkmcnt(6)\n"
    "v_mfma_f32_32x32x16_bf16 v[176:191], v[216:219], v[136:139], v[176:191]\n"
    "v_mfma_f32_32x32x16_bf16 v[192:207], v[216:219], v[140:143], v[192:207]\n"
    "buffer_load_dwordx4 v[168:171], v15, s[24:27], s72 offen offset:1024 sc1\n"
    "buffer_load_dwordx4 v[172:175], v15, s[24:27], s73 offen offset:1024 sc1\n"
    "ds_read_b64_tr_b16 v[232:233], v10 offset:12288\n"
    "ds_read_b64_tr_b16 v[234:235], v10 offset:12800\n"
    "ds_read_b64_tr_b16 v[236:237], v11 offset:12288\n"
    "ds_read_b64_tr_b16 v[238:239], v11 offset:12800\n"
    "s_sleep 1\n"
    "s_waitcnt lgkmcnt(8)\n"
    "v_mfma_f32_32x32x16_bf16 v[32:47], v[220:223], v[136:139], v[32:47]\n"
    "v_mfma_f32_32x32x16_bf16 v[48:63], v[220:223], v[140:143], v[48:63]\n"
    "s_add_u32 m0, s66, 4096\n"
    "s_add_u32 s74, s61, 43008\n"
    "buffer_load_dwordx4 v12, s[36:39], s74 offen lds\n"
    "s_add_u32 s74, s61, 49152\n"
    "buffer_load_dwordx4 v12, s[36:39], s74 offen offset:1024 lds\n"
    "s_sleep 1\n"
    "s_waitcnt vmcnt(12)\n"
    "s_waitcnt lgkmcnt(6)\n"
    "v_mfma_f32_32x32x16_bf16 v[176:191], v[224:227], v[144:147], v[176:191]\n"
    "v_mfma_f32_32x32x16_bf16 v[192:207], v[224:227], v[148:151], v[192:207]\n"
    "buffer_load_dwordx4 v[64:67], v15, s[24:27], s72 offen offset:2048 sc1\n"
    "buffer_load_dwordx4 v[68:71], v15, s[24:27], s73 offen offset:2048 sc1\n"
    "ds_read_b64_tr_b16 v[208:209], v10 offset:14336\n"
    "ds_read_b64_tr_b16 v[210:211], v10 offset:14848\n"
    "ds_read_b64_tr_b16 v[212:213], v11 offset:14336\n"
    "ds_read_b64_tr_b16 v[214:215], v11 offset:14848\n"
    "s_sleep 1\n"
    "s_waitcnt lgkmcnt(8)\n"
    "v_mfma_f32_32x32x16_bf16 v[32:47], v[228:231], v[144:147], v[32:47]\n"
    "v_mfma_f32_32x32x16_bf16 v[48:63], v[228:231], v[148:151], v[48:63]\n"
    "s_add_u32 m0, s66, 6144\n"
    "s_add_u32 s74, s61, 57344\n"
    "buffer_load_dwordx4 v12, s[36:39], s74 offen lds\n"
    "s_add_u32 s74, s61, 63488\n"
    "buffer_load_dwordx4 v12, s[36:39], s74 offen offset:1024 lds\n"
    "s_sleep 1\n"
    "s_waitcnt vmcnt(12)\n"
    "s_waitcnt lgkmcnt(6)\n"
    "v_mfma_f32_32x32x16_bf16 v[176:191], v[232:235], v[152:155], v[176:191]\n"
    "v_mfma_f32_32x32x16_bf16 v[192:207], v[232:235], v[156:159], v[192:207]\n"
    "buffer_load_dwordx4 v[72:75], v15, s[24:27], s72 offen offset:3072 sc1\n"
    "buffer_load_dwordx4 v[76:79], v15, s[24:27], s73 offen offset:3072 sc1\n"
    "ds_read_b64_tr_b16 v[216:217], v8 offset:0\n"
    "ds_read_b64_tr_b16 v[218:219], v8 offset:512\n"
    "ds_read_b64_tr_b16 v[220:221], v9 offset:0\n"
    "ds_read_b64_tr_b16 v[222:223], v9 offset:512\n"
    "s_sleep 1\n"
    "s_waitcnt lgkmcnt(8)\n"
    "v_mfma_f32_32x32x16_bf16 v[32:47], v[236:239], v[152:155], v[32:47]\n"
    "v_mfma_f32_32x32x16_bf16 v[48:63], v[236:239], v[156:159], v[48:63]\n"
    "s_add_u32 m0, s66, 8192\n"
    "s_add_u32 s74, s61, 71680\n"
    "buffer_load_dwordx4 v12, s[36:39], s74 offen lds\n"
    "s_add_u32 s74, s61, 77824\n"
    "buffer_load_dwordx4 v12, s[36:39], s74 offen offset:1024 lds\n"
    "s_sleep 1\n"
    "s_waitcnt vmcnt(12)\n"
    "s_waitcnt lgkmcnt(6)\n"
    "v_mfma_f32_32x32x16_bf16 v[176:191], v[208:211], v[160:163], v[176:191]\n"
    "v_mfma_f32_32x32x16_bf16 v[192:207], v[208:211], v[164:167], v[192:207]\n"
    "s_add_u32 s72, s17, 4096\n"
    "s_add_u32 s73, s72, s64\n"
    "buffer_load_dwordx4 v[80:83], v15, s[24:27], s72 offen sc1\n"
    "buffer_load_dwordx4 v[84:87], v15, s[24:27], s73 offen sc1\n"
    "ds_read_b64_tr_b16 v[224:225], v8 offset:2048\n"
    "ds_read_b64_tr_b16 v[226:227], v8 offset:2560\n"
    "ds_read_b64_tr_b16 v[228:229], v9 offset:2048\n"
    "ds_read_b64_tr_b16 v[230:231], v9 offset:2560\n"
    "s_sleep 1\n"
    "s_waitcnt lgkmcnt(8)\n"
    "v_mfma_f32_32x32x16_bf16 v[32:47], v[212:215], v[160:163], v[32:47]\n"
    "v_mfma_f32_32x32x16_bf16 v[48:63], v[212:215], v[164:167], v[48:63]\n"
    "s_add_u32 m0, s67, 0\n"
    "s_add_u32 s74, s61, 86016\n"
    "buffer_load_dwordx4 v12, s[36:39], s74 offen lds\n"
    "s_add_u32 s74, s61, 92160\n"
    "buffer_load_dwordx4 v12, s[36:39], s74 offen offset:1024 lds\n"
    "s_sleep 1\n"
    "s_waitcnt vmcnt(12)\n"
    "s_waitcnt lgkmcnt(6)\n"
    "v_mfma_f32_32x32x16_bf16 v[176:191], v[216:219], v[168:171], v[176:191]\n"
    "v_mfma_f32_32x32x16_bf16 v[192:207], v[216:219], v[172:175], v[192:207]\n"
    "buffer_load_dwordx4 v[88:91], v15, s[24:27], s72 offen offset:1024 sc1\n"
    "buffer_load_dwordx4 v[92:95], v15, s[24:27], s73 offen offset:1024 sc1\n"
    "ds_read_b64_tr_b16 v[232:233], v8 offset:4096\n"
    "ds_read_b64_tr_b16 v[234:235], v8 offset:4608\n"
    "ds_read_b64_tr_b16 v[236:237], v9 offset:4096\n"
    "ds_read_b64_tr_b16 v[238:239], v9 offset:4608\n"
    "s_sleep 1\n"
    "s_waitcnt lgkmcnt(8)\n"
    "v_mfma_f32_32x32x16_bf16 v[32:47], v[220:223], v[168:171], v[32:47]\n"
    "v_mfma_f32_32x32x16_bf16 v[48:63], v[220:223], v[172:175], v[48:63]\n"
    "s_add_u32 m0, s67, 2048\n"
    "s_add_u32 s74, s61, 100352\n"
    "buffer_load_dwordx4 v12, s[36:39], s74 offen lds\n"
    "s_add_u32 s74, s61, 106496\n"
    "buffer_load_dwordx4 v12, s[36:39], s74 offen offset:1024 lds\n"
    "s_sleep 1\n"
    "s_waitcnt vmcnt(12)\n"
    "s_waitcnt lgkmcnt(6)\n"
    "v_mfma_f32_32x32x16_bf16 v[176:191], v[224:227], v[64:67], v[176:191]\n"
    "v_mfma_f32_32x32x16_bf16 v[192:207], v[224:227], v[68:71], v[192:207]\n"
    "buffer_load_dwordx4 v[96:99], v15, s[24:27], s72 offen offset:2048 sc1\n"
    "buffer_load_dwordx4 v[100:103], v15, s[24:27], s73 offen offset:2048 sc1\n"
    "ds_read_b64_tr_b16 v[208:209], v8 offset:6144\n"
    "ds_read_b64_tr_b16 v[210:211], v8 offset:6656\n"
    "ds_read_b64_tr_b16 v[212:213], v9 offset:6144\n"
    "ds_read_b64_tr_b16 v[214:215], v9 offset:6656\n"
    "s_sleep 1\n"
    "s_waitcnt lgkmcnt(8)\n"
    "v_mfma_f32_32x32x16_bf16 v[32:47], v[228:231], v[64:67], v[32:47]\n"
    "v_mfma_f32_32x32x16_bf16 v[48:63], v[228:231], v[68:71], v[48:63]\n"
    "s_add_u32 m0, s67, 4096\n"
    "s_add_u32 s74, s61, 114688\n"
    "buffer_load_dwordx4 v12, s[36:39], s74 offen lds\n"
    "s_add_u32 s74, s61, 120832\n"
    "buffer_load_dwordx4 v12, s[36:39], s74 offen offset:1024 lds\n"
    "s_sleep 1\n"
    "s_waitcnt vmcnt(12)\n"
    "s_waitcnt lgkmcnt(6)\n"
    "v_mfma_f32_32x32x16_bf16 v[176:191], v[232:235], v[72:75], v[176:191]\n"
    "v_mfma_f32_32x32x16_bf16 v[192:207], v[232:235], v[76:79], v[192:207]\n"
    "buffer_load_dwordx4 v[104:107], v15, s[24:27], s72 offen offset:3072 sc1\n"
    "buffer_load_dwordx4 v[108:111], v15, s[24:27], s73 offen offset:3072 sc1\n"
    "ds_read_b64_tr_b16 v[216:217], v8 offset:8192\n"
    "ds_read_b64_tr_b16 v[218:219], v8 offset:8704\n"
    "ds_read_b64_tr_b16 v[220:221], v9 offset:8192\n"
    "ds_read_b64_tr_b16 v[222:223], v9 offset:8704\n"
    "s_sleep 1\n"
    "s_waitcnt lgkmcnt(8)\n"
    "v_mfma_f32_32x32x16_bf16 v[32:47], v[236:239], v[72:75], v[32:47]\n"
    "v_mfma_f32_32x32x16_bf16 v[48:63], v[236:239], v[76:79], v[48:63]\n"
    "s_add_u32 m0, s67, 6144\n"
    "s_add_u32 s74, s61, 129024\n"
    "buffer_load_dwordx4 v12, s[36:39], s74 offen lds\n"
    "s_add_u32 s74, s61, 135168\n"
    "buffer_load_dwordx4 v12, s[36:39], s74 offen offset:1024 lds\n"
    "s_sleep 1\n"
    "s_waitcnt vmcnt(12)\n"
    "s_waitcnt lgkmcnt(6)\n"
    "v_mfma_f32_32x32x16_bf16 v[176:191], v[208:211], v[80:83], v[176:191]\n"
    "v_mfma_f32_32x32x16_bf16 v[192:207], v[208:211], v[84:87], v[192:207]\n"
    "s_add_u32 s72, s17, 8192\n"
    "s_add_u32 s73, s72, s64\n"
    "buffer_load_dwordx4 v[112:115], v15, s[24:27], s72 offen sc1\n"
    "buffer_load_dwordx4 v[116:119], v15, s[24:27], s73 offen sc1\n"
    "ds_read_b64_tr_b16 v[224:225], v10 offset:0\n"
    "ds_read_b64_tr_b16 v[226:227], v10 offset:512\n"
    "ds_read_b64_tr_b16 v[228:229], v11 offset:0\n"
    "ds_read_b64_tr_b16 v[230:231], v11 offset:512\n"
    "s_sleep 1\n"
    "s_waitcnt lgkmcnt(8)\n"
    "v_mfma_f32_32x32x16_bf16 v[32:47], v[212:215], v[80:83], v[32:47]\n"
    "v_mfma_f32_32x32x16_bf16 v[48:63], v[212:215], v[84:87], v[48:63]\n"
    "s_add_u32 m0, s67, 8192\n"
    "s_add_u32 s74, s61, 143360\n"
    "buffer_load_dwordx4 v12, s[36:39], s74 offen lds\n"
    "s_add_u32 s74, s61, 149504\n"
    "buffer_load_dwordx4 v12, s[36:39], s74 offen offset:1024 lds\n"
    "s_add_u32 m0, s66, 4096\n"
    "s_add_u32 s74, s19, 0\n"
    "buffer_load_dwordx4 v26, s[40:43], s74 offen lds\n"
    "s_add_u32 s74, s19, 2560\n"
    "buffer_load_dwordx4 v26, s[40:43], s74 offen offset:1024 lds\n"
    "s_sleep 1\n"
    "s_waitcnt vmcnt(14)\n"
    "s_waitcnt lgkmcnt(6)\n"
    "v_mfma_f32_32x32x16_bf16 v[176:191], v[216:219], v[88:91], v[176:191]\n"
    "v_mfma_f32_32x32x16_bf16 v[192:207], v[216:219], v[92:95], v[192:207]\n"
    "buffer_load_dwordx4 v[120:123], v15, s[24:27], s72 offen offset:1024 sc1\n"
    "buffer_load_dwordx4 v[124:127], v15, s[24:27], s73 offen offset:1024 sc1\n"
    "ds_read_b64_tr_b16 v[232:233], v10 offset:2048\n"
    "ds_read_b64_tr_b16 v[234:235], v10 offset:2560\n"
    "ds_read_b64_tr_b16 v[236:237], v11 offset:2048\n"
    "ds_read_b64_tr_b16 v[238:239], v11 offset:2560\n"
    "s_sleep 1\n"
    "s_waitcnt lgkmcnt(8)\n"
    "v_mfma_f32_32x32x16_bf16 v[32:47], v[220:223], v[88:91], v[32:47]\n"
    "v_mfma_f32_32x32x16_bf16 v[48:63], v[220:223], v[92:95], v[48:63]\n"
    "s_add_u32 m0, s67, 10240\n"
    "s_add_u32 s74, s61, 157696\n"
    "buffer_load_dwordx4 v12, s[36:39], s74 offen lds\n"
    "s_add_u32 s74, s61, 163840\n"
    "buffer_load_dwordx4 v12, s[36:39], s74 offen offset:1024 lds\n"
    "s_add_u32 m0, s66, 6144\n"
    "s_add_u32 s74, s19, 14336\n"
    "buffer_load_dwordx4 v26, s[40:43], s74 offen lds\n"
    "s_add_u32 s74, s19, 16896\n"
    "buffer_load_dwordx4 v29, s[40:43], s74 offen offset:1024 lds\n"
    "s_sleep 1\n"
    "s_waitcnt vmcnt(16)\n"
    "s_waitcnt lgkmcnt(6)\n"
    "v_mfma_f32_32x32x16_bf16 v[176:191], v[224:227], v[96:99], v[176:191]\n"
    "v_mfma_f32_32x32x16_bf16 v[192:207], v[224:227], v[100:103], v[192:207]\n"
    "buffer_load_dwordx4 v[128:131], v15, s[24:27], s72 offen offset:2048 sc1\n"
    "buffer_load_dwordx4 v[132:135], v15, s[24:27], s73 offen offset:2048 sc1\n"
    "ds_read_b64_tr_b16 v[208:209], v10 offset:4096\n"
    "ds_read_b64_tr_b16 v[210:211], v10 offset:4608\n"
    "ds_read_b64_tr_b16 v[212:213], v11 offset:4096\n"
    "ds_read_b64_tr_b16 v[214:215], v11 offset:4608\n"
    "s_sleep 1\n"
    "s_waitcnt lgkmcnt(8)\n"
    "v_mfma_f32_32x32x16_bf16 v[32:47], v[228:231], v[96:99], v[32:47]\n"
    "v_mfma_f32_32x32x16_bf16 v[48:63], v[228:231], v[100:103], v[48:63]\n"
    "s_add_u32 m0, s67, 12288\n"
    "s_add_u32 s74, s61, 172032\n"
    "buffer_load_dwordx4 v12, s[36:39], s74 offen lds\n"
    "s_add_u32 s74, s61, 178176\n"
    "buffer_load_dwordx4 v12, s[36:39], s74 offen offset:1024 lds\n"
    "s_add_u32 m0, s66, 8192\n"
    "s_add_u32 s74, s19, 25088\n"
    "buffer_load_dwordx4 v26, s[40:43], s74 offen lds\n"
    "s_add_u32 s74, s19, 27648\n"
    "buffer_load_dwordx4 v26, s[40:43], s74 offen offset:1024 lds\n"
    "s_sleep 1\n"
    "s_waitcnt vmcnt(18)\n"
    "s_waitcnt lgkmcnt(6)\n"
    "v_mfma_f32_32x32x16_bf16 v[176:191], v[232:235], v[104:107], v[176:191]\n"
    "v_mfma_f32_32x32x16_bf16 v[192:207], v[232:235], v[108:111], v[192:207]\n"
    "buffer_load_dwordx4 v[136:139], v15, s[24:27], s72 offen offset:3072 sc1\n"
    "buffer_load_dwordx4 v[140:143], v15, s[24:27], s73 offen offset:3072 sc1\n"
    "ds_read_b64_tr_b16 v[216:217], v10 offset:6144\n"
    "ds_read_b64_tr_b16 v[218:219], v10 offset:6656\n"
    "ds_read_b64_tr_b16 v[220:221], v11 offset:6144\n"
    "ds_read_b64_tr_b16 v[222:223], v11 offset:6656\n"
    "s_sleep 1\n"
    "s_waitcnt lgkmcnt(8)\n"
    "v_mfma_f32_32x32x16_bf16 v[32:47], v[236:239], v[104:107], v[32:47]\n"
    "v_mfma_f32_32x32x16_bf16 v[48:63], v[236:239], v[108:111], v[48:63]\n"
    "s_add_u32 m0, s67, 14336\n"
    "s_add_u32 s74, s61, 186368\n"
    "buffer_load_dwordx4 v12, s[36:39], s74 offen lds\n"
    "s_add_u32 s74, s61, 192512\n"
    "buffer_load_dwordx4 v12, s[36:39], s74 offen offset:1024 lds\n"
    "s_add_u32 m0, s67, 0\n"
    "s_add_u32 s74, s19, 39424\n"
    "buffer_load_dwordx4 v26, s[40:43], s74 offen lds\n"
    "s_add_u32 s74, s19, 49152\n"
    "buffer_load_dwordx4 v26, s[40:43], s74 offen offset:1024 lds\n"
    "s_sleep 1\n"
    "s_waitcnt vmcnt(20)\n"
    "s_waitcnt lgkmcnt(6)\n"
    "v_mfma_f32_32x32x16_bf16 v[176:191], v[208:211], v[112:115], v[176:191]\n"
    "v_mfma_f32_32x32x16_bf16 v[192:207], v[208:211], v[116:119], v[192:207]\n"
    "s_add_u32 s72, s17, 12288\n"
    "s_add_u32 s73, s72, s64\n"
    "buffer_load_dwordx4 v[144:147], v15, s[24:27], s72 offen sc1\n"
    "buffer_load_dwordx4 v[148:151], v15, s[24:27], s73 offen sc1\n"
    "ds_read_b64_tr_b16 v[224:225], v10 offset:8192\n"
    "ds_read_b64_tr_b16 v[226:227], v10 offset:8704\n"
    "ds_read_b64_tr_b16 v[228:229], v11 offset:8192\n"
    "ds_read_b64_tr_b16 v[230:231], v11 offset:8704\n"
    "s_sleep 1\n"
    "s_waitcnt lgkmcnt(8)\n"
    "v_mfma_f32_32x32x16_bf16 v[32:47], v[212:215], v[112:115], v[32:47]\n"
    "v_mfma_f32_32x32x16_bf16 v[48:63], v[212:215], v[116:119], v[48:63]\n"
    "s_add_u32 m0, s66, 0\n"
    "s_add_u32 s74, s61, 200704\n"
    "buffer_load_dwordx4 v12, s[36:39], s74 offen lds\n"
    "s_add_u32 s74, s61, 206848\n"
    "buffer_load_dwordx4 v12, s[36:39], s74 offen offset:1024 lds\n"
    "s_add_u32 m0, s67, 2048\n"
    "s_add_u32 s74, s19, 53760\n"
    "buffer_load_dwordx4 v26, s[40:43], s74 offen lds\n"
    "s_add_u32 s74, s19, 63488\n"
    "buffer_load_dwordx4 v26, s[40:43], s74 offen offset:1024 lds\n"
    "s_sleep 1\n"
    "s_waitcnt vmcnt(20)\n"
    "s_waitcnt lgkmcnt(6)\n"
    "v_mfma_f32_32x32x16_bf16 v[176:191], v[216:219], v[120:123], v[176:191]\n"
    "v_mfma_f32_32x32x16_bf16 v[192:207], v[216:219], v[124:127], v[192:207]\n"
    "buffer_load_dwordx4 v[152:155], v15, s[24:27], s72 offen offset:1024 sc1\n"
    "buffer_load_dwordx4 v[156:159], v15, s[24:27], s73 offen offset:1024 sc1\n"
    "ds_read_b64_tr_b16 v[232:233], v10 offset:10240\n"
    "ds_read_b64_tr_b16 v[234:235], v10 offset:10752\n"
    "ds_read_b64_tr_b16 v[236:237], v11 offset:10240\n"
    "ds_read_b64_tr_b16 v[238:239], v11 offset:10752\n"
    "s_sleep 1\n"
    "s_waitcnt lgkmcnt(8)\n"
    "v_mfma_f32_32x32x16_bf16 v[32:47], v[220:223], v[120:123], v[32:47]\n"
    "v_mfma_f32_32x32x16_bf16 v[48:63], v[220:223], v[124:127], v[48:63]\n"
    "s_add_u32 m0, s66, 2048\n"
    "s_add_u32 s74, s61, 215040\n"
    "buffer_load_dwordx4 v12, s[36:39], s74 offen lds\n"
    "s_add_u32 s74, s61, 221184\n"
    "buffer_load_dwordx4 v12, s[36:39], s74 offen offset:1024 lds\n"
    "s_add_u32 m0, s67, 4096\n"
    "s_add_u32 s74, s19, 68096\n"
    "buffer_load_dwordx4 v29, s[40:43], s74 offen lds\n"
    "s_add_u32 s74, s19, 74240\n"
    "buffer_load_dwordx4 v26, s[40:43], s74 offen offset:1024 lds\n"
    "s_sleep 1\n"
    "s_waitcnt vmcnt(20)\n"
    "s_waitcnt lgkmcnt(6)\n"
    "v_mfma_f32_32x32x16_bf16 v[176:191], v[224:227], v[128:131], v[176:191]\n"
    "v_mfma_f32_32x32x16_bf16 v[192:207], v[224:227], v[132:135], v[192:207]\n"
    "buffer_load_dwordx4 v[160:163], v15, s[24:27], s72 offen offset:2048 sc1\n"
    "buffer_load_dwordx4 v[164:167], v15, s[24:27], s73 offen offset:2048 sc1\n"
    "buffer_load_dwordx4 v[0:3], v31, s[28:31], s65 offen\n"
    "buffer_load_dwordx4 v[4:7], v31, s[28:31], s65 offen offset:32\n"
    "ds_read_b64_tr_b16 v[208:209], v10 offset:12288\n"
    "ds_read_b64_tr_b16 v[210:211], v10 offset:12800\n"
    "ds_read_b64_tr_b16 v[212:213], v11 offset:12288\n"
    "ds_read_b64_tr_b16 v[214:215], v11 offset:12800\n"
    "s_sleep 1\n"
    "s_waitcnt lgkmcnt(8)\n"
    "v_mfma_f32_32x32x16_bf16 v[32:47], v[228:231], v[128:131], v[32:47]\n"
    "v_mfma_f32_32x32x16_bf16 v[48:63], v[228:231], v[132:135], v[48:63]\n"
    "s_add_u32 m0, s67, 6144\n"
    "s_add_u32 s74, s19, 78848\n"
    "buffer_load_dwordx4 v26, s[40:43], s74 offen lds\n"
    "s_add_u32 s74, s19, 88576\n"
    "buffer_load_dwordx4 v26, s[40:43], s74 offen offset:1024 lds\n"
    "s_sleep 1\n"
    "s_waitcnt vmcnt(20)\n"
    "s_waitcnt lgkmcnt(6)\n"
    "v_mfma_f32_32x32x16_bf16 v[176:191], v[232:235], v[136:139], v[176:191]\n"
    "v_mfma_f32_32x32x16_bf16 v[192:207], v[232:235], v[140:143], v[192:207]\n"
    "buffer_load_dwordx4 v[168:171], v15, s[24:27], s72 offen offset:3072 sc1\n"
    "buffer_load_dwordx4 v[172:175], v15, s[24:27], s73 offen offset:3072 sc1\n"
    "ds_read_b64_tr_b16 v[216:217], v10 offset:14336\n"
    "ds_read_b64_tr_b16 v[218:219], v10 offset:14848\n"
    "ds_read_b64_tr_b16 v[220:221], v11 offset:14336\n"
    "ds_read_b64_tr_b16 v[222:223], v11 offset:14848\n"
    "s_sleep 1\n"
    "s_waitcnt lgkmcnt(8)\n"
    "v_mfma_f32_32x32x16_bf16 v[32:47], v[236:239], v[136:139], v[32:47]\n"
    "v_mfma_f32_32x32x16_bf16 v[48:63], v[236:239], v[140:143], v[48:63]\n"
    "s_sleep 1\n"
    "s_waitcnt vmcnt(16)\n"
    "s_waitcnt lgkmcnt(6)\n"
    "v_mfma_f32_32x32x16_bf16 v[176:191], v[208:211], v[144:147], v[176:191]\n"
    "v_mfma_f32_32x32x16_bf16 v[192:207], v[208:211], v[148:151], v[192:207]\n"
    "ds_read_b64_tr_b16 v[224:225], v8 offset:0\n"
    "ds_read_b64_tr_b16 v[226:227], v8 offset:512\n"
    "ds_read_b64_tr_b16 v[228:229], v9 offset:0\n"
    "ds_read_b64_tr_b16 v[230:231], v9 offset:512\n"
    "s_sleep 1\n"
    "s_waitcnt lgkmcnt(8)\n"
    "v_mfma_f32_32x32x16_bf16 v[32:47], v[212:215], v[144:147], v[32:47]\n"
    "v_mfma_f32_32x32x16_bf16 v[48:63], v[212:215], v[148:151], v[48:63]\n"
    "s_sleep 1\n"
    "s_waitcnt vmcnt(10)\n"
    "s_waitcnt lgkmcnt(6)\n"
    "v_mfma_f32_32x32x16_bf16 v[176:191], v[216:219], v[152:155], v[176:191]\n"
    "v_mfma_f32_32x32x16_bf16 v[192:207], v[216:219], v[156:159], v[192:207]\n"
    "ds_read_b64_tr_b16 v[232:233], v8 offset:2048\n"
    "ds_read_b64_tr_b16 v[234:235], v8 offset:2560\n"
    "ds_read_b64_tr_b16 v[236:237], v9 offset:2048\n"
    "ds_read_b64_tr_b16 v[238:239], v9 offset:2560\n"
    "s_sleep 1\n"
    "s_waitcnt lgkmcnt(8)\n"
    "v_mfma_f32_32x32x16_bf16 v[32:47], v[220:223], v[152:155], v[32:47]\n"
    "v_mfma_f32_32x32x16_bf16 v[48:63], v[220:223], v[156:159], v[48:63]\n"
    "s_sleep 1\n"
    "s_waitcnt vmcnt(7) lgkmcnt(6)\n"
    "v_mfma_f32_32x32x16_bf16 v[176:191], v[224:227], v[160:163], v[176:191]\n"
    "s_waitcnt vmcnt(6)\n"
    "v_mfma_f32_32x32x16_bf16 v[192:207], v[224:227], v[164:167], v[192:207]\n"
    "s_setprio 3\n"
    "v_add_u32 v16, s67, v15\n"
    "v_add_u32 v17, s66, v15\n"
    "v_mov_b32 v18, 122408\n"
    "v_mov_b32 v19, 1\n"
    "s_bfe_u32 s72, s3, 0x20006\n"
    "s_xor_b32 s73, s72, 1\n"
    "s_lshl_b32 s73, s73, 14\n"
    "v_add_u32 v20, s73, v15\n"
    "s_xor_b32 s73, s72, 2\n"
    "s_lshl_b32 s73, s73, 14\n"
    "v_add_u32 v21, s73, v15\n"
    "s_xor_b32 s73, s72, 3\n"
    "s_mul_i32 s73, s73, 10240\n"
    "s_add_u32 s73, s73, 122880\n"
    "v_add_u32 v22, s73, v15\n"
    "s_and_b32 s73, s72, 2\n"
    "s_mul_i32 s74, s73, 10240\n"
    "s_add_u32 s74, s74, 122880\n"
    "s_lshl_b32 s73, s73, 14\n"
    "s_add_u32 s73, s73, 49152\n"
    "v_add_u32 v8, s74, v13\n"
    "v_add_u32 v9, s74, v14\n"
    "v_add_u32 v10, s73, v13\n"
    "v_add_u32 v11, s73, v14\n"
    "s_and_b32 s73, s72, 1\n"
    "s_lshl2_add_u32 s73, s73, 122416\n"
    "v_mov_b32 v23, s73\n"
    "s_setprio 0\n"
    "s_sleep 1\n"
    "s_waitcnt lgkmcnt(4)\n"
    "v_mfma_f32_32x32x16_bf16 v[32:47], v[228:231], v[160:163], v[32:47]\n"
    "v_mfma_f32_32x32x16_bf16 v[48:63], v[228:231], v[164:167], v[48:63]\n"
    "s_sleep 1\n"
    "s_waitcnt vmcnt(2)\n"
    "s_setprio 3\n"
    "v_mov_b32 v24, 122396\n"
    "v_mov_b32 v25, 1\n"
    "s_mov_b64 exec, 1\n"
    "ds_add_u32 v24, v25\n"
    "s_mov_b64 exec, -1\n"
    "s_setprio 0\n"
    "s_waitcnt vmcnt(1) lgkmcnt(3)\n"
    "v_mfma_f32_32x32x16_bf16 v[176:191], v[232:235], v[168:171], v[176:191]\n"
    "s_waitcnt vmcnt(0)\n"
    "v_mfma_f32_32x32x16_bf16 v[192:207], v[232:235], v[172:175], v[192:207]\n"
    "s_sleep 1\n"
    "s_waitcnt lgkmcnt(1)\n"
    "v_mfma_f32_32x32x16_bf16 v[32:47], v[236:239], v[168:171], v[32:47]\n"
    "v_mfma_f32_32x32x16_bf16 v[48:63], v[236:239], v[172:175], v[48:63]\n"
    "s_sleep 1\n"
    "s_setprio 0\n"
    "v_mov_b32 v27, 122396\n"
    "ds_read_b32 v26, v27\n"
    "s_nop 3\n"
    "ds_write_b128 v16, v[192:195] offset:8192\n"
    "v_lshlrev_b32 v24, 16, v0\n"
    "v_and_b32 v25, s77, v0\n"
    "v_add_f32 v176, v176, v24\n"
    "ds_write_b128 v16, v[196:199] offset:9216\n"
    "v_add_f32 v177, v177, v25\n"
    "v_lshlrev_b32 v24, 16, v1\n"
    "v_and_b32 v25, s77, v1\n"
    "ds_write_b128 v16, v[200:203] offset:10240\n"
    "v_add_f32 v178, v178, v24\n"
    "v_add_f32 v179, v179, v25\n"
    "ds_write_b128 v16, v[204:207] offset:11264\n"
    "v_lshlrev_b32 v24, 16, v2\n"
    "v_and_b32 v25, s77, v2\n"
    "v_add_f32 v180, v180, v24\n"
    "ds_write_b128 v16, v[32:35] offset:12288\n"
    "v_add_f32 v181, v181, v25\n"
    "v_lshlrev_b32 v24, 16, v3\n"
    "v_and_b32 v25, s77, v3\n"
    "ds_write_b128 v16, v[36:39] offset:13312\n"
    "v_add_f32 v182, v182, v24\n"
    "v_add_f32 v183, v183, v25\n"
    "ds_write_b128 v16, v[40:43] offset:14336\n"
    "v_lshlrev_b32 v24, 16, v4\n"
    "v_and_b32 v25, s77, v4\n"
    "v_add_f32 v184, v184, v24\n"
    "ds_write_b128 v16, v[44:47] offset:15360\n"
    "v_add_f32 v185, v185, v25\n"
    "v_lshlrev_b32 v24, 16, v5\n"
    "v_and_b32 v25, s77, v5\n"
    "ds_write_b128 v17, v[48:51] offset:0\n"
    "v_add_f32 v186, v186, v24\n"
    "v_add_f32 v187, v187, v25\n"
    "ds_write_b128 v17, v[52:55] offset:1024\n"
    "v_lshlrev_b32 v24, 16, v6\n"
    "v_and_b32 v25, s77, v6\n"
    "v_add_f32 v188, v188, v24\n"
    "ds_write_b128 v17, v[56:59] offset:2048\n"
    "v_add_f32 v189, v189, v25\n"
    "v_lshlrev_b32 v24, 16, v7\n"
    "v_and_b32 v25, s77, v7\n"
    "ds_write_b128 v17, v[60:63] offset:3072\n"
    "v_add_f32 v190, v190, v24\n"
    "v_add_f32 v191, v191, v25\n"
    "s_add_u32 s72, s63, 4096\n"
    "s_add_u32 s73, s63, 8192\n"
    "s_add_u32 s74, s63, 12288\n"
    "s_mov_b64 exec, 1\n"
    "ds_add_u32 v18, v19\n"
    "s_mov_b64 exec, -1\n"
    "s_waitcnt lgkmcnt(13)\n"
    "v_readfirstlane_b32 s75, v26\n"
    "s_nop 0\n"
    "s_cmp_ge_u32 s75, 4\n"
    "s_cbranch_scc1 cok33_%=\n"
    "s_mov_b32 s79, 0x100000\n"
    "csp34_%=:\n"
    "ds_read_b32 v26, v27\n"
    "s_sub_u32 s79, s79, 1\n"
    "s_cbranch_scc1 cok33_%=\n"
    "s_sleep 1\n"
    "s_waitcnt lgkmcnt(0)\n"
    "v_readfirstlane_b32 s75, v26\n"
    "s_nop 0\n"
    "s_cmp_ge_u32 s75, 4\n"
    "s_cbranch_scc0 csp34_%=\n"
    "cok33_%=:\n"
    "ds_read_b64_tr_b16 v[64:65], v8 offset:4096\n"
    "ds_read_b64_tr_b16 v[66:67], v8 offset:4608\n"
    "ds_read_b64_tr_b16 v[68:69], v9 offset:4096\n"
    "ds_read_b64_tr_b16 v[70:71], v9 offset:4608\n"
    "ds_read_b64_tr_b16 v[72:73], v8 offset:6144\n"
    "ds_read_b64_tr_b16 v[74:75], v8 offset:6656\n"
    "ds_read_b64_tr_b16 v[76:77], v9 offset:6144\n"
    "ds_read_b64_tr_b16 v[78:79], v9 offset:6656\n"
    "ds_read_b64_tr_b16 v[80:81], v8 offset:8192\n"
    "ds_read_b64_tr_b16 v[82:83], v8 offset:8704\n"
    "ds_read_b64_tr_b16 v[84:85], v9 offset:8192\n"
    "ds_read_b64_tr_b16 v[86:87], v9 offset:8704\n"
    "ds_read_b64_tr_b16 v[88:89], v10 offset:0\n"
    "ds_read_b64_tr_b16 v[90:91], v10 offset:512\n"
    "ds_read_b64_tr_b16 v[92:93], v11 offset:0\n"
    "ds_read_b64_tr_b16 v[94:95], v11 offset:512\n"
    "ds_read_b64_tr_b16 v[96:97], v10 offset:2048\n"
    "ds_read_b64_tr_b16 v[98:99], v10 offset:2560\n"
    "ds_read_b64_tr_b16 v[100:101], v11 offset:2048\n"
    "ds_read_b64_tr_b16 v[102:103], v11 offset:2560\n"
    "ds_read_b64_tr_b16 v[104:105], v10 offset:4096\n"
    "ds_read_b64_tr_b16 v[106:107], v10 offset:4608\n"
    "ds_read_b64_tr_b16 v[108:109], v11 offset:4096\n"
    "ds_read_b64_tr_b16 v[110:111], v11 offset:4608\n"
    "ds_read_b64_tr_b16 v[112:113], v10 offset:6144\n"
    "ds_read_b64_tr_b16 v[114:115], v10 offset:6656\n"
    "ds_read_b64_tr_b16 v[116:117], v11 offset:6144\n"
    "ds_read_b64_tr_b16 v[118:119], v11 offset:6656\n"
    "ds_read_b64_tr_b16 v[120:121], v8 offset:14336\n"
    "ds_read_b64_tr_b16 v[122:123], v8 offset:14848\n"
    "ds_read_b64_tr_b16 v[124:125], v9 offset:14336\n"
    "ds_read_b64_tr_b16 v[126:127], v9 offset:14848\n"
    "ds_read_b64_tr_b16 v[128:129], v8 offset:16384\n"
    "ds_read_b64_tr_b16 v[130:131], v8 offset:16896\n"
    "ds_read_b64_tr_b16 v[132:133], v9 offset:16384\n"
    "ds_read_b64_tr_b16 v[134:135], v9 offset:16896\n"
    "ds_read_b64_tr_b16 v[136:137], v8 offset:18432\n"
    "ds_read_b64_tr_b16 v[138:139], v8 offset:18944\n"
    "ds_read_b64_tr_b16 v[140:141], v9 offset:18432\n"
    "ds_read_b64_tr_b16 v[142:143], v9 offset:18944\n"
    "ds_read_b64_tr_b16 v[144:145], v10 offset:16384\n"
    "ds_read_b64_tr_b16 v[146:147], v10 offset:16896\n"
    "ds_read_b64_tr_b16 v[148:149], v11 offset:16384\n"
    "ds_read_b64_tr_b16 v[150:151], v11 offset:16896\n"
    "ds_read_b64_tr_b16 v[152:153], v10 offset:18432\n"
    "ds_read_b64_tr_b16 v[154:155], v10 offset:18944\n"
    "ds_read_b64_tr_b16 v[156:157], v11 offset:18432\n"
    "ds_read_b64_tr_b16 v[158:159], v11 offset:18944\n"
    "ds_read_b64_tr_b16 v[160:161], v10 offset:20480\n"
    "ds_read_b64_tr_b16 v[162:163], v10 offset:20992\n"
    "ds_read_b64_tr_b16 v[164:165], v11 offset:20480\n"
    "ds_read_b64_tr_b16 v[166:167], v11 offset:20992\n"
    "ds_read_b64_tr_b16 v[168:169], v10 offset:22528\n"
    "ds_read_b64_tr_b16 v[170:171], v10 offset:23040\n"
    "ds_read_b64_tr_b16 v[172:173], v11 offset:22528\n"
    "ds_read_b64_tr_b16 v[174:175], v11 offset:23040\n"
    "v_mov_b32 v26, 122404\n"
    "s_mov_b32 s79, 0x100000\n"
    "sp35_%=:\n"
    "ds_read_b32 v25, v26\n"
    "s_sub_u32 s79, s79, 1\n"
    "s_cbranch_scc1 so36_%=\n"
    "s_waitcnt lgkmcnt(0)\n"
    "v_readfirstlane_b32 s75, v25\n"
    "s_nop 0\n"
    "s_sleep 2\n"
    "s_cmp_ge_u32 s75, 4\n"
    "s_cbranch_scc0 sp35_%=\n"
    "so36_%=:\n"
    "buffer_load_dwordx4 v[208:211], v15, s[24:27], s63 offen sc1\n"
    "buffer_load_dwordx4 v[212:215], v15, s[24:27], s63 offen offset:1024 sc1\n"
    "buffer_load_dwordx4 v[216:219], v15, s[24:27], s63 offen offset:2048 sc1\n"
    "buffer_load_dwordx4 v[220:223], v15, s[24:27], s63 offen offset:3072 sc1\n"
    "buffer_load_dwordx4 v[224:227], v15, s[24:27], s72 offen sc1\n"
    "buffer_load_dwordx4 v[228:231], v15, s[24:27], s72 offen offset:1024 sc1\n"
    "buffer_load_dwordx4 v[232:235], v15, s[24:27], s72 offen offset:2048 sc1\n"
    "buffer_load_dwordx4 v[236:239], v15, s[24:27], s72 offen offset:3072 sc1\n"
    "buffer_load_dwordx4 v[240:243], v15, s[24:27], s73 offen sc1\n"
    "buffer_load_dwordx4 v[244:247], v15, s[24:27], s73 offen offset:1024 sc1\n"
    "buffer_load_dwordx4 v[248:251], v15, s[24:27], s73 offen offset:2048 sc1\n"
    "buffer_load_dwordx4 v[252:255], v15, s[24:27], s73 offen offset:3072 sc1\n"
    "buffer_load_dwordx4 v[0:3], v15, s[24:27], s74 offen sc1\n"
    "buffer_load_dwordx4 v[4:7], v15, s[24:27], s74 offen offset:1024 sc1\n"
    "s_mov_b32 s79, 0x100000\n"
    "sp37_%=:\n"
    "ds_read_b32 v25, v18\n"
    "s_sub_u32 s79, s79, 1\n"
    "s_cbranch_scc1 so38_%=\n"
    "s_waitcnt lgkmcnt(0)\n"
    "v_readfirstlane_b32 s75, v25\n"
    "s_nop 0\n"
    "s_cmp_ge_u32 s75, 4\n"
    "s_cbranch_scc0 sp37_%=\n"
    "so38_%=:\n"
    "ds_read_b128 v[192:195], v20 offset:57344\n"
    "ds_read_b128 v[196:199], v20 offset:58368\n"
    "ds_read_b128 v[200:203], v20 offset:59392\n"
    "ds_read_b128 v[204:207], v20 offset:60416\n"
    "ds_read_b128 v[32:35], v21 offset:61440\n"
    "ds_read_b128 v[36:39], v21 offset:62464\n"
    "ds_read_b128 v[40:43], v21 offset:63488\n"
    "ds_read_b128 v[44:47], v21 offset:64512\n"
    "ds_read_b128 v[48:51], v22 offset:0\n"
    "ds_read_b128 v[52:55], v22 offset:1024\n"
    "ds_read_b128 v[56:59], v22 offset:2048\n"
    "ds_read_b128 v[60:63], v22 offset:3072\n"
    "s_waitcnt lgkmcnt(11)\n"
    "v_add_f32 v176, v176, v192\n"
    "v_add_f32 v177, v177, v193\n"
    "v_add_f32 v178, v178, v194\n"
    "v_add_f32 v179, v179, v195\n"
    "s_waitcnt lgkmcnt(10)\n"
    "v_add_f32 v180, v180, v196\n"
    "v_add_f32 v181, v181, v197\n"
    "v_add_f32 v182, v182, v198\n"
    "v_add_f32 v183, v183, v199\n"
    "s_waitcnt lgkmcnt(9)\n"
    "v_add_f32 v184, v184, v200\n"
    "v_add_f32 v185, v185, v201\n"
    "v_add_f32 v186, v186, v202\n"
    "v_add_f32 v187, v187, v203\n"
    "s_waitcnt lgkmcnt(8)\n"
    "v_add_f32 v188, v188, v204\n"
    "v_add_f32 v189, v189, v205\n"
    "v_add_f32 v190, v190, v206\n"
    "v_add_f32 v191, v191, v207\n"
    "s_waitcnt lgkmcnt(3)\n"
    "v_add_f32 v32, v32, v48\n"
    "v_add_f32 v33, v33, v49\n"
    "v_add_f32 v34, v34, v50\n"
    "v_add_f32 v35, v35, v51\n"
    "s_waitcnt lgkmcnt(2)\n"
    "v_add_f32 v36, v36, v52\n"
    "v_add_f32 v37, v37, v53\n"
    "v_add_f32 v38, v38, v54\n"
    "v_add_f32 v39, v39, v55\n"
    "s_waitcnt lgkmcnt(1)\n"
    "v_add_f32 v40, v40, v56\n"
    "v_add_f32 v41, v41, v57\n"
    "v_add_f32 v42, v42, v58\n"
    "v_add_f32 v43, v43, v59\n"
    "s_waitcnt lgkmcnt(0)\n"
    "v_add_f32 v44, v44, v60\n"
    "v_add_f32 v45, v45, v61\n"
    "v_add_f32 v46, v46, v62\n"
    "v_add_f32 v47, v47, v63\n"
    "v_add_f32 v176, v176, v32\n"
    "v_add_f32 v177, v177, v33\n"
    "v_add_f32 v178, v178, v34\n"
    "v_add_f32 v179, v179, v35\n"
    "v_add_f32 v180, v180, v36\n"
    "v_add_f32 v181, v181, v37\n"
    "v_add_f32 v182, v182, v38\n"
    "v_add_f32 v183, v183, v39\n"
    "v_add_f32 v184, v184, v40\n"
    "v_add_f32 v185, v185, v41\n"
    "v_add_f32 v186, v186, v42\n"
    "v_add_f32 v187, v187, v43\n"
    "v_add_f32 v188, v188, v44\n"
    "v_add_f32 v189, v189, v45\n"
    "v_add_f32 v190, v190, v46\n"
    "v_add_f32 v191, v191, v47\n"
    "s_setprio 0\n"
    "s_mov_b32 s79, 0x20000\n"
    "s_waitcnt vmcnt(13)\n"
    "v_max3_u32 v25, v208, v209, v210\n"
    "v_max_u32 v25, v25, v211\n"
    "v_cmp_eq_u32 vcc, -1, v25\n"
    "s_cbranch_vccnz or0_%=\n"
    "og0_%=:\n"
    "v_mfma_f32_32x32x16_bf16 v[192:207], v[68:71], v[208:211], 0\n"
    "s_waitcnt vmcnt(12)\n"
    "v_max3_u32 v25, v212, v213, v214\n"
    "v_max_u32 v25, v25, v215\n"
    "v_cmp_eq_u32 vcc, -1, v25\n"
    "v_mfma_f32_32x32x16_bf16 v[176:191], v[64:67], v[208:211], v[176:191]\n"
    "s_cbranch_vccnz or1_%=\n"
    "og1_%=:\n"
    "v_mfma_f32_32x32x16_bf16 v[192:207], v[76:79], v[212:215], v[192:207]\n"
    "s_waitcnt vmcnt(11)\n"
    "v_max3_u32 v25, v216, v217, v218\n"
    "v_max_u32 v25, v25, v219\n"
    "v_cmp_eq_u32 vcc, -1, v25\n"
    "v_mfma_f32_32x32x16_bf16 v[176:191], v[72:75], v[212:215], v[176:191]\n"
    "s_cbranch_vccnz or2_%=\n"
    "og2_%=:\n"
    "v_mfma_f32_32x32x16_bf16 v[192:207], v[84:87], v[216:219], v[192:207]\n"
    "s_waitcnt vmcnt(10)\n"
    "v_max3_u32 v25, v220, v221, v222\n"
    "v_max_u32 v25, v25, v223\n"
    "v_cmp_eq_u32 vcc, -1, v25\n"
    "v_mfma_f32_32x32x16_bf16 v[176:191], v[80:83], v[216:219], v[176:191]\n"
    "s_cbranch_vccnz or3_%=\n"
    "og3_%=:\n"
    "v_mfma_f32_32x32x16_bf16 v[192:207], v[92:95], v[220:223], v[192:207]\n"
    "s_waitcnt vmcnt(9)\n"
    "v_max3_u32 v25, v224, v225, v226\n"
    "v_max_u32 v25, v25, v227\n"
    "v_cmp_eq_u32 vcc, -1, v25\n"
    "v_mfma_f32_32x32x16_bf16 v[176:191], v[88:91], v[220:223], v[176:191]\n"
    "s_cbranch_vccnz or4_%=\n"
    "og4_%=:\n"
    "v_mfma_f32_32x32x16_bf16 v[192:207], v[100:103], v[224:227], v[192:207]\n"
    "s_waitcnt vmcnt(8)\n"
    "v_max3_u32 v25, v228, v229, v230\n"
    "v_max_u32 v25, v25, v231\n"
    "v_cmp_eq_u32 vcc, -1, v25\n"
    "v_mfma_f32_32x32x16_bf16 v[176:191], v[96:99], v[224:227], v[176:191]\n"
    "s_cbranch_vccnz or5_%=\n"
    "og5_%=:\n"
    "v_mfma_f32_32x32x16_bf16 v[192:207], v[108:111], v[228:231], v[192:207]\n"
    "s_waitcnt vmcnt(7)\n"
    "v_max3_u32 v25, v232, v233, v234\n"
    "v_max_u32 v25, v25, v235\n"
    "v_cmp_eq_u32 vcc, -1, v25\n"
    "v_mfma_f32_32x32x16_bf16 v[176:191], v[104:107], v[228:231], v[176:191]\n"
    "s_cbranch_vccnz or6_%=\n"
    "og6_%=:\n"
    "v_mfma_f32_32x32x16_bf16 v[192:207], v[116:119], v[232:235], v[192:207]\n"
    "s_waitcnt vmcnt(6)\n"
    "v_max3_u32 v25, v236, v237, v238\n"
    "v_max_u32 v25, v25, v239\n"
    "v_cmp_eq_u32 vcc, -1, v25\n"
    "v_mfma_f32_32x32x16_bf16 v[176:191], v[112:115], v[232:235], v[176:191]\n"
    "s_cbranch_vccnz or7_%=\n"
    "og7_%=:\n"
    "v_mfma_f32_32x32x16_bf16 v[192:207], v[124:127], v[236:239], v[192:207]\n"
    "s_waitcnt vmcnt(5)\n"
    "v_max3_u32 v25, v240, v241, v242\n"
    "v_max_u32 v25, v25, v243\n"
    "v_cmp_eq_u32 vcc, -1, v25\n"
    "v_mfma_f32_32x32x16_bf16 v[176:191], v[120:123], v[236:239], v[176:191]\n"
    "s_cbranch_vccnz or8_%=\n"
    "og8_%=:\n"
    "v_mfma_f32_32x32x16_bf16 v[192:207], v[132:135], v[240:243], v[192:207]\n"
    "s_waitcnt vmcnt(4)\n"
    "v_max3_u32 v25, v244, v245, v246\n"
    "v_max_u32 v25, v25, v247\n"
    "v_cmp_eq_u32 vcc, -1, v25\n"
    "v_mfma_f32_32x32x16_bf16 v[176:191], v[128:131], v[240:243], v[176:191]\n"
    "s_cbranch_vccnz or9_%=\n"
    "og9_%=:\n"
    "v_mfma_f32_32x32x16_bf16 v[192:207], v[140:143], v[244:247], v[192:207]\n"
    "s_waitcnt vmcnt(3)\n"
    "v_max3_u32 v25, v248, v249, v250\n"
    "v_max_u32 v25, v25, v251\n"
    "v_cmp_eq_u32 vcc, -1, v25\n"
    "v_mfma_f32_32x32x16_bf16 v[176:191], v[136:139], v[244:247], v[176:191]\n"
    "s_cbranch_vccnz or10_%=\n"
    "og10_%=:\n"
    "v_mfma_f32_32x32x16_bf16 v[192:207], v[148:151], v[248:251], v[192:207]\n"
    "s_waitcnt vmcnt(2)\n"
    "v_max3_u32 v25, v252, v253, v254\n"
    "v_max_u32 v25, v25, v255\n"
    "v_cmp_eq_u32 vcc, -1, v25\n"
    "v_mfma_f32_32x32x16_bf16 v[176:191], v[144:147], v[248:251], v[176:191]\n"
    "s_cbranch_vccnz or11_%=\n"
    "og11_%=:\n"
    "v_mfma_f32_32x32x16_bf16 v[192:207], v[156:159], v[252:255], v[192:207]\n"
    "s_waitcnt vmcnt(1)\n"
    "v_max3_u32 v25, v0, v1, v2\n"
    "v_max_u32 v25, v25, v3\n"
    "v_cmp_eq_u32 vcc, -1, v25\n"
    "v_mfma_f32_32x32x16_bf16 v[176:191], v[152:155], v[252:255], v[176:191]\n"
    "s_cbranch_vccnz or12_%=\n"
    "og12_%=:\n"
    "v_mfma_f32_32x32x16_bf16 v[192:207], v[164:167], v[0:3], v[192:207]\n"
    "s_waitcnt vmcnt(0)\n"
    "v_max3_u32 v25, v4, v5, v6\n"
    "v_max_u32 v25, v25, v7\n"
    "v_cmp_eq_u32 vcc, -1, v25\n"
    "v_mfma_f32_32x32x16_bf16 v[176:191], v[160:163], v[0:3], v[176:191]\n"
    "s_cbranch_vccnz or13_%=\n"
    "og13_%=:\n"
    "v_mfma_f32_32x32x16_bf16 v[192:207], v[172:175], v[4:7], v[192:207]\n"
    "v_mfma_f32_32x32x16_bf16 v[176:191], v[168:171], v[4:7], v[176:191]\n"
    "s_nop 10\n"
    "ds_write_b128 v21, v[192:195] offset:61440\n"
    "ds_write_b128 v21, v[196:199] offset:62464\n"
    "ds_write_b128 v21, v[200:203] offset:63488\n"
    "ds_write_b128 v21, v[204:207] offset:64512\n"
    "s_mov_b64 exec, 1\n"
    "ds_add_u32 v23, v19\n"
    "s_mov_b64 exec, -1\n"
    "s_mov_b32 s79, 0x100000\n"
    "sp39_%=:\n"
    "ds_read_b32 v25, v23\n"
    "s_sub_u32 s79, s79, 1\n"
    "s_cbranch_scc1 so40_%=\n"
    "s_waitcnt lgkmcnt(0)\n"
    "v_readfirstlane_b32 s75, v25\n"
    "s_nop 0\n"
    "s_cmp_ge_u32 s75, 2\n"
    "s_cbranch_scc0 sp39_%=\n"
    "so40_%=:\n"
    "ds_read_b128 v[32:35], v16 offset:12288\n"
    "ds_read_b128 v[36:39], v16 offset:13312\n"
    "ds_read_b128 v[40:43], v16 offset:14336\n"
    "ds_read_b128 v[44:47], v16 offset:15360\n"
    "s_waitcnt lgkmcnt(3)\n"
    "v_add_f32 v176, v176, v32\n"
    "v_add_f32 v177, v177, v33\n"
    "v_add_f32 v178, v178, v34\n"
    "v_add_f32 v179, v179, v35\n"
    "s_waitcnt lgkmcnt(2)\n"
    "v_add_f32 v180, v180, v36\n"
    "v_add_f32 v181, v181, v37\n"
    "v_add_f32 v182, v182, v38\n"
    "v_add_f32 v183, v183, v39\n"
    "v_cvt_pk_bf16_f32 v48, v176, v177\n"
    "v_cvt_pk_bf16_f32 v49, v178, v179\n"
    "v_cvt_pk_bf16_f32 v50, v180, v181\n"
    "v_cvt_pk_bf16_f32 v51, v182, v183\n"
    "buffer_store_dwordx4 v[48:51], v31, s[32:35], s65 offen offset:0\n"
    "s_waitcnt lgkmcnt(1)\n"
    "v_add_f32 v184, v184, v40\n"
    "v_add_f32 v185, v185, v41\n"
    "v_add_f32 v186, v186, v42\n"
    "v_add_f32 v187, v187, v43\n"
    "s_waitcnt lgkmcnt(0)\n"
    "v_add_f32 v188, v188, v44\n"
    "v_add_f32 v189, v189, v45\n"
    "v_add_f32 v190, v190, v46\n"
    "v_add_f32 v191, v191, v47\n"
    "v_cvt_pk_bf16_f32 v52, v184, v185\n"
    "v_cvt_pk_bf16_f32 v53, v186, v187\n"
    "v_cvt_pk_bf16_f32 v54, v188, v189\n"
    "v_cvt_pk_bf16_f32 v55, v190, v191\n"
    "buffer_store_dwordx4 v[52:55], v31, s[32:35], s65 offen offset:32\n"
    "s_endpgm\n"
    "or0_%=:\n"
    "s_sub_u32 s79, s79, 1\n"
    "s_max_i32 s79, s79, 0\n"
    "s_cbranch_scc0 og0_%=\n"
    "s_waitcnt vmcnt(0)\n"
    "s_sleep 1\n"
    "s_call_b64 s[80:81], ol0_%=\n"
    "v_max3_u32 v25, v208, v209, v210\n"
    "v_max_u32 v25, v25, v211\n"
    "v_cmp_eq_u32 vcc, -1, v25\n"
    "s_cbranch_vccnz or0_%=\n"
    "s_branch og0_%=\n"
    "or1_%=:\n"
    "s_sub_u32 s79, s79, 1\n"
    "s_max_i32 s79, s79, 0\n"
    "s_cbranch_scc0 og1_%=\n"
    "s_waitcnt vmcnt(0)\n"
    "s_sleep 1\n"
    "s_call_b64 s[80:81], ol1_%=\n"
    "v_max3_u32 v25, v212, v213, v214\n"
    "v_max_u32 v25, v25, v215\n"
    "v_cmp_eq_u32 vcc, -1, v25\n"
    "s_cbranch_vccnz or1_%=\n"
    "s_branch og1_%=\n"
    "or2_%=:\n"
    "s_sub_u32 s79, s79, 1\n"
    "s_max_i32 s79, s79, 0\n"
    "s_cbranch_scc0 og2_%=\n"
    "s_waitcnt vmcnt(0)\n"
    "s_sleep 1\n"
    "s_call_b64 s[80:81], ol2_%=\n"
    "v_max3_u32 v25, v216, v217, v218\n"
    "v_max_u32 v25, v25, v219\n"
    "v_cmp_eq_u32 vcc, -1, v25\n"
    "s_cbranch_vccnz or2_%=\n"
    "s_branch og2_%=\n"
    "or3_%=:\n"
    "s_sub_u32 s79, s79, 1\n"
    "s_max_i32 s79, s79, 0\n"
    "s_cbranch_scc0 og3_%=\n"
    "s_waitcnt vmcnt(0)\n"
    "s_sleep 1\n"
    "s_call_b64 s[80:81], ol3_%=\n"
    "v_max3_u32 v25, v220, v221, v222\n"
    "v_max_u32 v25, v25, v223\n"
    "v_cmp_eq_u32 vcc, -1, v25\n"
    "s_cbranch_vccnz or3_%=\n"
    "s_branch og3_%=\n"
    "or4_%=:\n"
    "s_sub_u32 s79, s79, 1\n"
    "s_max_i32 s79, s79, 0\n"
    "s_cbranch_scc0 og4_%=\n"
    "s_waitcnt vmcnt(0)\n"
    "s_sleep 1\n"
    "s_call_b64 s[80:81], ol4_%=\n"
    "v_max3_u32 v25, v224, v225, v226\n"
    "v_max_u32 v25, v25, v227\n"
    "v_cmp_eq_u32 vcc, -1, v25\n"
    "s_cbranch_vccnz or4_%=\n"
    "s_branch og4_%=\n"
    "or5_%=:\n"
    "s_sub_u32 s79, s79, 1\n"
    "s_max_i32 s79, s79, 0\n"
    "s_cbranch_scc0 og5_%=\n"
    "s_waitcnt vmcnt(0)\n"
    "s_sleep 1\n"
    "s_call_b64 s[80:81], ol5_%=\n"
    "v_max3_u32 v25, v228, v229, v230\n"
    "v_max_u32 v25, v25, v231\n"
    "v_cmp_eq_u32 vcc, -1, v25\n"
    "s_cbranch_vccnz or5_%=\n"
    "s_branch og5_%=\n"
    "or6_%=:\n"
    "s_sub_u32 s79, s79, 1\n"
    "s_max_i32 s79, s79, 0\n"
    "s_cbranch_scc0 og6_%=\n"
    "s_waitcnt vmcnt(0)\n"
    "s_sleep 1\n"
    "s_call_b64 s[80:81], ol6_%=\n"
    "v_max3_u32 v25, v232, v233, v234\n"
    "v_max_u32 v25, v25, v235\n"
    "v_cmp_eq_u32 vcc, -1, v25\n"
    "s_cbranch_vccnz or6_%=\n"
    "s_branch og6_%=\n"
    "or7_%=:\n"
    "s_sub_u32 s79, s79, 1\n"
    "s_max_i32 s79, s79, 0\n"
    "s_cbranch_scc0 og7_%=\n"
    "s_waitcnt vmcnt(0)\n"
    "s_sleep 1\n"
    "s_call_b64 s[80:81], ol7_%=\n"
    "v_max3_u32 v25, v236, v237, v238\n"
    "v_max_u32 v25, v25, v239\n"
    "v_cmp_eq_u32 vcc, -1, v25\n"
    "s_cbranch_vccnz or7_%=\n"
    "s_branch og7_%=\n"
    "or8_%=:\n"
    "s_sub_u32 s79, s79, 1\n"
    "s_max_i32 s79, s79, 0\n"
    "s_cbranch_scc0 og8_%=\n"
    "s_waitcnt vmcnt(0)\n"
    "s_sleep 1\n"
    "s_call_b64 s[80:81], ol8_%=\n"
    "v_max3_u32 v25, v240, v241, v242\n"
    "v_max_u32 v25, v25, v243\n"
    "v_cmp_eq_u32 vcc, -1, v25\n"
    "s_cbranch_vccnz or8_%=\n"
    "s_branch og8_%=\n"
    "or9_%=:\n"
    "s_sub_u32 s79, s79, 1\n"
    "s_max_i32 s79, s79, 0\n"
    "s_cbranch_scc0 og9_%=\n"
    "s_waitcnt vmcnt(0)\n"
    "s_sleep 1\n"
    "s_call_b64 s[80:81], ol9_%=\n"
    "v_max3_u32 v25, v244, v245, v246\n"
    "v_max_u32 v25, v25, v247\n"
    "v_cmp_eq_u32 vcc, -1, v25\n"
    "s_cbranch_vccnz or9_%=\n"
    "s_branch og9_%=\n"
    "or10_%=:\n"
    "s_sub_u32 s79, s79, 1\n"
    "s_max_i32 s79, s79, 0\n"
    "s_cbranch_scc0 og10_%=\n"
    "s_waitcnt vmcnt(0)\n"
    "s_sleep 1\n"
    "s_call_b64 s[80:81], ol10_%=\n"
    "v_max3_u32 v25, v248, v249, v250\n"
    "v_max_u32 v25, v25, v251\n"
    "v_cmp_eq_u32 vcc, -1, v25\n"
    "s_cbranch_vccnz or10_%=\n"
    "s_branch og10_%=\n"
    "or11_%=:\n"
    "s_sub_u32 s79, s79, 1\n"
    "s_max_i32 s79, s79, 0\n"
    "s_cbranch_scc0 og11_%=\n"
    "s_waitcnt vmcnt(0)\n"
    "s_sleep 1\n"
    "s_call_b64 s[80:81], ol11_%=\n"
    "v_max3_u32 v25, v252, v253, v254\n"
    "v_max_u32 v25, v25, v255\n"
    "v_cmp_eq_u32 vcc, -1, v25\n"
    "s_cbranch_vccnz or11_%=\n"
    "s_branch og11_%=\n"
    "or12_%=:\n"
    "s_sub_u32 s79, s79, 1\n"
    "s_max_i32 s79, s79, 0\n"
    "s_cbranch_scc0 og12_%=\n"
    "s_waitcnt vmcnt(0)\n"
    "s_sleep 1\n"
    "s_call_b64 s[80:81], ol12_%=\n"
    "v_max3_u32 v25, v0, v1, v2\n"
    "v_max_u32 v25, v25, v3\n"
    "v_cmp_eq_u32 vcc, -1, v25\n"
    "s_cbranch_vccnz or12_%=\n"
    "s_branch og12_%=\n"
    "or13_%=:\n"
    "s_sub_u32 s79, s79, 1\n"
    "s_max_i32 s79, s79, 0\n"
    "s_cbranch_scc0 og13_%=\n"
    "s_waitcnt vmcnt(0)\n"
    "s_sleep 1\n"
    "s_call_b64 s[80:81], ol13_%=\n"
    "v_max3_u32 v25, v4, v5, v6\n"
    "v_max_u32 v25, v25, v7\n"
    "v_cmp_eq_u32 vcc, -1, v25\n"
    "s_cbranch_vccnz or13_%=\n"
    "s_branch og13_%=\n"
    "ol0_%=:\n"
    "buffer_load_dwordx4 v[208:211], v15, s[24:27], s63 offen sc1\n"
    "on1_%=:\n"
    "v_max3_u32 v25, v212, v213, v214\n"
    "v_max_u32 v25, v25, v215\n"
    "v_cmp_eq_u32 vcc, -1, v25\n"
    "s_cbranch_vccz on2_%=\n"
    "ol1_%=:\n"
    "buffer_load_dwordx4 v[212:215], v15, s[24:27], s63 offen offset:1024 sc1\n"
    "on2_%=:\n"
    "v_max3_u32 v25, v216, v217, v218\n"
    "v_max_u32 v25, v25, v219\n"
    "v_cmp_eq_u32 vcc, -1, v25\n"
    "s_cbranch_vccz on3_%=\n"
    "ol2_%=:\n"
    "buffer_load_dwordx4 v[216:219], v15, s[24:27], s63 offen offset:2048 sc1\n"
    "on3_%=:\n"
    "v_max3_u32 v25, v220, v221, v222\n"
    "v_max_u32 v25, v25, v223\n"
    "v_cmp_eq_u32 vcc, -1, v25\n"
    "s_cbranch_vccz on4_%=\n"
    "ol3_%=:\n"
    "buffer_load_dwordx4 v[220:223], v15, s[24:27], s63 offen offset:3072 sc1\n"
    "on4_%=:\n"
    "v_max3_u32 v25, v224, v225, v226\n"
    "v_max_u32 v25, v25, v227\n"
    "v_cmp_eq_u32 vcc, -1, v25\n"
    "s_cbranch_vccz on5_%=\n"
    "ol4_%=:\n"
    "buffer_load_dwordx4 v[224:227], v15, s[24:27], s72 offen sc1\n"
    "on5_%=:\n"
    "v_max3_u32 v25, v228, v229, v230\n"
    "v_max_u32 v25, v25, v231\n"
    "v_cmp_eq_u32 vcc, -1, v25\n"
    "s_cbranch_vccz on6_%=\n"
    "ol5_%=:\n"
    "buffer_load_dwordx4 v[228:231], v15, s[24:27], s72 offen offset:1024 sc1\n"
    "on6_%=:\n"
    "v_max3_u32 v25, v232, v233, v234\n"
    "v_max_u32 v25, v25, v235\n"
    "v_cmp_eq_u32 vcc, -1, v25\n"
    "s_cbranch_vccz on7_%=\n"
    "ol6_%=:\n"
    "buffer_load_dwordx4 v[232:235], v15, s[24:27], s72 offen offset:2048 sc1\n"
    "on7_%=:\n"
    "v_max3_u32 v25, v236, v237, v238\n"
    "v_max_u32 v25, v25, v239\n"
    "v_cmp_eq_u32 vcc, -1, v25\n"
    "s_cbranch_vccz on8_%=\n"
    "ol7_%=:\n"
    "buffer_load_dwordx4 v[236:239], v15, s[24:27], s72 offen offset:3072 sc1\n"
    "on8_%=:\n"
    "v_max3_u32 v25, v240, v241, v242\n"
    "v_max_u32 v25, v25, v243\n"
    "v_cmp_eq_u32 vcc, -1, v25\n"
    "s_cbranch_vccz on9_%=\n"
    "ol8_%=:\n"
    "buffer_load_dwordx4 v[240:243], v15, s[24:27], s73 offen sc1\n"
    "on9_%=:\n"
    "v_max3_u32 v25, v244, v245, v246\n"
    "v_max_u32 v25, v25, v247\n"
    "v_cmp_eq_u32 vcc, -1, v25\n"
    "s_cbranch_vccz on10_%=\n"
    "ol9_%=:\n"
    "buffer_load_dwordx4 v[244:247], v15, s[24:27], s73 offen offset:1024 sc1\n"
    "on10_%=:\n"
    "v_max3_u32 v25, v248, v249, v250\n"
    "v_max_u32 v25, v25, v251\n"
    "v_cmp_eq_u32 vcc, -1, v25\n"
    "s_cbranch_vccz on11_%=\n"
    "ol10_%=:\n"
    "buffer_load_dwordx4 v[248:251], v15, s[24:27], s73 offen offset:2048 sc1\n"
    "on11_%=:\n"
    "v_max3_u32 v25, v252, v253, v254\n"
    "v_max_u32 v25, v25, v255\n"
    "v_cmp_eq_u32 vcc, -1, v25\n"
    "s_cbranch_vccz on12_%=\n"
    "ol11_%=:\n"
    "buffer_load_dwordx4 v[252:255], v15, s[24:27], s73 offen offset:3072 sc1\n"
    "on12_%=:\n"
    "v_max3_u32 v25, v0, v1, v2\n"
    "v_max_u32 v25, v25, v3\n"
    "v_cmp_eq_u32 vcc, -1, v25\n"
    "s_cbranch_vccz on13_%=\n"
    "ol12_%=:\n"
    "buffer_load_dwordx4 v[0:3], v15, s[24:27], s74 offen sc1\n"
    "on13_%=:\n"
    "v_max3_u32 v25, v4, v5, v6\n"
    "v_max_u32 v25, v25, v7\n"
    "v_cmp_eq_u32 vcc, -1, v25\n"
    "s_cbranch_vccz on14_%=\n"
    "ol13_%=:\n"
    "buffer_load_dwordx4 v[4:7], v15, s[24:27], s74 offen offset:1024 sc1\n"
    "on14_%=:\n"
    "s_waitcnt vmcnt(0)\n"
    "s_setpc_b64 s[80:81]\n"
    "idle_%=:\n"
    "s_waitcnt vmcnt(0)\n"
    "v_mov_b32 v24, 122392\n"
    "v_mov_b32 v25, 1\n"
    "s_mov_b64 exec, 1\n"
    "ds_add_rtn_u32 v253, v24, v25\n"
    "s_mov_b64 exec, -1\n"
    "s_waitcnt lgkmcnt(0)\n"
    "v_readfirstlane_b32 s75, v253\n"
    "v_mov_b32 v25, s69\n"
    "v_mov_b32 v24, 0\n"
    "s_cmp_eq_u32 s75, 3\n"
    "s_cselect_b32 s75, s71, 0x40000000\n"
    "s_mov_b64 exec, 1\n"
    "buffer_store_dword v25, v24, s[24:27], s75 offen sc1\n"
    "s_mov_b64 exec, -1\n"
    "s_setprio 0\n"
    "s_lshl_b32 s72, s2, 9\n"
    "s_add_u32 s72, s72, s3\n"
    "s_cmp_lg_u32 s72, 130816\n"
    "s_cbranch_scc1 end_%=\n"
    "v_lshrrev_b32 v0, 8, v15\n"
    "v_lshlrev_b32 v0, 7, v0\n"
    "v_bfe_u32 v1, v15, 4, 4\n"
    "v_lshl_add_u32 v0, v1, 2, v0\n"
    "v_lshlrev_b32 v0, 5, v0\n"
    "v_add_u32 v0, 16793600, v0\n"
    "buffer_load_dword v1, v0, s[24:27], 0 offen sc1\n"
    "s_waitcnt vmcnt(0)\n"
    "v_cmp_ne_u32 vcc, s69, v1\n"
    "s_mov_b32 s79, 0x40000\n"
    "s_cmp_eq_u64 vcc, 0\n"
    "s_cbranch_scc1 pok41_%=\n"
    "psp42_%=:\n"
    "buffer_load_dword v1, v0, s[24:27], 0 offen sc1\n"
    "s_sub_u32 s79, s79, 1\n"
    "s_cbranch_scc1 pok41_%=\n"
    "s_waitcnt vmcnt(0)\n"
    "v_cmp_ne_u32 vcc, s69, v1\n"
    "s_sleep 1\n"
    "s_cmp_eq_u64 vcc, 0\n"
    "s_cbranch_scc0 psp42_%=\n"
    "pok41_%=:\n"
    "v_add_u32 v0, 16384, v0\n"
    "buffer_load_dword v1, v0, s[24:27], 0 offen sc1\n"
    "s_waitcnt vmcnt(0)\n"
    "v_cmp_ne_u32 vcc, s69, v1\n"
    "s_mov_b32 s79, 0x40000\n"
    "s_cmp_eq_u64 vcc, 0\n"
    "s_cbranch_scc1 pok43_%=\n"
    "psp44_%=:\n"
    "buffer_load_dword v1, v0, s[24:27], 0 offen sc1\n"
    "s_sub_u32 s79, s79, 1\n"
    "s_cbranch_scc1 pok43_%=\n"
    "s_waitcnt vmcnt(0)\n"
    "v_cmp_ne_u32 vcc, s69, v1\n"
    "s_sleep 1\n"
    "s_cmp_eq_u64 vcc, 0\n"
    "s_cbranch_scc0 psp44_%=\n"
    "pok43_%=:\n"
    "v_add_u32 v0, 16384, v0\n"
    "buffer_load_dword v1, v0, s[24:27], 0 offen sc1\n"
    "s_waitcnt vmcnt(0)\n"
    "v_cmp_ne_u32 vcc, s69, v1\n"
    "s_mov_b32 s79, 0x40000\n"
    "s_cmp_eq_u64 vcc, 0\n"
    "s_cbranch_scc1 pok45_%=\n"
    "psp46_%=:\n"
    "buffer_load_dword v1, v0, s[24:27], 0 offen sc1\n"
    "s_sub_u32 s79, s79, 1\n"
    "s_cbranch_scc1 pok45_%=\n"
    "s_waitcnt vmcnt(0)\n"
    "v_cmp_ne_u32 vcc, s69, v1\n"
    "s_sleep 1\n"
    "s_cmp_eq_u64 vcc, 0\n"
    "s_cbranch_scc0 psp46_%=\n"
    "pok45_%=:\n"
    "v_add_u32 v0, 16384, v0\n"
    "buffer_load_dword v1, v0, s[24:27], 0 offen sc1\n"
    "s_waitcnt vmcnt(0)\n"
    "v_cmp_ne_u32 vcc, s69, v1\n"
    "s_mov_b32 s79, 0x40000\n"
    "s_cmp_eq_u64 vcc, 0\n"
    "s_cbranch_scc1 pok47_%=\n"
    "psp48_%=:\n"
    "buffer_load_dword v1, v0, s[24:27], 0 offen sc1\n"
    "s_sub_u32 s79, s79, 1\n"
    "s_cbranch_scc1 pok47_%=\n"
    "s_waitcnt vmcnt(0)\n"
    "v_cmp_ne_u32 vcc, s69, v1\n"
    "s_sleep 1\n"
    "s_cmp_eq_u64 vcc, 0\n"
    "s_cbranch_scc0 psp48_%=\n"
    "pok47_%=:\n"
    "v_add_u32 v0, 16384, v0\n"
    "v_mov_b32 v1, s69\n"
    "s_mov_b32 s72, 17055744\n"
    "s_mov_b64 exec, 1\n"
    "buffer_store_dword v1, off, s[24:27], s72 sc1\n"
    "s_mov_b64 exec, -1\n"
    "end_%=:\n"
    "s_waitcnt vmcnt(0)\n"
    "s_endpgm\n"
    "last_%=:\n"
    "slow_%=:\n"
    "s_nop 7\n"
    "v_mov_b32 v16, 0xff800000\n"
    "v_mov_b32 v17, 0xff800000\n"
    "v_mov_b32 v18, 0xff800000\n"
    "v_mov_b32 v19, 0xff800000\n"
    "s_mov_b32 s73, s16\n"
    "s_add_u32 s72, s16, 2097152\n"
    "sl49_%=:\n"
    "buffer_load_dwordx4 v[96:99], v8, s[24:27], s73 offen sc1\n"
    "buffer_load_dwordx4 v[100:103], v8, s[24:27], s73 offen offset:1024 sc1\n"
    "s_waitcnt vmcnt(1)\n"
    "v_mfma_f32_32x32x16_bf16 v[208:223], v[96:99], v[64:67], 0\n"
    "s_waitcnt vmcnt(0)\n"
    "v_mfma_f32_32x32x16_bf16 v[208:223], v[100:103], v[68:71], v[208:223]\n"
    "s_nop 11\n"
    "v_max3_f32 v16, v208, v209, v16\n"
    "v_max3_f32 v16, v210, v211, v16\n"
    "v_max3_f32 v16, v212, v213, v16\n"
    "v_max3_f32 v16, v214, v215, v16\n"
    "v_max3_f32 v16, v216, v217, v16\n"
    "v_max3_f32 v16, v218, v219, v16\n"
    "v_max3_f32 v16, v220, v221, v16\n"
    "v_max3_f32 v16, v222, v223, v16\n"
    "v_mfma_f32_32x32x16_bf16 v[208:223], v[96:99], v[72:75], 0\n"
    "v_mfma_f32_32x32x16_bf16 v[208:223], v[100:103], v[76:79], v[208:223]\n"
    "s_nop 11\n"
    "v_max3_f32 v17, v208, v209, v17\n"
    "v_max3_f32 v17, v210, v211, v17\n"
    "v_max3_f32 v17, v212, v213, v17\n"
    "v_max3_f32 v17, v214, v215, v17\n"
    "v_max3_f32 v17, v216, v217, v17\n"
    "v_max3_f32 v17, v218, v219, v17\n"
    "v_max3_f32 v17, v220, v221, v17\n"
    "v_max3_f32 v17, v222, v223, v17\n"
    "v_mfma_f32_32x32x16_bf16 v[208:223], v[96:99], v[80:83], 0\n"
    "v_mfma_f32_32x32x16_bf16 v[208:223], v[100:103], v[84:87], v[208:223]\n"
    "s_nop 11\n"
    "v_max3_f32 v18, v208, v209, v18\n"
    "v_max3_f32 v18, v210, v211, v18\n"
    "v_max3_f32 v18, v212, v213, v18\n"
    "v_max3_f32 v18, v214, v215, v18\n"
    "v_max3_f32 v18, v216, v217, v18\n"
    "v_max3_f32 v18, v218, v219, v18\n"
    "v_max3_f32 v18, v220, v221, v18\n"
    "v_max3_f32 v18, v222, v223, v18\n"
    "v_mfma_f32_32x32x16_bf16 v[208:223], v[96:99], v[88:91], 0\n"
    "v_mfma_f32_32x32x16_bf16 v[208:223], v[100:103], v[92:95], v[208:223]\n"
    "s_nop 11\n"
    "v_max3_f32 v19, v208, v209, v19\n"
    "v_max3_f32 v19, v210, v211, v19\n"
    "v_max3_f32 v19, v212, v213, v19\n"
    "v_max3_f32 v19, v214, v215, v19\n"
    "v_max3_f32 v19, v216, v217, v19\n"
    "v_max3_f32 v19, v218, v219, v19\n"
    "v_max3_f32 v19, v220, v221, v19\n"
    "v_max3_f32 v19, v222, v223, v19\n"
    "s_add_u32 s73, s73, 131072\n"
    "s_cmp_lg_u32 s73, s72\n"
    "s_cbranch_scc1 sl49_%=\n"
    "v_and_b32 v20, 0x1f0, v15\n"
    "v_lshrrev_b32 v20, 2, v20\n"
    "s_lshr_b32 s78, s66, 5\n"
    "v_add_u32 v20, s78, v20\n"
    "v_add_u32 v20, 114688, v20\n"
    "s_lshr_b32 s78, s66, 3\n"
    "v_mov_b32 v6, v16\n"
    "v_xor_b32 v21, 0, v20\n"
    "s_nop 0\n"
    "v_permlane32_swap_b32 v6, v16\n"
    "v_max_f32 v6, v6, v16\n"
    "v_add_u32 v7, s78, v21\n"
    "ds_write_b32 v7, v6\n"
    "v_mov_b32 v6, v17\n"
    "v_xor_b32 v22, 128, v20\n"
    "s_nop 0\n"
    "v_permlane32_swap_b32 v6, v17\n"
    "v_max_f32 v6, v6, v17\n"
    "v_add_u32 v7, s78, v22\n"
    "ds_write_b32 v7, v6\n"
    "v_mov_b32 v6, v18\n"
    "v_xor_b32 v23, 256, v20\n"
    "s_nop 0\n"
    "v_permlane32_swap_b32 v6, v18\n"
    "v_max_f32 v6, v6, v18\n"
    "v_add_u32 v7, s78, v23\n"
    "ds_write_b32 v7, v6\n"
    "v_mov_b32 v6, v19\n"
    "v_xor_b32 v24, 384, v20\n"
    "s_nop 0\n"
    "v_permlane32_swap_b32 v6, v19\n"
    "v_max_f32 v6, v6, v19\n"
    "v_add_u32 v7, s78, v24\n"
    "ds_write_b32 v7, v6\n"
    "s_waitcnt lgkmcnt(0)\n"
    "s_mov_b64 exec, 1\n"
    "ds_add_u32 v4, v5\n"
    "s_mov_b64 exec, -1\n"
    "s_mov_b32 s79, 0x100000\n"
    "sp50_%=:\n"
    "ds_read_b32 v6, v4\n"
    "s_sub_u32 s79, s79, 1\n"
    "s_cbranch_scc1 so51_%=\n"
    "s_waitcnt lgkmcnt(0)\n"
    "v_readfirstlane_b32 s75, v6\n"
    "s_nop 0\n"
    "s_cmp_ge_u32 s75, 8\n"
    "s_cbranch_scc0 sp50_%=\n"
    "so51_%=:\n"
    "ds_read_b32 v208, v21 offset:0\n"
    "ds_read_b32 v209, v21 offset:512\n"
    "ds_read_b32 v210, v21 offset:1024\n"
    "ds_read_b32 v211, v21 offset:1536\n"
    "s_waitcnt lgkmcnt(2)\n"
    "v_max_f32 v16, v208, v209\n"
    "s_waitcnt lgkmcnt(0)\n"
    "v_max3_f32 v16, v210, v211, v16\n"
    "v_mov_b32 v176, 0\n"
    "v_mov_b32 v177, 0\n"
    "v_mov_b32 v178, 0\n"
    "v_mov_b32 v179, 0\n"
    "v_mov_b32 v180, 0\n"
    "v_mov_b32 v181, 0\n"
    "v_mov_b32 v182, 0\n"
    "v_mov_b32 v183, 0\n"
    "v_mov_b32 v184, 0\n"
    "v_mov_b32 v185, 0\n"
    "v_mov_b32 v186, 0\n"
    "v_mov_b32 v187, 0\n"
    "v_mov_b32 v188, 0\n"
    "v_mov_b32 v189, 0\n"
    "v_mov_b32 v190, 0\n"
    "v_mov_b32 v191, 0\n"
    "ds_read_b32 v208, v22 offset:0\n"
    "ds_read_b32 v209, v22 offset:512\n"
    "ds_read_b32 v210, v22 offset:1024\n"
    "ds_read_b32 v211, v22 offset:1536\n"
    "s_waitcnt lgkmcnt(2)\n"
    "v_max_f32 v17, v208, v209\n"
    "s_waitcnt lgkmcnt(0)\n"
    "v_max3_f32 v17, v210, v211, v17\n"
    "v_mov_b32 v192, 0\n"
    "v_mov_b32 v193, 0\n"
    "v_mov_b32 v194, 0\n"
    "v_mov_b32 v195, 0\n"
    "v_mov_b32 v196, 0\n"
    "v_mov_b32 v197, 0\n"
    "v_mov_b32 v198, 0\n"
    "v_mov_b32 v199, 0\n"
    "v_mov_b32 v200, 0\n"
    "v_mov_b32 v201, 0\n"
    "v_mov_b32 v202, 0\n"
    "v_mov_b32 v203, 0\n"
    "v_mov_b32 v204, 0\n"
    "v_mov_b32 v205, 0\n"
    "v_mov_b32 v206, 0\n"
    "v_mov_b32 v207, 0\n"
    "ds_read_b32 v208, v23 offset:0\n"
    "ds_read_b32 v209, v23 offset:512\n"
    "ds_read_b32 v210, v23 offset:1024\n"
    "ds_read_b32 v211, v23 offset:1536\n"
    "s_waitcnt lgkmcnt(2)\n"
    "v_max_f32 v18, v208, v209\n"
    "s_waitcnt lgkmcnt(0)\n"
    "v_max3_f32 v18, v210, v211, v18\n"
    "v_mov_b32 v32, 0\n"
    "v_mov_b32 v33, 0\n"
    "v_mov_b32 v34, 0\n"
    "v_mov_b32 v35, 0\n"
    "v_mov_b32 v36, 0\n"
    "v_mov_b32 v37, 0\n"
    "v_mov_b32 v38, 0\n"
    "v_mov_b32 v39, 0\n"
    "v_mov_b32 v40, 0\n"
    "v_mov_b32 v41, 0\n"
    "v_mov_b32 v42, 0\n"
    "v_mov_b32 v43, 0\n"
    "v_mov_b32 v44, 0\n"
    "v_mov_b32 v45, 0\n"
    "v_mov_b32 v46, 0\n"
    "v_mov_b32 v47, 0\n"
    "ds_read_b32 v208, v24 offset:0\n"
    "ds_read_b32 v209, v24 offset:512\n"
    "ds_read_b32 v210, v24 offset:1024\n"
    "ds_read_b32 v211, v24 offset:1536\n"
    "s_waitcnt lgkmcnt(2)\n"
    "v_max_f32 v19, v208, v209\n"
    "s_waitcnt lgkmcnt(0)\n"
    "v_max3_f32 v19, v210, v211, v19\n"
    "v_mov_b32 v48, 0\n"
    "v_mov_b32 v49, 0\n"
    "v_mov_b32 v50, 0\n"
    "v_mov_b32 v51, 0\n"
    "v_mov_b32 v52, 0\n"
    "v_mov_b32 v53, 0\n"
    "v_mov_b32 v54, 0\n"
    "v_mov_b32 v55, 0\n"
    "v_mov_b32 v56, 0\n"
    "v_mov_b32 v57, 0\n"
    "v_mov_b32 v58, 0\n"
    "v_mov_b32 v59, 0\n"
    "v_mov_b32 v60, 0\n"
    "v_mov_b32 v61, 0\n"
    "v_mov_b32 v62, 0\n"
    "v_mov_b32 v63, 0\n"
    "s_mov_b32 s73, s16\n"
    "sl52_%=:\n"
    "s_nop 15\n"
    "buffer_load_dwordx4 v[96:99], v8, s[24:27], s73 offen sc1\n"
    "buffer_load_dwordx4 v[100:103], v8, s[24:27], s73 offen offset:1024 sc1\n"
    "buffer_load_dwordx4 v[128:131], v9, s[24:27], s73 offen sc1\n"
    "buffer_load_dwordx4 v[132:135], v9, s[24:27], s73 offen offset:1024 sc1\n"
    "s_waitcnt vmcnt(3)\n"
    "v_mfma_f32_32x32x16_bf16 v[208:223], v[96:99], v[64:67], 0\n"
    "s_waitcnt vmcnt(2)\n"
    "v_mfma_f32_32x32x16_bf16 v[208:223], v[100:103], v[68:71], v[208:223]\n"
    "s_nop 11\n"
    "v_sub_f32 v208, v208, v16\n"
    "v_sub_f32 v209, v209, v16\n"
    "v_sub_f32 v210, v210, v16\n"
    "v_sub_f32 v211, v211, v16\n"
    "v_sub_f32 v212, v212, v16\n"
    "v_sub_f32 v213, v213, v16\n"
    "v_sub_f32 v214, v214, v16\n"
    "v_sub_f32 v215, v215, v16\n"
    "v_sub_f32 v216, v216, v16\n"
    "v_sub_f32 v217, v217, v16\n"
    "v_sub_f32 v218, v218, v16\n"
    "v_sub_f32 v219, v219, v16\n"
    "v_sub_f32 v220, v220, v16\n"
    "v_sub_f32 v221, v221, v16\n"
    "v_sub_f32 v222, v222, v16\n"
    "v_sub_f32 v223, v223, v16\n"
    "v_exp_f32 v208, v208\n"
    "v_exp_f32 v209, v209\n"
    "v_exp_f32 v210, v210\n"
    "v_exp_f32 v211, v211\n"
    "v_exp_f32 v212, v212\n"
    "v_exp_f32 v213, v213\n"
    "v_exp_f32 v214, v214\n"
    "v_exp_f32 v215, v215\n"
    "v_exp_f32 v216, v216\n"
    "v_exp_f32 v217, v217\n"
    "v_exp_f32 v218, v218\n"
    "v_exp_f32 v219, v219\n"
    "v_exp_f32 v220, v220\n"
    "v_exp_f32 v221, v221\n"
    "v_exp_f32 v222, v222\n"
    "v_exp_f32 v223, v223\n"
    "s_nop 7\n"
    "v_cvt_pk_bf16_f32 v160, v208, v209\n"
    "v_cvt_pk_bf16_f32 v161, v210, v211\n"
    "v_cvt_pk_bf16_f32 v162, v212, v213\n"
    "v_cvt_pk_bf16_f32 v163, v214, v215\n"
    "v_cvt_pk_bf16_f32 v164, v216, v217\n"
    "v_cvt_pk_bf16_f32 v165, v218, v219\n"
    "v_cvt_pk_bf16_f32 v166, v220, v221\n"
    "v_cvt_pk_bf16_f32 v167, v222, v223\n"
    "s_waitcnt vmcnt(1)\n"
    "v_mfma_f32_32x32x16_bf16 v[176:191], v[128:131], v[160:163], v[176:191]\n"
    "s_waitcnt vmcnt(0)\n"
    "v_mfma_f32_32x32x16_bf16 v[176:191], v[132:135], v[164:167], v[176:191]\n"
    "v_mfma_f32_32x32x16_bf16 v[208:223], v[96:99], v[72:75], 0\n"
    "v_mfma_f32_32x32x16_bf16 v[208:223], v[100:103], v[76:79], v[208:223]\n"
    "s_nop 11\n"
    "v_sub_f32 v208, v208, v17\n"
    "v_sub_f32 v209, v209, v17\n"
    "v_sub_f32 v210, v210, v17\n"
    "v_sub_f32 v211, v211, v17\n"
    "v_sub_f32 v212, v212, v17\n"
    "v_sub_f32 v213, v213, v17\n"
    "v_sub_f32 v214, v214, v17\n"
    "v_sub_f32 v215, v215, v17\n"
    "v_sub_f32 v216, v216, v17\n"
    "v_sub_f32 v217, v217, v17\n"
    "v_sub_f32 v218, v218, v17\n"
    "v_sub_f32 v219, v219, v17\n"
    "v_sub_f32 v220, v220, v17\n"
    "v_sub_f32 v221, v221, v17\n"
    "v_sub_f32 v222, v222, v17\n"
    "v_sub_f32 v223, v223, v17\n"
    "v_exp_f32 v208, v208\n"
    "v_exp_f32 v209, v209\n"
    "v_exp_f32 v210, v210\n"
    "v_exp_f32 v211, v211\n"
    "v_exp_f32 v212, v212\n"
    "v_exp_f32 v213, v213\n"
    "v_exp_f32 v214, v214\n"
    "v_exp_f32 v215, v215\n"
    "v_exp_f32 v216, v216\n"
    "v_exp_f32 v217, v217\n"
    "v_exp_f32 v218, v218\n"
    "v_exp_f32 v219, v219\n"
    "v_exp_f32 v220, v220\n"
    "v_exp_f32 v221, v221\n"
    "v_exp_f32 v222, v222\n"
    "v_exp_f32 v223, v223\n"
    "s_nop 7\n"
    "v_cvt_pk_bf16_f32 v160, v208, v209\n"
    "v_cvt_pk_bf16_f32 v161, v210, v211\n"
    "v_cvt_pk_bf16_f32 v162, v212, v213\n"
    "v_cvt_pk_bf16_f32 v163, v214, v215\n"
    "v_cvt_pk_bf16_f32 v164, v216, v217\n"
    "v_cvt_pk_bf16_f32 v165, v218, v219\n"
    "v_cvt_pk_bf16_f32 v166, v220, v221\n"
    "v_cvt_pk_bf16_f32 v167, v222, v223\n"
    "v_mfma_f32_32x32x16_bf16 v[192:207], v[128:131], v[160:163], v[192:207]\n"
    "s_nop 0\n"
    "v_mfma_f32_32x32x16_bf16 v[192:207], v[132:135], v[164:167], v[192:207]\n"
    "v_mfma_f32_32x32x16_bf16 v[208:223], v[96:99], v[80:83], 0\n"
    "v_mfma_f32_32x32x16_bf16 v[208:223], v[100:103], v[84:87], v[208:223]\n"
    "s_nop 11\n"
    "v_sub_f32 v208, v208, v18\n"
    "v_sub_f32 v209, v209, v18\n"
    "v_sub_f32 v210, v210, v18\n"
    "v_sub_f32 v211, v211, v18\n"
    "v_sub_f32 v212, v212, v18\n"
    "v_sub_f32 v213, v213, v18\n"
    "v_sub_f32 v214, v214, v18\n"
    "v_sub_f32 v215, v215, v18\n"
    "v_sub_f32 v216, v216, v18\n"
    "v_sub_f32 v217, v217, v18\n"
    "v_sub_f32 v218, v218, v18\n"
    "v_sub_f32 v219, v219, v18\n"
    "v_sub_f32 v220, v220, v18\n"
    "v_sub_f32 v221, v221, v18\n"
    "v_sub_f32 v222, v222, v18\n"
    "v_sub_f32 v223, v223, v18\n"
    "v_exp_f32 v208, v208\n"
    "v_exp_f32 v209, v209\n"
    "v_exp_f32 v210, v210\n"
    "v_exp_f32 v211, v211\n"
    "v_exp_f32 v212, v212\n"
    "v_exp_f32 v213, v213\n"
    "v_exp_f32 v214, v214\n"
    "v_exp_f32 v215, v215\n"
    "v_exp_f32 v216, v216\n"
    "v_exp_f32 v217, v217\n"
    "v_exp_f32 v218, v218\n"
    "v_exp_f32 v219, v219\n"
    "v_exp_f32 v220, v220\n"
    "v_exp_f32 v221, v221\n"
    "v_exp_f32 v222, v222\n"
    "v_exp_f32 v223, v223\n"
    "s_nop 7\n"
    "v_cvt_pk_bf16_f32 v160, v208, v209\n"
    "v_cvt_pk_bf16_f32 v161, v210, v211\n"
    "v_cvt_pk_bf16_f32 v162, v212, v213\n"
    "v_cvt_pk_bf16_f32 v163, v214, v215\n"
    "v_cvt_pk_bf16_f32 v164, v216, v217\n"
    "v_cvt_pk_bf16_f32 v165, v218, v219\n"
    "v_cvt_pk_bf16_f32 v166, v220, v221\n"
    "v_cvt_pk_bf16_f32 v167, v222, v223\n"
    "v_mfma_f32_32x32x16_bf16 v[32:47], v[128:131], v[160:163], v[32:47]\n"
    "s_nop 0\n"
    "v_mfma_f32_32x32x16_bf16 v[32:47], v[132:135], v[164:167], v[32:47]\n"
    "v_mfma_f32_32x32x16_bf16 v[208:223], v[96:99], v[88:91], 0\n"
    "v_mfma_f32_32x32x16_bf16 v[208:223], v[100:103], v[92:95], v[208:223]\n"
    "s_nop 11\n"
    "v_sub_f32 v208, v208, v19\n"
    "v_sub_f32 v209, v209, v19\n"
    "v_sub_f32 v210, v210, v19\n"
    "v_sub_f32 v211, v211, v19\n"
    "v_sub_f32 v212, v212, v19\n"
    "v_sub_f32 v213, v213, v19\n"
    "v_sub_f32 v214, v214, v19\n"
    "v_sub_f32 v215, v215, v19\n"
    "v_sub_f32 v216, v216, v19\n"
    "v_sub_f32 v217, v217, v19\n"
    "v_sub_f32 v218, v218, v19\n"
    "v_sub_f32 v219, v219, v19\n"
    "v_sub_f32 v220, v220, v19\n"
    "v_sub_f32 v221, v221, v19\n"
    "v_sub_f32 v222, v222, v19\n"
    "v_sub_f32 v223, v223, v19\n"
    "v_exp_f32 v208, v208\n"
    "v_exp_f32 v209, v209\n"
    "v_exp_f32 v210, v210\n"
    "v_exp_f32 v211, v211\n"
    "v_exp_f32 v212, v212\n"
    "v_exp_f32 v213, v213\n"
    "v_exp_f32 v214, v214\n"
    "v_exp_f32 v215, v215\n"
    "v_exp_f32 v216, v216\n"
    "v_exp_f32 v217, v217\n"
    "v_exp_f32 v218, v218\n"
    "v_exp_f32 v219, v219\n"
    "v_exp_f32 v220, v220\n"
    "v_exp_f32 v221, v221\n"
    "v_exp_f32 v222, v222\n"
    "v_exp_f32 v223, v223\n"
    "s_nop 7\n"
    "v_cvt_pk_bf16_f32 v160, v208, v209\n"
    "v_cvt_pk_bf16_f32 v161, v210, v211\n"
    "v_cvt_pk_bf16_f32 v162, v212, v213\n"
    "v_cvt_pk_bf16_f32 v163, v214, v215\n"
    "v_cvt_pk_bf16_f32 v164, v216, v217\n"
    "v_cvt_pk_bf16_f32 v165, v218, v219\n"
    "v_cvt_pk_bf16_f32 v166, v220, v221\n"
    "v_cvt_pk_bf16_f32 v167, v222, v223\n"
    "v_mfma_f32_32x32x16_bf16 v[48:63], v[128:131], v[160:163], v[48:63]\n"
    "s_nop 0\n"
    "v_mfma_f32_32x32x16_bf16 v[48:63], v[132:135], v[164:167], v[48:63]\n"
    "s_add_u32 s73, s73, 131072\n"
    "s_cmp_lg_u32 s73, s72\n"
    "s_cbranch_scc1 sl52_%=\n"
    "s_mov_b32 s56, 12\n"
    "s_mov_b32 s59, 1\n"
    "s_nop 15\n"
    "s_branch red_%=\n"

        : "+{v0}"(t) : "{s[0:1]}"(ka), "{s2}"(b) : "memory", "vcc", "scc", "s3","s4","s5","s6","s7","s8","s9","s10","s11","s12","s13","s14","s15","s16","s17","s18","s19","s20","s21","s22","s23","s24","s25","s26","s27","s28","s29","s30","s31","s32","s33","s34","s35","s36","s37","s38","s39","s40","s41","s42","s43","s44","s45","s46","s47","s48","s49","s50","s51","s52","s53","s54","s55","s56","s57","s58","s59","s60","s61","s62","s63","s64","s65","s66","s67","s68","s69","s70","s71","s72","s73","s74","s75","s76","s77","s78","s79","s80","s81","s82","s83","s84","s85","s86","s87","s88","s89","s90","s91","s92","s93","s94","s95","s96","s97","s98","s99","s100","s101", "v1","v2","v3","v4","v5","v6","v7","v8","v9","v10","v11","v12","v13","v14","v15","v16","v17","v18","v19","v20","v21","v22","v23","v24","v25","v26","v27","v28","v29","v30","v31","v32","v33","v34","v35","v36","v37","v38","v39","v40","v41","v42","v43","v44","v45","v46","v47","v48","v49","v50","v51","v52","v53","v54","v55","v56","v57","v58","v59","v60","v61","v62","v63","v64","v65","v66","v67","v68","v69","v70","v71","v72","v73","v74","v75","v76","v77","v78","v79","v80","v81","v82","v83","v84","v85","v86","v87","v88","v89","v90","v91","v92","v93","v94","v95","v96","v97","v98","v99","v100","v101","v102","v103","v104","v105","v106","v107","v108","v109","v110","v111","v112","v113","v114","v115","v116","v117","v118","v119","v120","v121","v122","v123","v124","v125","v126","v127","v128","v129","v130","v131","v132","v133","v134","v135","v136","v137","v138","v139","v140","v141","v142","v143","v144","v145","v146","v147","v148","v149","v150","v151","v152","v153","v154","v155","v156","v157","v158","v159","v160","v161","v162","v163","v164","v165","v166","v167","v168","v169","v170","v171","v172","v173","v174","v175","v176","v177","v178","v179","v180","v181","v182","v183","v184","v185","v186","v187","v188","v189","v190","v191","v192","v193","v194","v195","v196","v197","v198","v199","v200","v201","v202","v203","v204","v205","v206","v207","v208","v209","v210","v211","v212","v213","v214","v215","v216","v217","v218","v219","v220","v221","v222","v223","v224","v225","v226","v227","v228","v229","v230","v231","v232","v233","v234","v235","v236","v237","v238","v239","v240","v241","v242","v243","v244","v245","v246","v247","v248","v249","v250","v251","v252","v253","v254","v255");
}

extern "C" void kernel_launch(void* const* d_in, const int* in_sizes, int n_in, void* d_out, int out_size, void* d_ws, size_t ws_size, hipStream_t stream) {
    Args a = {{0,0,0x1044080,0x20000,0,0,0x1c0000,0x20000,0,0,0x1c0000,0x20000,0,0,0x188000,0x20000,0,0,0x62000,0x20000,0,0,0x0,0x20000,0,0,0x0,0x20000,0x0,0x0,0x0,0x0,0,0,0x126000,0x20000,0,0,0x188000,0x20000,0,0,0x188000,0x20000,0,0,0x188000,0x20000,0,0,0x188000,0x20000,0xe00,0x700,0x0,0x0,0x0,0x1,0x3e8b97f6,0x3,0x1de00,0x7000,0x777700cc,0x8,0,0,0x126000,0x20000,0,0,0x188000,0x20000,0,0,0x188000,0x20000,0,0,0x188000,0x20000,0,0,0x188000,0x20000,0xe00,0x780,0x80,0x80,0x0,0x1,0x3e8b97f6,0x3,0x1de00,0x8c00,0x989821dd,0x8,0,0,0x126000,0x20000,0,0,0x188000,0x20000,0,0,0x188000,0x20000,0,0,0x188000,0x20000,0,0,0x188000,0x20000,0xe00,0x800,0x100,0x100,0x0,0x1,0x3e8b97f6,0x3,0x1de00,0xa800,0xaaaa33ee,0x8,0,0,0x126000,0x20000,0,0,0x188000,0x20000,0,0,0x188000,0x20000,0,0,0x188000,0x20000,0,0,0x188000,0x20000,0xe00,0x880,0x180,0x180,0x0,0x1,0x3e8b97f6,0x3,0x1de00,0xc400,0xcccc55ff,0x8,0,0,0x126000,0x20000,0,0,0x188000,0x20000,0,0,0x188000,0x20000,0,0,0x188000,0x20000,0,0,0x188000,0x20000,0xe00,0x900,0x200,0x200,0x0,0x1,0x3e8b97f6,0x3,0x1de00,0x7600,0x777700cc,0x8,0,0,0x126000,0x20000,0,0,0x188000,0x20000,0,0,0x188000,0x20000,0,0,0x188000,0x20000,0,0,0x188000,0x20000,0xe00,0x980,0x280,0x280,0x0,0x1,0x3e8b97f6,0x3,0x1de00,0x9200,0x999922dd,0x8,0,0,0x126000,0x20000,0,0,0x188000,0x20000,0,0,0x188000,0x20000,0,0,0x188000,0x20000,0,0,0x188000,0x20000,0xe00,0xa00,0x300,0x300,0x0,0x1,0x3e8b97f6,0x3,0x1de00,0xae00,0xbaba43ee,0x8,0,0,0x126000,0x20000,0,0,0x188000,0x20000,0,0,0x188000,0x20000,0,0,0x188000,0x20000,0,0,0x188000,0x20000,0xe00,0x0,0x380,0x380,0x0,0x0,0x3e8b97f6,0x3,0x1de00,0xca00,0xcccc65ff,0x14,0,0,0x126000,0x20000,0,0,0x188000,0x20000,0,0,0x188000,0x20000,0,0,0x188000,0x20000,0,0,0x188000,0x20000,0xe00,0x80,0x400,0x400,0x1000,0x0,0x3e8b97f6,0x3,0x1de00,0x7e00,0x878710dc,0x14,0,0,0x126000,0x20000,0,0,0x188000,0x20000,0,0,0x188000,0x20000,0,0,0x188000,0x20000,0,0,0x188000,0x20000,0xe00,0x100,0x480,0x480,0x2000,0x0,0x3e8b97f6,0x3,0x1de00,0x9a00,0xa9a932ed,0x14,0,0,0x126000,0x20000,0,0,0x188000,0x20000,0,0,0x188000,0x20000,0,0,0x188000,0x20000,0,0,0x188000,0x20000,0xe00,0x180,0x500,0x500,0x3000,0x0,0x3e8b97f6,0x3,0x1de00,0xb600,0xbbbb44ee,0x14,0,0,0x126000,0x20000,0,0,0x188000,0x20000,0,0,0x188000,0x20000,0,0,0x188000,0x20000,0,0,0x188000,0x20000,0xe00,0x200,0x580,0x580,0x4000,0x0,0x3e8b97f6,0x3,0x1de00,0xd200,0xcccc66ff,0x14,0,0,0x126000,0x20000,0,0,0x126000,0x20000,0,0,0x126000,0x20000,0,0,0x188000,0x20000,0,0,0x188000,0x20000,0xa80,0x280,0x300,0x380,0x5000,0x0,0x3e8b97f6,0x7,0x1de00,0x8400,0x888811dd,0x14,0,0,0x0,0x20000,0,0,0x126000,0x20000,0,0,0x126000,0x20000,0,0,0x188000,0x20000,0,0,0x188000,0x20000,0xa80,0x300,0x400,0x480,0x40000000,0x0,0x3e8b97f6,0x63,0x1de38,0xa000,0xaaaa33ee,0x14,0,0,0x0,0x20000,0,0,0x126000,0x20000,0,0,0x126000,0x20000,0,0,0x188000,0x20000,0,0,0x188000,0x20000,0xa80,0x380,0x500,0x580,0x40000000,0x0,0x3e8b97f6,0x63,0x1de38,0xbc00,0xcbcb54fe,0x14,0,0,0x0,0x20000,0,0,0x126000,0x20000,0,0,0x126000,0x20000,0,0,0x188000,0x20000,0,0,0x188000,0x20000,0xa80,0x400,0x600,0x680,0x40000000,0x0,0x3e8b97f6,0x63,0x1de38,0xd800,0xcccc66ff,0x14,0xe1c0,0x0,0x0,0x0,0x0,0x0,0x0,0x0,0x800000,0x3,0x3f800000,0x3,0x1de04,0x1014000,0x1000,0x1c000,0xf1c0,0x0,0x0,0x0,0x0,0x0,0x0,0x0,0x801000,0x3,0x3f800000,0x3,0x1de04,0x1014080,0x1000,0x1c000,0x101c0,0x0,0x0,0x0,0x0,0x0,0x0,0x0,0x802000,0x3,0x3f800000,0x3,0x1de04,0x1014100,0x1000,0x1c000,0x111c0,0x0,0x0,0x0,0x0,0x0,0x0,0x0,0x803000,0x3,0x3f800000,0x3,0x1de04,0x1014180,0x1000,0x1c000,0x129c0,0x0,0x0,0x0,0x0,0x0,0x0,0x0,0x804000,0x3,0x3f800000,0x3,0x1de04,0x1014200,0x1000,0x1c000,0x139c0,0x0,0x0,0x0,0x0,0x0,0x0,0x0,0x805000,0x3,0x3f800000,0x3,0x1de04,0x1014280,0x1000,0x1c000,0x149c0,0x0,0x0,0x0,0x0,0x0,0x0,0x0,0x806000,0x3,0x3f800000,0x3,0x1de04,0x1014300,0x1000,0x1c000,0x40000000,0x0,0x0,0x0,0x0,0x0,0x0,0x0,0x807000,0x3,0x3f800000,0x3,0x1de04,0x1014380,0x1000,0x1c000,0x40000000,0x0,0x0,0x0,0x0,0x0,0x0,0x0,0x808000,0x3,0x3f800000,0x3,0x1de04,0x1014400,0x1000,0x1c000,0x40000000,0x0,0x0,0x0,0x0,0x0,0x0,0x0,0x809000,0x3,0x3f800000,0x3,0x1de04,0x1014480,0x1000,0x1c000,0x40000000,0x0,0x0,0x0,0x0,0x0,0x0,0x0,0x80a000,0x3,0x3f800000,0x3,0x1de04,0x1014500,0x1000,0x1c000,0x40000000,0x0,0x0,0x0,0x0,0x0,0x0,0x0,0x80b000,0x3,0x3f800000,0x3,0x1de04,0x1014580,0x1000,0x1c000,0x40000000,0x0,0x0,0x0,0x0,0x0,0x0,0x0,0x6000,0x4,0x3e8b97f6,0x7,0x1de00,0x1010000,0x80,0x20000,0x40000000,0x0,0x0,0x0,0x0,0x0,0x0,0x0,0x8000,0x4,0x3f800000,0x3,0x1de00,0x1011000,0x80,0x20000,0x40000000,0x0,0x0,0x0,0x0,0x0,0x0,0x0,0xa000,0x4,0x3f800000,0x3,0x1de00,0x1012000,0x80,0x20000,0x40000000,0x0,0x0,0x0,0x0,0x0,0x0,0x0,0xc000,0x4,0x3f800000,0x3,0x1de00,0x1013000,0x80,0x20000}};
    const unsigned where[] = {3,32,71,102,130,163,195,257,292,325,356,389,513,548,581,612,645,769,804,837,868,901,1025,1060,1093,1124,1157,1281,1316,1349,1380,1413,1537,1572,1605,1636,1669,1793,1828,1861,1892,1925,2049,2084,2117,2148,2181,2305,2340,2373,2404,2437,2561,2596,2629,2660,2693,2817,2852,2885,2916,2949,3073,3108,3141,3172,3205,3329,3361,3393,3428,3461,3585,3617,3649,3684,3717,3841,3873,3905,3940,3973,4097,4129,4161,4196,4229};
    for (unsigned e : where) {
        const void* p = (e & 7) == 7 ? d_out : (e & 7) == 3 ? d_ws : d_in[e & 7];
        memcpy(&a.w[e >> 3], &p, 8);
    }
    (void)hipFuncSetAttribute((const void*)fused, hipFuncAttributeMaxDynamicSharedMemorySize, 163840);
    fused<<<256, 512, 163840, stream>>>(a);
}
```
